```python
import torch

def _softmax(x, axis=-1):
    return torch.softmax(x, dim=axis)

def _gelu(x):
    return torch.nn.functional.gelu(x, approximate='tanh')

def _silu(x):
    return torch.nn.functional.silu(x)

def _sigmoid(x):
    return torch.sigmoid(x)

def _layernorm(x, axis=-1, eps=1e-05):
    m = x.mean(dim=axis, keepdim=True)
    v = x.var(dim=axis, keepdim=True, unbiased=False)
    return (x - m) / torch.sqrt(v + eps)

def _scatter_add(tgt, idx, src, axis=0):
    return tgt.clone().index_add_(axis, idx, src)

def _segment_sum(src, seg, n):
    out = torch.zeros((n,) + src.shape[1:], dtype=src.dtype)
    return out.index_add_(0, seg, src)

def _pad(x, pw):
    flat = tuple((v for pair in reversed(pw) for v in pair))
    return torch.nn.functional.pad(x, flat)
kernel_name = 'adaLN_cond__sliding_block__glu_gated_geglu__prompt'

def setup_inputs():

    _w = lambda a: (lambda t: t.to(torch.bfloat16) if t.is_floating_point() else t)(torch.as_tensor(a))

    def _A_setup():
        B, S, D = (1, 2048, 448)
        return {'x': _w((lambda *s: torch.randn(*s, dtype=torch.bfloat16) * 0.05)(B, S, D)), 'wqkv': _w((lambda *s: torch.randn(*s, dtype=torch.bfloat16) * 0.05)(D, 3 * D)), 'wo': _w((lambda *s: torch.randn(*s, dtype=torch.bfloat16) * 0.05)(D, D))}
    _A_in = _A_setup()

    def _B_setup():
        B, S, Dm, Df = (1, 2048, 448, 1792)
        return {'x': _w((lambda *s: torch.randn(*s, dtype=torch.bfloat16) * 0.05)(B, S, Dm)), 'w_gate': _w((lambda *s: torch.randn(*s, dtype=torch.bfloat16) * 0.8)(Dm, Df) * Dm ** (-0.5)), 'w_up': _w((lambda *s: torch.randn(*s, dtype=torch.bfloat16) * 0.8)(Dm, Df) * Dm ** (-0.5)), 'w_down': _w((lambda *s: torch.randn(*s, dtype=torch.bfloat16) * 0.8)(Df, Dm) * Df ** (-0.5))}
    _B_in = _B_setup()
    return {'A_x': _A_in['x'], 'A_wqkv': _A_in['wqkv'], 'A_wo': _A_in['wo'], 'B_x': _B_in['x'], 'B_w_gate': _B_in['w_gate'], 'B_w_up': _B_in['w_up'], 'B_w_down': _B_in['w_down'], 'c': _w((lambda *s: torch.randn(*s, dtype=torch.bfloat16) * 0.1)(1, 448) * 8.0), 'w_mod': _w((lambda *s: torch.randn(*s, dtype=torch.bfloat16) * 0.1)(448, 6 * 448) * 448 ** (-0.5) * 8.0)}

def reference(A_x, A_wqkv, A_wo, B_x, B_w_gate, B_w_up, B_w_down, c, w_mod):

    def _A_ref(x, wqkv, wo):
        B, S, D = x.shape
        H = 16
        Dh = D // H
        W = 256
        qkv = (lambda x, *d: x.permute(*d))((x @ wqkv).reshape(B, S, 3, H, Dh), 2, 0, 3, 1, 4)
        q, k, v = (qkv[0], qkv[1], qkv[2])
        scores = torch.einsum('bhqd,bhkd->bhqk', q, k) * Dh ** (-0.5)
        idx = torch.arange(S)
        mask = (idx[:, None] - idx[None, :] >= 0) & (idx[:, None] - idx[None, :] < W)
        scores = torch.where(mask[None, None], scores, -1000000000.0)
        p = _softmax(scores, axis=-1)
        o = torch.einsum('bhqk,bhkd->bhqd', p, v)
        return (lambda x, *d: x.permute(*d))(o, 0, 2, 1, 3).reshape(B, S, D) @ wo

    def _B_ref(x, w_gate, w_up, w_down):
        g = torch.einsum('bsd,df->bsf', x, w_gate)
        u = torch.einsum('bsd,df->bsf', x, w_up)
        h = _gelu(g) * u
        return torch.einsum('bsf,fd->bsd', h, w_down)
    x = A_x
    mod = (_silu(c) @ w_mod).reshape(c.shape[0], 6, 1, -1)
    sa, ca, ga, sb, cb, gb = (mod[:, 0].clone(), mod[:, 1].clone(), mod[:, 2].clone(), mod[:, 3].clone(), mod[:, 4].clone(), mod[:, 5].clone())
    h = _layernorm(x, axis=-1) * (1 + ca) + sa
    x = x + ga * _A_ref(h, A_wqkv, A_wo)
    h = _layernorm(x, axis=-1) * (1 + cb) + sb
    x = x + gb * _B_ref(h, B_w_gate, B_w_up, B_w_down)
    return x
```

```cpp
#include <hip/hip_runtime.h>
#include <stdint.h>


typedef __attribute__((ext_vector_type(8))) short bf16x8;
typedef __attribute__((ext_vector_type(16))) float f32x16;
typedef __attribute__((ext_vector_type(4))) float f32x4;
typedef __attribute__((ext_vector_type(2))) float f32x2;
typedef __attribute__((ext_vector_type(2))) __bf16 bf16x2_t;
typedef __attribute__((ext_vector_type(4))) unsigned u32x4;
typedef __attribute__((ext_vector_type(2))) unsigned u32x2;

#define DEVI __device__ __forceinline__

DEVI unsigned pack2(float a, float b) {
  f32x2 v = {a, b};
  bf16x2_t r = __builtin_convertvector(v, bf16x2_t);
  return __builtin_bit_cast(unsigned, r);
}
DEVI float bflo(unsigned w) { return __builtin_bit_cast(float, w << 16); }
DEVI float bfhi(unsigned w) { return __builtin_bit_cast(float, w & 0xffff0000u); }
DEVI float rbf(float a) { return bflo(pack2(a, a)); }
DEVI int swap23(int d) { return (d & ~12) | ((d & 4) << 1) | ((d & 8) >> 1); }

constexpr size_t OFF_XN = 0;
constexpr size_t OFF_WQKV = OFF_XN + 64 * 28 * 1024;
constexpr size_t OFF_WO = OFF_WQKV + 48 * 28 * 1024;
constexpr size_t OFF_WGU = OFF_WO + 14 * 28 * 1024;
constexpr size_t OFF_WD = OFF_WGU + 112 * 28 * 1024;
constexpr size_t OFF_Q = OFF_WD + 14 * 112 * 1024;
constexpr size_t OFF_K = OFF_Q + 16 * 128 * 1024;
constexpr size_t OFF_V = OFF_K + 16 * 128 * 1024;
constexpr size_t OFF_O = OFF_V + 16 * 128 * 1024;
constexpr size_t OFF_X1 = OFF_O + 64 * 28 * 1024;
constexpr size_t OFF_HFF = OFF_X1 + 64 * 28 * 1024;
constexpr size_t OFF_STATS = OFF_HFF + 64 * 112 * 1024;
constexpr size_t OFF_BQKV = OFF_STATS + 64 * 4096;
constexpr size_t OFF_GUP = OFF_BQKV + 16 * 8192;
constexpr size_t OFF_GUF = OFF_GUP + 14 * 2 * 3584 * 4;
constexpr size_t OFF_GAB = OFF_GUF + 112 * 256 + 4096;
constexpr size_t WS_END = OFF_GAB + 4096;

#define GLDS16(src, dst) __builtin_amdgcn_global_load_lds((const unsigned*)(src), (unsigned*)(dst), 16, 0, 0)
#define GLDS4(src, dst) __builtin_amdgcn_global_load_lds((const unsigned*)(src), (unsigned*)(dst), 4, 0, 0)

template <int N>
DEVI void wait_bar() {
  asm volatile("s_waitcnt vmcnt(%0) lgkmcnt(0)\n\ts_barrier" ::"n"(N) : "memory");
}

__global__ __launch_bounds__(256) void k0_prep(const uint16_t* __restrict__ x, const uint16_t* __restrict__ wqkv,
                                               const uint16_t* __restrict__ wo, const uint16_t* __restrict__ wg,
                                               const uint16_t* __restrict__ wu, const uint16_t* __restrict__ wd,
                                               const uint16_t* __restrict__ cvec, const uint16_t* __restrict__ wmod,
                                               char* __restrict__ ws) {
  __shared__ __attribute__((aligned(16))) float sm[2 * 4 * 448 + 4 * 64 + 64];
  float* red = sm + 2 * 4 * 448;
  float* fin = red + 256;
  const int b = blockIdx.x, t = threadIdx.x, lane = t & 63, w = t >> 6;

  if (b >= 224 && b < 288) {
    const int mt = b - 224, tok = lane & 31, half = lane >> 5;
    const uint16_t* src = x + (size_t)(mt * 32 + tok) * 448 + (7 * w) * 16 + half * 8;
    u32x4 v[7];
#pragma unroll
    for (int i = 0; i < 7; ++i) v[i] = *(const u32x4*)(src + i * 16);
    float s = 0.f;
#pragma unroll
    for (int i = 0; i < 7; ++i)
#pragma unroll
      for (int j = 0; j < 4; ++j) s += bflo(v[i][j]) + bfhi(v[i][j]);
    const float lm = s * (1.f / 56.f);
    float m2 = 0.f;
#pragma unroll
    for (int i = 0; i < 7; ++i)
#pragma unroll
      for (int j = 0; j < 4; ++j) {
        float a = bflo(v[i][j]) - lm, c = bfhi(v[i][j]) - lm;
        m2 += a * a + c * c;
      }
    f32x2* part = (f32x2*)sm;
    part[(w * 2 + half) * 32 + tok] = f32x2{lm, m2};
    __syncthreads();
    float mu = 0.f, M2 = 0.f;
    f32x2 p[8];
#pragma unroll
    for (int i = 0; i < 8; ++i) { p[i] = part[i * 32 + tok]; mu += p[i][0]; M2 += p[i][1]; }
    mu *= 0.125f;
#pragma unroll
    for (int i = 0; i < 8; ++i) { float d = p[i][0] - mu; M2 += 56.f * d * d; }
    const float rstd = rsqrtf(M2 * (1.f / 448.f) + 1e-5f);
    char* dst = ws + OFF_XN + ((size_t)(mt * 28 + 7 * w) * 64 + lane) * 16;
#pragma unroll
    for (int i = 0; i < 7; ++i) {
      u32x4 o;
#pragma unroll
      for (int j = 0; j < 4; ++j) o[j] = pack2((bflo(v[i][j]) - mu) * rstd, (bfhi(v[i][j]) - mu) * rstd);
      *(u32x4*)(dst + i * 1024) = o;
    }
    return;
  }

  int role, kr, sub = 0;
  if (b < 42) { role = 0; kr = b / 3; sub = b % 3; }
  else if (b < 154) { role = 1; kr = (b - 42) >> 3; sub = (b - 42) & 7; }
  else if (b < 168) { role = 2; kr = b - 154; }
  else if (b < 224) { role = 3; kr = b - 168; }
  else { role = 4; kr = b - 288; }
  const int k0 = kr * 32;
  const int koct = t / 56, coct = t % 56;
  const bool act = t < 224 && role != 4;

  const uint16_t* wsrc; int ldw, c0;
  if (role == 0) { wsrc = wqkv; ldw = 1344; c0 = sub * 448; }
  else if (role == 1) { wsrc = (sub & 4) ? wu : wg; ldw = 1792; c0 = (sub & 3) * 448; }
  else if (role == 2) { wsrc = wo; ldw = 448; c0 = 0; }
  else { wsrc = wd; ldw = 448; c0 = 0; }
  u32x4 wv[8];
  if (act) {
    const uint16_t* p = wsrc + (size_t)(k0 + koct * 8) * ldw + c0 + coct * 8;
#pragma unroll
    for (int i = 0; i < 8; ++i) wv[i] = *(const u32x4*)(p + (size_t)i * ldw);
  } else {
#pragma unroll
    for (int i = 0; i < 8; ++i) wv[i] = u32x4{0, 0, 0, 0};
  }

  float sc[8], sh[8];
#pragma unroll
  for (int i = 0; i < 8; ++i) { sc[i] = 1.f; sh[i] = 0.f; }

  if (role == 0 || role == 1 || role == 4) {
    int cA, cB;
    if (role == 0) { cA = k0; cB = 448 + k0; }
    else if (role == 1) { cA = 1344 + k0; cB = 1792 + k0; }
    else { cA = (kr < 7) ? 896 + kr * 64 : 2240 + (kr - 7) * 64; cB = cA + 32; }
    const int seg = (t >> 2) & 1, q4 = t & 3, rs = t >> 3;
    const uint16_t* mp = wmod + (size_t)rs * 2688 + (seg ? cB : cA) + q4 * 8;
    u32x4 mv[14];
    float cs[14];
#pragma unroll
    for (int i = 0; i < 14; ++i) mv[i] = *(const u32x4*)(mp + (size_t)i * 32 * 2688);
#pragma unroll
    for (int i = 0; i < 14; ++i) {
      float cv = bflo((unsigned)cvec[rs + 32 * i]);
      cs[i] = rbf(cv / (1.f + __expf(-cv)));
    }
    float acc[8];
#pragma unroll
    for (int j = 0; j < 8; ++j) acc[j] = 0.f;
#pragma unroll
    for (int i = 0; i < 14; ++i)
#pragma unroll
      for (int j = 0; j < 4; ++j) {
        acc[2 * j] += cs[i] * bflo(mv[i][j]);
        acc[2 * j + 1] += cs[i] * bfhi(mv[i][j]);
      }
#pragma unroll
    for (int j = 0; j < 8; ++j) {
      acc[j] += __shfl_xor(acc[j], 8);
      acc[j] += __shfl_xor(acc[j], 16);
      acc[j] += __shfl_xor(acc[j], 32);
    }
    if (lane < 8) {
#pragma unroll
      for (int j = 0; j < 8; ++j) red[w * 64 + lane * 8 + j] = acc[j];
    }
    __syncthreads();
    if (t < 64) {
      float m = rbf(red[t] + red[64 + t] + red[128 + t] + red[192 + t]);
      if (role == 4) {
        float* gab = (float*)(ws + OFF_GAB);
        gab[kr * 64 + t] = m;
      } else {
        fin[t] = (t >= 32) ? rbf(1.f + m) : m;
      }
    }
    if (role == 4) return;
    __syncthreads();
    const int kk = (koct & 3) * 8;
#pragma unroll
    for (int i = 0; i < 8; ++i) { sh[i] = fin[kk + i]; sc[i] = fin[32 + kk + i]; }
  }

  const int ksl = koct >> 1, half = koct & 1;
  float* bp = sm;
  float* up = sm + 4 * 448;
  if (act) {
#pragma unroll
    for (int c = 0; c < 8; ++c) {
      float v[8];
#pragma unroll
      for (int i = 0; i < 8; ++i) v[i] = (c & 1) ? bfhi(wv[i][c >> 1]) : bflo(wv[i][c >> 1]);
      u32x4 o;
#pragma unroll
      for (int i = 0; i < 4; ++i) o[i] = pack2(v[2 * i] * sc[2 * i], v[2 * i + 1] * sc[2 * i + 1]);
      const int n = coct * 8 + c;
      char* dst;
      if (role == 0) {
        const int head = n / 28, d = n % 28;
        const int idx = (sub < 2) ? swap23(d) : d;
        dst = ws + OFF_WQKV + ((size_t)((head * 3 + sub) * 28 + 2 * kr + ksl) * 64 + half * 32 + idx) * 16;
      } else if (role == 1) {
        const int f = (sub & 3) * 448 + n;
        const int idx = (sub >> 2) * 16 + swap23(f & 15);
        dst = ws + OFF_WGU + ((size_t)((f >> 4) * 28 + 2 * kr + ksl) * 64 + half * 32 + idx) * 16;
      } else if (role == 2) {
        dst = ws + OFF_WO + ((size_t)((n >> 5) * 28 + 2 * kr + ksl) * 64 + half * 32 + swap23(n & 31)) * 16;
      } else {
        dst = ws + OFF_WD + ((size_t)((n >> 5) * 112 + 2 * kr + ksl) * 64 + half * 32 + swap23(n & 31)) * 16;
      }
      *(u32x4*)dst = o;
      if (role <= 1) {
        float bsum = 0.f;
#pragma unroll
        for (int i = 0; i < 8; ++i) bsum += sh[i] * v[i];
        bp[koct * 448 + n] = bsum;
        if (role == 1) {
          float us = 0.f;
#pragma unroll
          for (int i = 0; i < 4; ++i) us += bflo(o[i]) + bfhi(o[i]);
          up[koct * 448 + n] = us;
        }
      }
    }
  } else if (role == 0) {
    const int z = t - 224;
#pragma unroll
    for (int i = 0; i < 8; ++i) {
      const int pc = z * 8 + i, head = pc >> 4, d = 28 + ((pc >> 2) & 3), ko = pc & 3;
      const int idx = (sub < 2) ? swap23(d) : d;
      char* dst = ws + OFF_WQKV + ((size_t)((head * 3 + sub) * 28 + 2 * kr + (ko >> 1)) * 64 + (ko & 1) * 32 + idx) * 16;
      *(u32x4*)dst = u32x4{0, 0, 0, 0};
    }
    float* bq = (float*)(ws + OFF_BQKV);
#pragma unroll
    for (int i = 0; i < 2; ++i) {
      const int pc = z * 2 + i, head = pc >> 2, d = 28 + (pc & 3);
      bq[(head * 16 + kr) * 128 + sub * 32 + d] = (sub == 2 && d == 28 && kr == 0) ? 1.f : 0.f;
    }
  }
  if (role <= 1) {
    __syncthreads();
#pragma unroll
    for (int i = 0; i < 2; ++i) {
      const int n = t + i * 256;
      if (n < 448) {
        const float bs = bp[n] + bp[448 + n] + bp[896 + n] + bp[1344 + n];
        if (role == 0) {
          float* bq = (float*)(ws + OFF_BQKV);
          bq[((n / 28) * 16 + kr) * 128 + sub * 32 + (n % 28)] = bs;
        } else {
          const float us = up[n] + up[448 + n] + up[896 + n] + up[1344 + n];
          float* gp = (float*)(ws + OFF_GUP) + (size_t)kr * 2 * 3584 + (sub >> 2) * 1792 + (sub & 3) * 448 + n;
          gp[0] = us;
          gp[3584] = bs;
        }
      }
    }
  }
}

template <int MT, int NT, int KS, int CH, int NSLOT>
struct Ring {
  static constexpr int NC = KS / CH;
  static constexpr int PIECES = (MT + NT) * CH;
  static constexpr int PPW = PIECES / 4;
  static constexpr int SLOTB = PIECES * 1024;
  static_assert(PIECES % 4 == 0, "");
  static_assert(KS % CH == 0, "");

  static DEVI void issue(const char* A, const char* B, char* lds, int w, int lane, int c) {
#pragma unroll
    for (int i = 0; i < PPW; ++i) {
      const int p = w * PPW + i;
      const int tile = p / CH, ksl = p % CH;
      const char* src = (tile < MT) ? A + (size_t)(tile * KS) * 1024 : B + (size_t)((tile - MT) * KS) * 1024;
      GLDS16(src + (size_t)(c * CH + ksl) * 1024 + lane * 16, lds + (c % NSLOT) * SLOTB + p * 1024);
    }
  }
  static DEVI const char* frag(const char* lds, int c, int tile, int ksl, int lane) {
    return lds + (c % NSLOT) * SLOTB + (tile * CH + ksl) * 1024 + lane * 16;
  }
  static constexpr int younger(int c) {
    int issued = NSLOT - 1 + (c > 1 ? c - 1 : 0);
    if (issued > NC - 1) issued = NC - 1;
    return (issued - c) * PPW;
  }
};

template <int I, int N, class F>
DEVI void static_for(F&& f) {
  if constexpr (I < N) {
    f(std::integral_constant<int, I>{});
    static_for<I + 1, N>(f);
  }
}

#define MFMA32(a, b, c) __builtin_amdgcn_mfma_f32_32x32x16_bf16(a, b, c, 0, 0, 0)
#define MFMA16(a, b, c) __builtin_amdgcn_mfma_f32_16x16x32_bf16(a, b, c, 0, 0, 0)

__global__ __launch_bounds__(256) void k1_qkv(char* __restrict__ ws) {
  extern __shared__ __attribute__((aligned(16))) char lds[];
  using R = Ring<4, 3, 28, 4, 5>;
  const int t = threadIdx.x, lane = t & 63, w = __builtin_amdgcn_readfirstlane(t >> 6);
  const int xc = blockIdx.x & 7, bi = blockIdx.x >> 3;
  const int mb = (xc & 3) * 4 + (bi & 3), head = (xc >> 2) * 8 + (bi >> 2);
  const char* A = ws + OFF_XN + (size_t)mb * 4 * 28 * 1024;
  const char* B = ws + OFF_WQKV + (size_t)head * 3 * 28 * 1024;
  char* aux = lds + 5 * R::SLOTB;
  float* bfin = (float*)(aux + 8192);
  {
    const char* bsrc = ws + OFF_BQKV + (size_t)head * 8192 + w * 2048 + lane * 16;
    GLDS16(bsrc, aux + w * 2048);
    GLDS16(bsrc + 1024, aux + w * 2048 + 1024);
  }
#pragma unroll
  for (int c = 0; c < 5; ++c) R::issue(A, B, lds, w, lane, c);
  f32x16 acc[3];
#pragma unroll
  for (int i = 0; i < 3; ++i)
#pragma unroll
    for (int r = 0; r < 16; ++r) acc[i][r] = 0.f;

  static_for<0, R::NC>([&](auto cc) {
    constexpr int c = decltype(cc)::value;
    wait_bar<R::younger(c)>();
    if constexpr (c >= 1 && c - 1 + 5 < R::NC) R::issue(A, B, lds, w, lane, c - 1 + 5);
    if constexpr (c == 0) {
      if (t < 96) {
        const float* pp = (const float*)aux + t;
        float s = 0.f;
#pragma unroll
        for (int i = 0; i < 14; ++i) s += pp[i * 128];
        bfin[t] = s;
      }
    }
#pragma unroll
    for (int k = 0; k < 4; ++k) {
      bf16x8 xa = *(const bf16x8*)R::frag(lds, c, w, k, lane);
      bf16x8 w0 = *(const bf16x8*)R::frag(lds, c, 4, k, lane);
      bf16x8 w1 = *(const bf16x8*)R::frag(lds, c, 5, k, lane);
      bf16x8 w2 = *(const bf16x8*)R::frag(lds, c, 6, k, lane);
      acc[0] = MFMA32(w0, xa, acc[0]);
      acc[1] = MFMA32(w1, xa, acc[1]);
      acc[2] = MFMA32(xa, w2, acc[2]);
    }
  });

  const int hi = lane >> 5, l31 = lane & 31;
  const int mt = mb * 4 + w;
  const float qs = 0.18898223650461362f * 1.4426950408889634f;
#pragma unroll
  for (int p = 0; p < 2; ++p) {
    char* base = ws + (p ? OFF_K : OFF_Q) + ((size_t)(head * 128 + mt * 2 + (l31 >> 4)) * 64 + (l31 & 15)) * 16;
#pragma unroll
    for (int s = 0; s < 2; ++s) {
      const f32x4 b0 = *(const f32x4*)(bfin + p * 32 + s * 16 + hi * 8);
      const f32x4 b1 = *(const f32x4*)(bfin + p * 32 + s * 16 + hi * 8 + 4);
      float v[8];
#pragma unroll
      for (int j = 0; j < 4; ++j) { v[j] = acc[p][8 * s + j] + b0[j]; v[4 + j] = acc[p][8 * s + 4 + j] + b1[j]; }
      if (p == 0) {
#pragma unroll
        for (int j = 0; j < 8; ++j) v[j] *= qs;
      }
      u32x4 o = {pack2(v[0], v[1]), pack2(v[2], v[3]), pack2(v[4], v[5]), pack2(v[6], v[7])};
      *(u32x4*)(base + (2 * s + hi) * 256) = o;
    }
  }
  {
    const float bv = bfin[64 + l31];
    char* base = ws + OFF_V + ((size_t)((head * 64 + mt) * 2 + (l31 >> 4)) * 64 + (l31 & 15)) * 16;
#pragma unroll
    for (int s = 0; s < 2; ++s) {
      u32x4 o = {pack2(acc[2][4 * s] + bv, acc[2][4 * s + 1] + bv), pack2(acc[2][4 * s + 2] + bv, acc[2][4 * s + 3] + bv),
                 pack2(acc[2][8 + 4 * s] + bv, acc[2][8 + 4 * s + 1] + bv),
                 pack2(acc[2][8 + 4 * s + 2] + bv, acc[2][8 + 4 * s + 3] + bv)};
      *(u32x4*)(base + (2 * s + hi) * 256) = o;
    }
  }
}

template <bool ODD, bool EDGE>
DEVI void attn_wave(const char* ldsK, const char* ldsV, const char* ldsQ, int w, int lane, int nneg, char* odst, int kbase) {
  const int g = lane >> 4, qq = lane & 15;
  const int s0 = (w >> 1) * 2;
  const bf16x8 q = *(const bf16x8*)(ldsQ + w * 1024 + lane * 16);
  const float NEG = -1e30f;
  f32x4 mlo, mhi;
#pragma unroll
  for (int r = 0; r < 4; ++r) {
    mlo[r] = (4 * g + r > qq) ? 0.f : NEG;
    mhi[r] = (4 * g + r <= qq) ? 0.f : NEG;
  }
  f32x4 s[17];
  const char* kp = ldsK + (s0 + (ODD ? 1 : 0)) * 1024 + lane * 16;
#pragma unroll
  for (int tt = 0; tt < 17; ++tt) {
    f32x4 ci = (tt == 0) ? mlo : (tt == 16) ? mhi : f32x4{0.f, 0.f, 0.f, 0.f};
    if (EDGE) {
      if (w + tt < nneg) ci = f32x4{NEG, NEG, NEG, NEG};
    }
    const bf16x8 kf = *(const bf16x8*)(kp + tt * 1024);
    s[tt] = MFMA16(kf, q, ci);
  }
  float m = NEG;
#pragma unroll
  for (int tt = 0; tt < 17; ++tt) m = fmaxf(fmaxf(m, fmaxf(s[tt][0], s[tt][1])), fmaxf(s[tt][2], s[tt][3]));
  m = fmaxf(m, __shfl_xor(m, 16));
  m = fmaxf(m, __shfl_xor(m, 32));
  u32x2 pb[18];
  pb[ODD ? 0 : 17] = u32x2{0, 0};
#pragma unroll
  for (int tt = 0; tt < 17; ++tt) {
    const float e0 = __builtin_amdgcn_exp2f(s[tt][0] - m), e1 = __builtin_amdgcn_exp2f(s[tt][1] - m);
    const float e2 = __builtin_amdgcn_exp2f(s[tt][2] - m), e3 = __builtin_amdgcn_exp2f(s[tt][3] - m);
    pb[tt + (ODD ? 1 : 0)] = u32x2{pack2(e0, e1), pack2(e2, e3)};
  }
  wait_bar<0>();
  f32x4 o0 = {0.f, 0.f, 0.f, 0.f}, o1 = {0.f, 0.f, 0.f, 0.f};
  const char* vp = ldsV + (w >> 1) * 2048 + lane * 16;
#pragma unroll
  for (int v = 0; v < 9; ++v) {
    const u32x4 pw = {pb[2 * v][0], pb[2 * v][1], pb[2 * v + 1][0], pb[2 * v + 1][1]};
    const bf16x8 pf = __builtin_bit_cast(bf16x8, pw);
    const bf16x8 v0 = *(const bf16x8*)(vp + v * 2048);
    const bf16x8 v1 = *(const bf16x8*)(vp + v * 2048 + 1024);
    o0 = MFMA16(v0, pf, o0);
    o1 = MFMA16(v1, pf, o1);
  }
  const float sum = __shfl(o1[0], 48 + qq);
  const float inv = 1.f / sum;
  {
    const int k = kbase + 4 * g;
    u32x2 o = {pack2(o0[0] * inv, o0[1] * inv), pack2(o0[2] * inv, o0[3] * inv)};
    *(u32x2*)(odst + (size_t)(k >> 4) * 1024 + ((k >> 3) & 1) * 512 + (k & 4) * 2) = o;
  }
  if (g < 3) {
    const int k = kbase + 16 + 4 * g;
    u32x2 o = {pack2(o1[0] * inv, o1[1] * inv), pack2(o1[2] * inv, o1[3] * inv)};
    *(u32x2*)(odst + (size_t)(k >> 4) * 1024 + ((k >> 3) & 1) * 512 + (k & 4) * 2) = o;
  }
}

__global__ __launch_bounds__(512) void k2_attn(char* __restrict__ ws) {
  extern __shared__ __attribute__((aligned(16))) char lds[];
  const int t = threadIdx.x, lane = t & 63, w = __builtin_amdgcn_readfirstlane(t >> 6);
  const int xc = blockIdx.x & 7, bi = blockIdx.x >> 3;
  const int head = xc * 2 + (bi & 1), qb = bi >> 1;
  char* ldsK = lds;
  char* ldsV = lds + 24 * 1024;
  char* ldsQ = lds + 48 * 1024;
  const int sub0 = qb * 8 - 16;
  GLDS16(ws + OFF_Q + (size_t)(head * 128 + qb * 8 + w) * 1024 + lane * 16, ldsQ + w * 1024);
#pragma unroll
  for (int i = 0; i < 3; ++i) {
    const int p = w * 3 + i;
    const int sidx = max(sub0 + p, 0);
    GLDS16(ws + OFF_K + (size_t)(head * 128 + sidx) * 1024 + lane * 16, ldsK + p * 1024);
  }
#pragma unroll
  for (int i = 0; i < 3; ++i) {
    const int p = w * 3 + i;
    const int sidx = max(sub0 + p, 0);
    GLDS16(ws + OFF_V + (size_t)(head * 128 + sidx) * 1024 + lane * 16, ldsV + p * 1024);
  }
  wait_bar<3>();
  const int tok = qb * 128 + w * 16 + (lane & 15);
  char* odst = ws + OFF_O + (size_t)(tok >> 5) * 28 * 1024 + (tok & 31) * 16;
  const int kbase = head * 28;
  const int nneg = -sub0;
  if (qb >= 2) {
    if (w & 1) attn_wave<true, false>(ldsK, ldsV, ldsQ, w, lane, nneg, odst, kbase);
    else attn_wave<false, false>(ldsK, ldsV, ldsQ, w, lane, nneg, odst, kbase);
  } else {
    if (w & 1) attn_wave<true, true>(ldsK, ldsV, ldsQ, w, lane, nneg, odst, kbase);
    else attn_wave<false, true>(ldsK, ldsV, ldsQ, w, lane, nneg, odst, kbase);
  }
}

__global__ __launch_bounds__(256) void k3_wo(const uint16_t* __restrict__ x, char* __restrict__ ws) {
  extern __shared__ __attribute__((aligned(16))) char lds[];
  using R = Ring<2, 2, 28, 4, 7>;
  const int t = threadIdx.x, lane = t & 63, w = __builtin_amdgcn_readfirstlane(t >> 6);
  if (blockIdx.x >= 224) {
    const int i = (blockIdx.x - 224) * 256 + t;
    const float* gp = (const float*)(ws + OFF_GUP) + i;
    float s = 0.f;
#pragma unroll
    for (int r = 0; r < 14; ++r) s += gp[r * 7168];
    const int arr = i / 3584, mf = i % 3584, mat = mf / 1792, f = mf % 1792;
    ((float*)(ws + OFF_GUF))[(f >> 4) * 64 + (mat * 2 + arr) * 16 + (f & 15)] = s;
    return;
  }
  const int xc = blockIdx.x & 7, bi = blockIdx.x >> 3;
  const int mb = xc * 4 + (bi & 3), nb = bi >> 2;
  const char* A = ws + OFF_O + (size_t)mb * 2 * 28 * 1024;
  const char* B = ws + OFF_WO + (size_t)nb * 2 * 28 * 1024;
  const int wm = w & 1, wn = w >> 1;
  const int hi = lane >> 5, l31 = lane & 31;
  const int mt = mb * 2 + wm, nt = nb * 2 + wn;
  char* aux = lds + 7 * R::SLOTB + w * 2304;
  {
    const uint16_t* xs = x + (size_t)(mt * 32 + l31) * 448 + nt * 32 + hi * 8;
    GLDS16(xs, aux);
    GLDS16(xs + 16, aux + 1024);
    GLDS4(ws + OFF_GAB + (nt * 32 + l31) * 4, aux + 2048);
  }
#pragma unroll
  for (int c = 0; c < 7; ++c) R::issue(A, B, lds, w, lane, c);
  f32x16 acc;
#pragma unroll
  for (int r = 0; r < 16; ++r) acc[r] = 0.f;
  static_for<0, R::NC>([&](auto cc) {
    constexpr int c = decltype(cc)::value;
    wait_bar<R::younger(c)>();
#pragma unroll
    for (int k = 0; k < 4; ++k) {
      bf16x8 xa = *(const bf16x8*)R::frag(lds, c, wm, k, lane);
      bf16x8 wb = *(const bf16x8*)R::frag(lds, c, 2 + wn, k, lane);
      acc = MFMA32(wb, xa, acc);
    }
  });
  float v[16];
#pragma unroll
  for (int s = 0; s < 2; ++s) {
    const u32x4 xv = *(const u32x4*)(aux + s * 1024 + lane * 16);
    const f32x4 g0 = *(const f32x4*)(aux + 2048 + (s * 16 + hi * 8) * 4);
    const f32x4 g1 = *(const f32x4*)(aux + 2048 + (s * 16 + hi * 8 + 4) * 4);
    u32x4 o;
#pragma unroll
    for (int j = 0; j < 4; ++j) {
      const float ga0 = (j < 2) ? g0[2 * j] : g1[2 * j - 4], ga1 = (j < 2) ? g0[2 * j + 1] : g1[2 * j - 3];
      o[j] = pack2(bflo(xv[j]) + ga0 * acc[8 * s + 2 * j], bfhi(xv[j]) + ga1 * acc[8 * s + 2 * j + 1]);
      v[8 * s + 2 * j] = bflo(o[j]);
      v[8 * s + 2 * j + 1] = bfhi(o[j]);
    }
    *(u32x4*)(ws + OFF_X1 + ((size_t)(mt * 28 + nt * 2 + s) * 64 + lane) * 16) = o;
  }
  float sm = 0.f;
#pragma unroll
  for (int j = 0; j < 16; ++j) sm += v[j];
  sm += __shfl_xor(sm, 32);
  const float mean = sm * (1.f / 32.f);
  float m2 = 0.f;
#pragma unroll
  for (int j = 0; j < 16; ++j) { const float d = v[j] - mean; m2 += d * d; }
  m2 += __shfl_xor(m2, 32);
  if (hi == 0) *(f32x2*)(ws + OFF_STATS + ((size_t)(mt * 16 + nt) * 32 + l31) * 8) = f32x2{mean, m2};
}

__global__ __launch_bounds__(256) void k4_gu(char* __restrict__ ws) {
  extern __shared__ __attribute__((aligned(16))) char lds[];
  using R = Ring<4, 7, 28, 4, 3>;
  const int t = threadIdx.x, lane = t & 63, w = __builtin_amdgcn_readfirstlane(t >> 6);
  const int xc = blockIdx.x & 7, bi = blockIdx.x >> 3;
  const int mb = (xc & 1) * 8 + (bi & 7), fb = (xc >> 1) * 4 + (bi >> 3);
  const char* A = ws + OFF_X1 + (size_t)mb * 4 * 28 * 1024;
  const char* B = ws + OFF_WGU + (size_t)fb * 7 * 28 * 1024;
  const int hi = lane >> 5, l31 = lane & 31;
  const int mt = mb * 4 + w;
  char* auxS = lds + 3 * R::SLOTB + w * 4096;
  char* auxG = lds + 3 * R::SLOTB + 16384 + (w >> 1) * 2048;
  {
    const char* ss = ws + OFF_STATS + (size_t)mt * 4096 + lane * 16;
#pragma unroll
    for (int i = 0; i < 4; ++i) GLDS16(ss + i * 1024, auxS + i * 1024);
    GLDS16(ws + OFF_GUF + (size_t)fb * 7 * 256 + (w & 1) * 1024 + lane * 16, auxG + (w & 1) * 1024);
  }
#pragma unroll
  for (int c = 0; c < 3; ++c) R::issue(A, B, lds, w, lane, c);
  f32x16 acc[7];
#pragma unroll
  for (int i = 0; i < 7; ++i)
#pragma unroll
    for (int r = 0; r < 16; ++r) acc[i][r] = 0.f;
  static_for<0, R::NC>([&](auto cc) {
    constexpr int c = decltype(cc)::value;
    wait_bar<R::younger(c)>();
    if constexpr (c >= 1 && c - 1 + 3 < R::NC) R::issue(A, B, lds, w, lane, c - 1 + 3);
#pragma unroll
    for (int k = 0; k < 4; ++k) {
      bf16x8 xa = *(const bf16x8*)R::frag(lds, c, w, k, lane);
#pragma unroll
      for (int n = 0; n < 7; ++n) {
        bf16x8 wb = *(const bf16x8*)R::frag(lds, c, 4 + n, k, lane);
        acc[n] = MFMA32(wb, xa, acc[n]);
      }
    }
  });
  float mu = 0.f, M2 = 0.f;
  {
    f32x2 p[14];
#pragma unroll
    for (int i = 0; i < 14; ++i) { p[i] = *(const f32x2*)(auxS + (i * 32 + l31) * 8); mu += p[i][0]; M2 += p[i][1]; }
    mu *= (1.f / 14.f);
#pragma unroll
    for (int i = 0; i < 14; ++i) { const float d = p[i][0] - mu; M2 += 32.f * d * d; }
  }
  const float rstd = rsqrtf(M2 * (1.f / 448.f) + 1e-5f);
  const float nb = -rstd * mu;
  const float c0 = -2.f * 0.7978845608028654f * 1.4426950408889634f, c1 = c0 * 0.044715f;
  char* hdst = ws + OFF_HFF + ((size_t)(mt * 112 + fb * 7) * 64 + lane) * 16;
#pragma unroll
  for (int n = 0; n < 7; ++n) {
    const float* gv = (const float*)(auxG + n * 256) + hi * 8;
    float h[8];
#pragma unroll
    for (int q = 0; q < 2; ++q) {
      const f32x4 ug = *(const f32x4*)(gv + q * 4), bg = *(const f32x4*)(gv + 16 + q * 4);
      const f32x4 uu = *(const f32x4*)(gv + 32 + q * 4), bu = *(const f32x4*)(gv + 48 + q * 4);
#pragma unroll
      for (int j = 0; j < 4; ++j) {
        const float g = fmaf(rstd, acc[n][q * 4 + j], fmaf(nb, ug[j], bg[j]));
        const float u = fmaf(rstd, acc[n][8 + q * 4 + j], fmaf(nb, uu[j], bu[j]));
        const float e = __builtin_amdgcn_exp2f(g * fmaf(g * g, c1, c0));
        h[q * 4 + j] = g * u * __builtin_amdgcn_rcpf(1.f + e);
      }
    }
    u32x4 o = {pack2(h[0], h[1]), pack2(h[2], h[3]), pack2(h[4], h[5]), pack2(h[6], h[7])};
    *(u32x4*)(hdst + n * 1024) = o;
  }
}

__global__ __launch_bounds__(256) void k5_down(char* __restrict__ ws, uint16_t* __restrict__ out) {
  extern __shared__ __attribute__((aligned(16))) char lds[];
  using R = Ring<2, 2, 112, 4, 9>;
  const int t = threadIdx.x, lane = t & 63, w = __builtin_amdgcn_readfirstlane(t >> 6);
  const int xc = blockIdx.x & 7, bi = blockIdx.x >> 3;
  const int mb = xc * 4 + (bi & 3), nb = bi >> 2;
  const char* A = ws + OFF_HFF + (size_t)mb * 2 * 112 * 1024;
  const char* B = ws + OFF_WD + (size_t)nb * 2 * 112 * 1024;
  const int wm = w & 1, wn = w >> 1;
  const int hi = lane >> 5, l31 = lane & 31;
  const int mt = mb * 2 + wm, nt = nb * 2 + wn;
  char* aux = lds + 9 * R::SLOTB + w * 2304;
  {
    const char* xs = ws + OFF_X1 + ((size_t)(mt * 28 + nt * 2) * 64 + lane) * 16;
    GLDS16(xs, aux);
    GLDS16(xs + 1024, aux + 1024);
    GLDS4(ws + OFF_GAB + (448 + nt * 32 + l31) * 4, aux + 2048);
  }
#pragma unroll
  for (int c = 0; c < 9; ++c) R::issue(A, B, lds, w, lane, c);
  f32x16 acc;
#pragma unroll
  for (int r = 0; r < 16; ++r) acc[r] = 0.f;
  static_for<0, R::NC>([&](auto cc) {
    constexpr int c = decltype(cc)::value;
    wait_bar<R::younger(c)>();
    if constexpr (c >= 1 && c - 1 + 9 < R::NC) R::issue(A, B, lds, w, lane, c - 1 + 9);
#pragma unroll
    for (int k = 0; k < 4; ++k) {
      bf16x8 xa = *(const bf16x8*)R::frag(lds, c, wm, k, lane);
      bf16x8 wb = *(const bf16x8*)R::frag(lds, c, 2 + wn, k, lane);
      acc = MFMA32(wb, xa, acc);
    }
  });
#pragma unroll
  for (int s = 0; s < 2; ++s) {
    const u32x4 xv = *(const u32x4*)(aux + s * 1024 + lane * 16);
    const f32x4 g0 = *(const f32x4*)(aux + 2048 + (s * 16 + hi * 8) * 4);
    const f32x4 g1 = *(const f32x4*)(aux + 2048 + (s * 16 + hi * 8 + 4) * 4);
    u32x4 o;
#pragma unroll
    for (int j = 0; j < 4; ++j) {
      const float ga0 = (j < 2) ? g0[2 * j] : g1[2 * j - 4], ga1 = (j < 2) ? g0[2 * j + 1] : g1[2 * j - 3];
      o[j] = pack2(bflo(xv[j]) + ga0 * acc[8 * s + 2 * j], bfhi(xv[j]) + ga1 * acc[8 * s + 2 * j + 1]);
    }
    *(u32x4*)(out + (size_t)(mt * 32 + l31) * 448 + nt * 32 + s * 16 + hi * 8) = o;
  }
}

constexpr int LDS_K1 = 5 * 28 * 1024 + 8192 + 512;
constexpr int LDS_K2 = 56 * 1024;
constexpr int LDS_K3 = 7 * 16 * 1024 + 4 * 2304;
constexpr int LDS_K4 = 3 * 44 * 1024 + 16384 + 4096;
constexpr int LDS_K5 = 9 * 16 * 1024 + 4 * 2304;

extern "C" void kernel_launch(void* const* d_in, const int* in_sizes, int n_in, void* d_out, int out_size, void* d_ws,
                              size_t ws_size, hipStream_t stream) {
  const uint16_t* x = (const uint16_t*)d_in[0];
  const uint16_t* wqkv = (const uint16_t*)d_in[1];
  const uint16_t* wo = (const uint16_t*)d_in[2];
  const uint16_t* wg = (const uint16_t*)d_in[4];
  const uint16_t* wu = (const uint16_t*)d_in[5];
  const uint16_t* wd = (const uint16_t*)d_in[6];
  const uint16_t* cv = (const uint16_t*)d_in[7];
  const uint16_t* wmod = (const uint16_t*)d_in[8];
  char* ws = (char*)d_ws;
  (void)hipFuncSetAttribute((const void*)k1_qkv, hipFuncAttributeMaxDynamicSharedMemorySize, LDS_K1);
  (void)hipFuncSetAttribute((const void*)k2_attn, hipFuncAttributeMaxDynamicSharedMemorySize, LDS_K2);
  (void)hipFuncSetAttribute((const void*)k3_wo, hipFuncAttributeMaxDynamicSharedMemorySize, LDS_K3);
  (void)hipFuncSetAttribute((const void*)k4_gu, hipFuncAttributeMaxDynamicSharedMemorySize, LDS_K4);
  (void)hipFuncSetAttribute((const void*)k5_down, hipFuncAttributeMaxDynamicSharedMemorySize, LDS_K5);
  k0_prep<<<302, 256, 0, stream>>>(x, wqkv, wo, wg, wu, wd, cv, wmod, ws);
  k1_qkv<<<256, 256, LDS_K1, stream>>>(ws);
  k2_attn<<<256, 512, LDS_K2, stream>>>(ws);
  k3_wo<<<252, 256, LDS_K3, stream>>>(x, ws);
  k4_gu<<<256, 256, LDS_K4, stream>>>(ws);
  k5_down<<<224, 256, LDS_K5, stream>>>(ws, (uint16_t*)d_out);
}
```

```cpp
#include <hip/hip_runtime.h>
#include <stdint.h>

typedef __attribute__((ext_vector_type(8))) short bf16x8;
typedef __attribute__((ext_vector_type(16))) float f32x16;
typedef __attribute__((ext_vector_type(4))) float f32x4;
typedef __attribute__((ext_vector_type(2))) float f32x2;
typedef __attribute__((ext_vector_type(2))) __bf16 bf16x2_t;
typedef __attribute__((ext_vector_type(4))) unsigned u32x4;
typedef __attribute__((ext_vector_type(2))) unsigned u32x2;

#define DEVI __device__ __forceinline__

DEVI unsigned pack2(float a, float b) {
  f32x2 v = {a, b};
  bf16x2_t r = __builtin_convertvector(v, bf16x2_t);
  return __builtin_bit_cast(unsigned, r);
}
DEVI float bflo(unsigned w) { return __builtin_bit_cast(float, w << 16); }
DEVI float bfhi(unsigned w) { return __builtin_bit_cast(float, w & 0xffff0000u); }
DEVI float rbf(float a) { return bflo(pack2(a, a)); }
DEVI int swap23(int d) { return (d & ~12) | ((d & 4) << 1) | ((d & 8) >> 1); }

constexpr size_t OFF_XN = 0;
constexpr size_t OFF_WQKV = OFF_XN + 64 * 28 * 1024;
constexpr size_t OFF_WO = OFF_WQKV + 48 * 28 * 1024;
constexpr size_t OFF_WGU = OFF_WO + 14 * 28 * 1024;
constexpr size_t OFF_WD = OFF_WGU + 112 * 28 * 1024;
constexpr size_t OFF_Q = OFF_WD + 14 * 112 * 1024;
constexpr size_t OFF_K = OFF_Q + 16 * 128 * 1024;
constexpr size_t OFF_V = OFF_K + 16 * 128 * 1024;
constexpr size_t OFF_O = OFF_V + 16 * 128 * 1024;
constexpr size_t OFF_X1 = OFF_O + 64 * 28 * 1024;
constexpr size_t OFF_HFF = OFF_X1 + 64 * 28 * 1024;
constexpr size_t OFF_STATS = OFF_HFF + 64 * 112 * 1024;
constexpr size_t OFF_MODA = OFF_STATS + 64 * 4096;
constexpr size_t OFF_MODB = OFF_MODA + 4096;
constexpr size_t OFF_GAB = OFF_MODB + 4096;
constexpr size_t WS_END = OFF_GAB + 4096;

#define PDECL
#define STAMP(i)
#define PFLUSH(kid)
#define GLDS16(src, dst) __builtin_amdgcn_global_load_lds((const unsigned*)(src), (unsigned*)(dst), 16, 0, 0)
#define GLDS4(src, dst) __builtin_amdgcn_global_load_lds((const unsigned*)(src), (unsigned*)(dst), 4, 0, 0)

template <int N>
DEVI void wait_bar() {
  asm volatile("s_waitcnt vmcnt(%0) lgkmcnt(0)\n\ts_barrier" ::"n"(N) : "memory");
}
template <int N>
DEVI void wait_bar4(u32x4& a, u32x4& b, u32x4& c, u32x4& d) {
  asm volatile("s_waitcnt vmcnt(%4) lgkmcnt(0)\n\ts_barrier" : "+v"(a), "+v"(b), "+v"(c), "+v"(d) : "n"(N) : "memory");
}
DEVI void gload16(u32x4& r, const char* p) {
  asm volatile("global_load_dwordx4 %0, %1, off" : "=v"(r) : "v"(p) : "memory");
}

template <int I, int N, class F>
DEVI void static_for(F&& f) {
  if constexpr (I < N) {
    f(std::integral_constant<int, I>{});
    static_for<I + 1, N>(f);
  }
}

#define MFMA32(a, b, c) __builtin_amdgcn_mfma_f32_32x32x16_bf16(a, b, c, 0, 0, 0)
#define MFMA16(a, b, c) __builtin_amdgcn_mfma_f32_16x16x32_bf16(a, b, c, 0, 0, 0)

__global__ __launch_bounds__(256) void k0_prep(const uint16_t* __restrict__ x, const uint16_t* __restrict__ wqkv,
                                               const uint16_t* __restrict__ wo, const uint16_t* __restrict__ wg,
                                               const uint16_t* __restrict__ wu, const uint16_t* __restrict__ wd,
                                               const uint16_t* __restrict__ cvec, const uint16_t* __restrict__ wmod,
                                               char* __restrict__ ws) {
  __shared__ __attribute__((aligned(16))) float sm[512];
  const int b = blockIdx.x, t = threadIdx.x, lane = t & 63, w = t >> 6;
  PDECL; STAMP(0);

  if (b < 42) {
    const int q8 = t & 7, rs = t >> 3;
    const uint16_t* mp = wmod + (size_t)rs * 2688 + b * 64 + q8 * 8;
    u32x4 mv[14];
    float cs[14];
#pragma unroll
    for (int i = 0; i < 14; ++i) mv[i] = *(const u32x4*)(mp + (size_t)i * 32 * 2688);
#pragma unroll
    for (int i = 0; i < 14; ++i) {
      float cv = bflo((unsigned)cvec[rs + 32 * i]);
      cs[i] = rbf(cv / (1.f + __expf(-cv)));
    }
    float acc[8];
#pragma unroll
    for (int j = 0; j < 8; ++j) acc[j] = 0.f;
#pragma unroll
    for (int i = 0; i < 14; ++i)
#pragma unroll
      for (int j = 0; j < 4; ++j) {
        acc[2 * j] += cs[i] * bflo(mv[i][j]);
        acc[2 * j + 1] += cs[i] * bfhi(mv[i][j]);
      }
#pragma unroll
    for (int j = 0; j < 8; ++j) {
      acc[j] += __shfl_xor(acc[j], 8);
      acc[j] += __shfl_xor(acc[j], 16);
      acc[j] += __shfl_xor(acc[j], 32);
    }
    if (lane < 8) {
#pragma unroll
      for (int j = 0; j < 8; ++j) sm[w * 64 + lane * 8 + j] = acc[j];
    }
    __syncthreads();
    STAMP(1);
    if (t < 64) {
      const float m = rbf(sm[t] + sm[64 + t] + sm[128 + t] + sm[192 + t]);
      const int part = b / 7, k = (b % 7) * 64 + t;
      if (part == 2 || part == 5) {
        ((float*)(ws + OFF_GAB))[(part == 5 ? 448 : 0) + k] = m;
      } else {
        float* dst = (float*)(ws + (part >= 3 ? OFF_MODB : OFF_MODA)) + (k >> 3) * 16 + (k & 7);
        if (part == 1 || part == 4) dst[0] = rbf(1.f + m);
        else dst[8] = m;
      }
    }
    PFLUSH(0);
    return;
  }
  if (b < 106) {
    const int mt = b - 42, tok = lane & 31, half = lane >> 5;
    const uint16_t* src = x + (size_t)(mt * 32 + tok) * 448 + (7 * w) * 16 + half * 8;
    u32x4 v[7];
#pragma unroll
    for (int i = 0; i < 7; ++i) v[i] = *(const u32x4*)(src + i * 16);
    float s = 0.f;
#pragma unroll
    for (int i = 0; i < 7; ++i)
#pragma unroll
      for (int j = 0; j < 4; ++j) s += bflo(v[i][j]) + bfhi(v[i][j]);
    const float lm = s * (1.f / 56.f);
    float m2 = 0.f;
#pragma unroll
    for (int i = 0; i < 7; ++i)
#pragma unroll
      for (int j = 0; j < 4; ++j) {
        float a = bflo(v[i][j]) - lm, c = bfhi(v[i][j]) - lm;
        m2 += a * a + c * c;
      }
    f32x2* part = (f32x2*)sm;
    part[(w * 2 + half) * 32 + tok] = f32x2{lm, m2};
    __syncthreads();
    STAMP(1);
    float mu = 0.f, M2 = 0.f;
    f32x2 p[8];
#pragma unroll
    for (int i = 0; i < 8; ++i) { p[i] = part[i * 32 + tok]; mu += p[i][0]; M2 += p[i][1]; }
    mu *= 0.125f;
#pragma unroll
    for (int i = 0; i < 8; ++i) { float d = p[i][0] - mu; M2 += 56.f * d * d; }
    const float rstd = rsqrtf(M2 * (1.f / 448.f) + 1e-5f);
    char* dst = ws + OFF_XN + ((size_t)(mt * 28 + 7 * w) * 64 + lane) * 16;
#pragma unroll
    for (int i = 0; i < 7; ++i) {
      u32x4 o;
#pragma unroll
      for (int j = 0; j < 4; ++j) o[j] = pack2((bflo(v[i][j]) - mu) * rstd, (bfhi(v[i][j]) - mu) * rstd);
      *(u32x4*)(dst + i * 1024) = o;
    }
    PFLUSH(0);
    return;
  }

  const int rb = b - 106;
  int role, kr, sub = 0;
  if (rb < 42) { role = 0; kr = rb / 3; sub = rb % 3; }
  else if (rb < 56) { role = 2; kr = rb - 42; }
  else if (rb < 168) { role = 1; kr = (rb - 56) >> 3; sub = (rb - 56) & 7; }
  else { role = 3; kr = rb - 168; }
  const int k0 = kr * 32;
  const int koct = t / 56, coct = t % 56;
  const uint16_t* wsrc; int ldw, c0;
  if (role == 0) { wsrc = wqkv; ldw = 1344; c0 = sub * 448; }
  else if (role == 1) { wsrc = (sub & 4) ? wu : wg; ldw = 1792; c0 = (sub & 3) * 448; }
  else if (role == 2) { wsrc = wo; ldw = 448; c0 = 0; }
  else { wsrc = wd; ldw = 448; c0 = 0; }
  if (t < 224) {
    u32x4 wv[8];
    const uint16_t* p = wsrc + (size_t)(k0 + koct * 8) * ldw + c0 + coct * 8;
#pragma unroll
    for (int i = 0; i < 8; ++i) wv[i] = *(const u32x4*)(p + (size_t)i * ldw);
    const int ksl = koct >> 1, half = koct & 1;
#pragma unroll
    for (int c = 0; c < 8; ++c) {
      u32x4 o;
#pragma unroll
      for (int i = 0; i < 4; ++i)
        o[i] = __builtin_amdgcn_perm(wv[2 * i + 1][c >> 1], wv[2 * i][c >> 1], (c & 1) ? 0x07060302u : 0x05040100u);
      const int n = coct * 8 + c;
      char* dst;
      if (role == 0) {
        const int head = n / 28, d = n % 28;
        const int idx = (sub < 2) ? swap23(d) : d;
        dst = ws + OFF_WQKV + ((size_t)((head * 3 + sub) * 28 + 2 * kr + ksl) * 64 + half * 32 + idx) * 16;
      } else if (role == 1) {
        const int f = (sub & 3) * 448 + n;
        const int idx = (sub >> 2) * 16 + swap23(f & 15);
        dst = ws + OFF_WGU + ((size_t)((f >> 4) * 28 + 2 * kr + ksl) * 64 + half * 32 + idx) * 16;
      } else if (role == 2) {
        dst = ws + OFF_WO + ((size_t)((n >> 5) * 28 + 2 * kr + ksl) * 64 + half * 32 + swap23(n & 31)) * 16;
      } else {
        dst = ws + OFF_WD + ((size_t)((n >> 5) * 112 + 2 * kr + ksl) * 64 + half * 32 + swap23(n & 31)) * 16;
      }
      *(u32x4*)dst = o;
    }
  } else if (role == 0) {
    const int z = t - 224;
#pragma unroll
    for (int i = 0; i < 8; ++i) {
      const int pc = z * 8 + i, head = pc >> 4, d = 28 + ((pc >> 2) & 3), ko = pc & 3;
      const int idx = (sub < 2) ? swap23(d) : d;
      char* dst = ws + OFF_WQKV + ((size_t)((head * 3 + sub) * 28 + 2 * kr + (ko >> 1)) * 64 + (ko & 1) * 32 + idx) * 16;
      *(u32x4*)dst = u32x4{0, 0, 0, 0};
    }
  }
  PFLUSH(0);
}

template <bool AFF>
DEVI bf16x8 modulate(const u32x4 xv, const char* mv, float a, float b) {
  const f32x4 s0 = *(const f32x4*)(mv), s1 = *(const f32x4*)(mv + 16);
  const f32x4 h0 = *(const f32x4*)(mv + 32), h1 = *(const f32x4*)(mv + 48);
  u32x4 o;
#pragma unroll
  for (int j = 0; j < 4; ++j) {
    f32x2 v = {bflo(xv[j]), bfhi(xv[j])};
    if (AFF) v = __builtin_elementwise_fma(v, f32x2{a, a}, f32x2{b, b});
    const f32x2 sc = (j < 2) ? f32x2{s0[2 * j], s0[2 * j + 1]} : f32x2{s1[2 * j - 4], s1[2 * j - 3]};
    const f32x2 sh = (j < 2) ? f32x2{h0[2 * j], h0[2 * j + 1]} : f32x2{h1[2 * j - 4], h1[2 * j - 3]};
    v = __builtin_elementwise_fma(v, sc, sh);
    o[j] = pack2(v[0], v[1]);
  }
  return __builtin_bit_cast(bf16x8, o);
}

__global__ __launch_bounds__(256) __attribute__((amdgpu_waves_per_eu(1, 1))) void k1_qkv(char* __restrict__ ws) {
  extern __shared__ __attribute__((aligned(16))) char lds[];
  const int t = threadIdx.x, lane = t & 63, w = __builtin_amdgcn_readfirstlane(t >> 6);
  PDECL; STAMP(0);
  const int xc = blockIdx.x & 7, bi = blockIdx.x >> 3;
  const int mb = (xc & 3) * 4 + (bi & 3), head = (xc >> 2) * 8 + (bi >> 2);
  const int mt = mb * 4 + w;
  const char* A = ws + OFF_XN + (size_t)mt * 28 * 1024 + lane * 16;
  const char* B = ws + OFF_WQKV + (size_t)head * 3 * 28 * 1024 + lane * 16;
  char* modv = lds + 84 * 1024;
  GLDS16(ws + OFF_MODA + w * 1024 + lane * 16, modv + w * 1024);
  u32x4 xr[28];
#pragma unroll
  for (int c = 0; c < 7; ++c) {
#pragma unroll
    for (int i = 0; i < 3; ++i) GLDS16(B + (size_t)(i * 28 + 4 * c + w) * 1024, lds + ((4 * c + w) * 3 + i) * 1024);
#pragma unroll
    for (int i = 0; i < 4; ++i) gload16(xr[4 * c + i], A + (4 * c + i) * 1024);
  }
  f32x16 acc[3];
#pragma unroll
  for (int i = 0; i < 3; ++i)
#pragma unroll
    for (int r = 0; r < 16; ++r) acc[i][r] = 0.f;
  const char* wl = lds + lane * 16;
  const char* ml = modv + (lane >> 5) * 64;
  wait_bar4<42>(xr[0], xr[1], xr[2], xr[3]);
  STAMP(1);
  bf16x8 wb[2][3], h[2];
#pragma unroll
  for (int i = 0; i < 3; ++i) wb[0][i] = *(const bf16x8*)(wl + i * 1024);
  h[0] = modulate<false>(xr[0], ml, 0.f, 0.f);
  static_for<0, 28>([&](auto kc) {
    constexpr int k = decltype(kc)::value;
    if constexpr (k + 1 < 28) {
      if constexpr ((k + 1) % 4 == 0) wait_bar4<42 - 7 * ((k + 1) / 4)>(xr[k + 1], xr[k + 2], xr[k + 3], xr[k + 4]);
#pragma unroll
      for (int i = 0; i < 3; ++i) wb[(k + 1) & 1][i] = *(const bf16x8*)(wl + ((k + 1) * 3 + i) * 1024);
      h[(k + 1) & 1] = modulate<false>(xr[k + 1], ml + (k + 1) * 128, 0.f, 0.f);
    }
    acc[0] = MFMA32(wb[k & 1][0], h[k & 1], acc[0]);
    acc[1] = MFMA32(wb[k & 1][1], h[k & 1], acc[1]);
    acc[2] = MFMA32(h[k & 1], wb[k & 1][2], acc[2]);
  });
  STAMP(2);
  const int hi = lane >> 5, l31 = lane & 31;
  const float qs = 0.18898223650461362f * 1.4426950408889634f;
#pragma unroll
  for (int p = 0; p < 2; ++p) {
    char* base = ws + (p ? OFF_K : OFF_Q) + ((size_t)(head * 128 + mt * 2 + (l31 >> 4)) * 64 + (l31 & 15)) * 16;
    const float sc = p ? 1.f : qs;
#pragma unroll
    for (int s = 0; s < 2; ++s) {
      u32x4 o;
#pragma unroll
      for (int j = 0; j < 4; ++j) o[j] = pack2(acc[p][8 * s + 2 * j] * sc, acc[p][8 * s + 2 * j + 1] * sc);
      *(u32x4*)(base + (2 * s + hi) * 256) = o;
    }
  }
  {
    char* base = ws + OFF_V + ((size_t)((head * 64 + mt) * 2 + (l31 >> 4)) * 64 + (l31 & 15)) * 16;
#pragma unroll
    for (int s = 0; s < 2; ++s) {
      u32x4 o = {pack2(acc[2][4 * s], acc[2][4 * s + 1]), pack2(acc[2][4 * s + 2], acc[2][4 * s + 3]),
                 pack2(acc[2][8 + 4 * s], acc[2][8 + 4 * s + 1]), pack2(acc[2][8 + 4 * s + 2], acc[2][8 + 4 * s + 3])};
      if (l31 == 28) o = u32x4{0x3f803f80u, 0x3f803f80u, 0x3f803f80u, 0x3f803f80u};
      *(u32x4*)(base + (2 * s + hi) * 256) = o;
    }
  }
  PFLUSH(1);
}

template <bool ODD, bool EDGE>
DEVI void attn_wave(const char* ldsK, const char* ldsV, const char* ldsQ, int w, int lane, int nneg, char* odst, int kbase) {
  const int g = lane >> 4, qq = lane & 15;
  const int s0 = (w >> 1) * 2;
  const bf16x8 q = *(const bf16x8*)(ldsQ + w * 1024 + lane * 16);
  const float NEG = -1e30f;
  f32x4 mlo, mhi;
#pragma unroll
  for (int r = 0; r < 4; ++r) {
    mlo[r] = (4 * g + r > qq) ? 0.f : NEG;
    mhi[r] = (4 * g + r <= qq) ? 0.f : NEG;
  }
  f32x4 s[17];
  const char* kp = ldsK + (s0 + (ODD ? 1 : 0)) * 1024 + lane * 16;
#pragma unroll
  for (int tt = 0; tt < 17; ++tt) {
    f32x4 ci = (tt == 0) ? mlo : (tt == 16) ? mhi : f32x4{0.f, 0.f, 0.f, 0.f};
    if (EDGE) {
      if (w + tt < nneg) ci = f32x4{NEG, NEG, NEG, NEG};
    }
    const bf16x8 kf = *(const bf16x8*)(kp + tt * 1024);
    s[tt] = MFMA16(kf, q, ci);
  }
  float m = NEG;
#pragma unroll
  for (int tt = 0; tt < 17; ++tt) m = fmaxf(fmaxf(m, fmaxf(s[tt][0], s[tt][1])), fmaxf(s[tt][2], s[tt][3]));
  m = fmaxf(m, __shfl_xor(m, 16));
  m = fmaxf(m, __shfl_xor(m, 32));
  u32x2 pb[18];
  pb[ODD ? 0 : 17] = u32x2{0, 0};
#pragma unroll
  for (int tt = 0; tt < 17; ++tt) {
    const float e0 = __builtin_amdgcn_exp2f(s[tt][0] - m), e1 = __builtin_amdgcn_exp2f(s[tt][1] - m);
    const float e2 = __builtin_amdgcn_exp2f(s[tt][2] - m), e3 = __builtin_amdgcn_exp2f(s[tt][3] - m);
    pb[tt + (ODD ? 1 : 0)] = u32x2{pack2(e0, e1), pack2(e2, e3)};
  }
  wait_bar<0>();
  f32x4 o0 = {0.f, 0.f, 0.f, 0.f}, o1 = {0.f, 0.f, 0.f, 0.f};
  const char* vp = ldsV + (w >> 1) * 2048 + lane * 16;
#pragma unroll
  for (int v = 0; v < 9; ++v) {
    const u32x4 pw = {pb[2 * v][0], pb[2 * v][1], pb[2 * v + 1][0], pb[2 * v + 1][1]};
    const bf16x8 pf = __builtin_bit_cast(bf16x8, pw);
    const bf16x8 v0 = *(const bf16x8*)(vp + v * 2048);
    const bf16x8 v1 = *(const bf16x8*)(vp + v * 2048 + 1024);
    o0 = MFMA16(v0, pf, o0);
    o1 = MFMA16(v1, pf, o1);
  }
  const float sum = __shfl(o1[0], 48 + qq);
  const float inv = 1.f / sum;
  {
    const int k = kbase + 4 * g;
    u32x2 o = {pack2(o0[0] * inv, o0[1] * inv), pack2(o0[2] * inv, o0[3] * inv)};
    *(u32x2*)(odst + (size_t)(k >> 4) * 1024 + ((k >> 3) & 1) * 512 + (k & 4) * 2) = o;
  }
  if (g < 3) {
    const int k = kbase + 16 + 4 * g;
    u32x2 o = {pack2(o1[0] * inv, o1[1] * inv), pack2(o1[2] * inv, o1[3] * inv)};
    *(u32x2*)(odst + (size_t)(k >> 4) * 1024 + ((k >> 3) & 1) * 512 + (k & 4) * 2) = o;
  }
}

__global__ __launch_bounds__(512) void k2_attn(char* __restrict__ ws) {
  extern __shared__ __attribute__((aligned(16))) char lds[];
  const int t = threadIdx.x, lane = t & 63, w = __builtin_amdgcn_readfirstlane(t >> 6);
  PDECL; STAMP(0);
  const int xc = blockIdx.x & 7, bi = blockIdx.x >> 3;
  const int head = xc * 2 + (bi & 1), qb = bi >> 1;
  char* ldsK = lds;
  char* ldsV = lds + 24 * 1024;
  char* ldsQ = lds + 48 * 1024;
  const int sub0 = qb * 8 - 16;
  GLDS16(ws + OFF_Q + (size_t)(head * 128 + qb * 8 + w) * 1024 + lane * 16, ldsQ + w * 1024);
#pragma unroll
  for (int i = 0; i < 3; ++i) {
    const int p = w * 3 + i;
    const int sidx = max(sub0 + p, 0);
    GLDS16(ws + OFF_K + (size_t)(head * 128 + sidx) * 1024 + lane * 16, ldsK + p * 1024);
  }
#pragma unroll
  for (int i = 0; i < 3; ++i) {
    const int p = w * 3 + i;
    const int sidx = max(sub0 + p, 0);
    GLDS16(ws + OFF_V + (size_t)(head * 128 + sidx) * 1024 + lane * 16, ldsV + p * 1024);
  }
  wait_bar<3>();
  STAMP(1);
  const int tok = qb * 128 + w * 16 + (lane & 15);
  char* odst = ws + OFF_O + (size_t)(tok >> 5) * 28 * 1024 + (tok & 31) * 16;
  const int kbase = head * 28;
  const int nneg = -sub0;
  if (qb >= 2) {
    if (w & 1) attn_wave<true, false>(ldsK, ldsV, ldsQ, w, lane, nneg, odst, kbase);
    else attn_wave<false, false>(ldsK, ldsV, ldsQ, w, lane, nneg, odst, kbase);
  } else {
    if (w & 1) attn_wave<true, true>(ldsK, ldsV, ldsQ, w, lane, nneg, odst, kbase);
    else attn_wave<false, true>(ldsK, ldsV, ldsQ, w, lane, nneg, odst, kbase);
  }
  PFLUSH(2);
}

DEVI f32x16 ksplit_reduce(f32x16 (&acc)[4], char* lds, int w, int lane) {
#pragma unroll
  for (int tl = 0; tl < 4; ++tl) {
    if (tl != w) {
      const int slot = (w > tl) ? w - 1 : w;
      char* p = lds + (tl * 3 + slot) * 4096 + lane * 16;
#pragma unroll
      for (int q = 0; q < 4; ++q)
        *(f32x4*)(p + q * 1024) = f32x4{acc[tl][4 * q], acc[tl][4 * q + 1], acc[tl][4 * q + 2], acc[tl][4 * q + 3]};
    }
  }
  f32x16 r = (w == 0) ? acc[0] : (w == 1) ? acc[1] : (w == 2) ? acc[2] : acc[3];
  __syncthreads();
#pragma unroll
  for (int s = 0; s < 3; ++s) {
    const char* p = lds + (w * 3 + s) * 4096 + lane * 16;
#pragma unroll
    for (int q = 0; q < 4; ++q) {
      const f32x4 v = *(const f32x4*)(p + q * 1024);
#pragma unroll
      for (int j = 0; j < 4; ++j) r[4 * q + j] += v[j];
    }
  }
  return r;
}

__global__ __launch_bounds__(256) __attribute__((amdgpu_waves_per_eu(1, 1))) void k3_wo(const uint16_t* __restrict__ x,
                                                                                        char* __restrict__ ws) {
  __shared__ __attribute__((aligned(16))) char lds[48 * 1024];
  const int t = threadIdx.x, lane = t & 63, w = __builtin_amdgcn_readfirstlane(t >> 6);
  PDECL; STAMP(0);
  const int xc = blockIdx.x & 7, bi = blockIdx.x >> 3;
  const int mb = xc * 4 + (bi & 3), nb = bi >> 2;
  const char* A = ws + OFF_O + ((size_t)mb * 2 * 28 + 7 * w) * 1024 + lane * 16;
  const char* B = ws + OFF_WO + ((size_t)nb * 2 * 28 + 7 * w) * 1024 + lane * 16;
  const int wm = w & 1, wn = w >> 1;
  const int hi = lane >> 5, l31 = lane & 31;
  const int mt = mb * 2 + wm, nt = nb * 2 + wn;
  bf16x8 a[7][2], bw[7][2];
#pragma unroll
  for (int k = 0; k < 7; ++k) {
    a[k][0] = *(const bf16x8*)(A + k * 1024);
    bw[k][0] = *(const bf16x8*)(B + k * 1024);
    a[k][1] = *(const bf16x8*)(A + (28 + k) * 1024);
    bw[k][1] = *(const bf16x8*)(B + (28 + k) * 1024);
  }
  u32x4 xv[2];
  f32x4 gv[4];
  {
    const uint16_t* xs = x + (size_t)(mt * 32 + l31) * 448 + nt * 32 + hi * 8;
    xv[0] = *(const u32x4*)xs;
    xv[1] = *(const u32x4*)(xs + 16);
    const float* gp = (const float*)(ws + OFF_GAB) + nt * 32 + hi * 8;
    gv[0] = *(const f32x4*)gp; gv[1] = *(const f32x4*)(gp + 4);
    gv[2] = *(const f32x4*)(gp + 16); gv[3] = *(const f32x4*)(gp + 20);
  }
  __builtin_amdgcn_sched_barrier(0);
  f32x16 acc[4];
#pragma unroll
  for (int i = 0; i < 4; ++i)
#pragma unroll
    for (int r = 0; r < 16; ++r) acc[i][r] = 0.f;
#pragma unroll
  for (int k = 0; k < 7; ++k) {
    acc[0] = MFMA32(bw[k][0], a[k][0], acc[0]);
    acc[1] = MFMA32(bw[k][0], a[k][1], acc[1]);
    acc[2] = MFMA32(bw[k][1], a[k][0], acc[2]);
    acc[3] = MFMA32(bw[k][1], a[k][1], acc[3]);
  }
  STAMP(1);
  const f32x16 r = ksplit_reduce(acc, lds, w, lane);
  STAMP(2);
  float v[16];
#pragma unroll
  for (int s = 0; s < 2; ++s) {
    u32x4 o;
#pragma unroll
    for (int j = 0; j < 4; ++j) {
      const float ga0 = gv[2 * s + (j >> 1)][(2 * j) & 3], ga1 = gv[2 * s + (j >> 1)][(2 * j + 1) & 3];
      o[j] = pack2(bflo(xv[s][j]) + ga0 * r[8 * s + 2 * j], bfhi(xv[s][j]) + ga1 * r[8 * s + 2 * j + 1]);
      v[8 * s + 2 * j] = bflo(o[j]);
      v[8 * s + 2 * j + 1] = bfhi(o[j]);
    }
    *(u32x4*)(ws + OFF_X1 + ((size_t)(mt * 28 + nt * 2 + s) * 64 + lane) * 16) = o;
  }
  float sm = 0.f;
#pragma unroll
  for (int j = 0; j < 16; ++j) sm += v[j];
  sm += __shfl_xor(sm, 32);
  const float mean = sm * (1.f / 32.f);
  float m2 = 0.f;
#pragma unroll
  for (int j = 0; j < 16; ++j) { const float d = v[j] - mean; m2 += d * d; }
  m2 += __shfl_xor(m2, 32);
  if (hi == 0) *(f32x2*)(ws + OFF_STATS + ((size_t)(mt * 16 + nt) * 32 + l31) * 8) = f32x2{mean, m2};
  PFLUSH(3);
}

__global__ __launch_bounds__(256) __attribute__((amdgpu_waves_per_eu(1, 1))) void k4_gu(char* __restrict__ ws) {
  extern __shared__ __attribute__((aligned(16))) char lds[];
  const int t = threadIdx.x, lane = t & 63, w = __builtin_amdgcn_readfirstlane(t >> 6);
  PDECL; STAMP(0);
  const int xc = blockIdx.x & 7, bi = blockIdx.x >> 3;
  const int mb = (xc & 1) * 8 + (bi & 7), fb = (xc >> 1) * 4 + (bi >> 3);
  const int mt = mb * 4 + w;
  const char* A = ws + OFF_X1 + (size_t)mt * 28 * 1024 + lane * 16;
  const char* B = ws + OFF_WGU + (size_t)fb * 7 * 28 * 1024 + lane * 16;
  const int hi = lane >> 5, l31 = lane & 31;
  char* modv = lds + 140 * 1024;
  char* auxS = modv + 4096 + w * 4096;
  GLDS16(ws + OFF_MODB + w * 1024 + lane * 16, modv + w * 1024);
  {
    const char* ss = ws + OFF_STATS + (size_t)mt * 4096 + lane * 16;
#pragma unroll
    for (int i = 0; i < 4; ++i) GLDS16(ss + i * 1024, auxS + i * 1024);
  }
  u32x4 xr[28];
  auto issue = [&](auto cc) {
    constexpr int c = decltype(cc)::value;
#pragma unroll
    for (int i = 0; i < 7; ++i)
      GLDS16(B + (size_t)(i * 28 + 4 * c + w) * 1024, lds + (c % 5) * 28672 + (w * 7 + i) * 1024);
#pragma unroll
    for (int i = 0; i < 4; ++i) gload16(xr[4 * c + i], A + (4 * c + i) * 1024);
  };
  static_for<0, 5>(issue);
  f32x16 acc[7];
#pragma unroll
  for (int i = 0; i < 7; ++i)
#pragma unroll
    for (int r = 0; r < 16; ++r) acc[i][r] = 0.f;
  const char* wl = lds + lane * 16;
  const char* ml = modv + hi * 64;
  wait_bar4<44>(xr[0], xr[1], xr[2], xr[3]);
  STAMP(1);
  float mu = 0.f, M2 = 0.f;
  {
    f32x2 p[14];
#pragma unroll
    for (int i = 0; i < 14; ++i) { p[i] = *(const f32x2*)(auxS + (i * 32 + l31) * 8); mu += p[i][0]; M2 += p[i][1]; }
    mu *= (1.f / 14.f);
#pragma unroll
    for (int i = 0; i < 14; ++i) { const float d = p[i][0] - mu; M2 += 32.f * d * d; }
  }
  const float rstd = rsqrtf(M2 * (1.f / 448.f) + 1e-5f);
  const float nb = -rstd * mu;
  bf16x8 wb[2][7], h[2];
#pragma unroll
  for (int i = 0; i < 7; ++i) wb[0][i] = *(const bf16x8*)(wl + i * 1024);
  h[0] = modulate<true>(xr[0], ml, rstd, nb);
  static_for<0, 28>([&](auto kc) {
    constexpr int k = decltype(kc)::value;
    if constexpr (k + 1 < 28) {
      constexpr int cn = (k + 1) / 4;
      if constexpr ((k + 1) % 4 == 0) {
        constexpr int issued = (4 + cn - 1 < 6) ? 4 + cn - 1 : 6;
        wait_bar4<(issued - cn) * 11>(xr[k + 1], xr[k + 2], xr[k + 3], xr[k + 4]);
        if constexpr (cn + 4 < 7) issue(std::integral_constant<int, cn + 4>{});
      }
#pragma unroll
      for (int i = 0; i < 7; ++i)
        wb[(k + 1) & 1][i] = *(const bf16x8*)(wl + (cn % 5) * 28672 + (((k + 1) & 3) * 7 + i) * 1024);
      h[(k + 1) & 1] = modulate<true>(xr[k + 1], ml + (k + 1) * 128, rstd, nb);
    }
#pragma unroll
    for (int n = 0; n < 7; ++n) acc[n] = MFMA32(wb[k & 1][n], h[k & 1], acc[n]);
  });
  STAMP(2);
  const float c0 = -2.f * 0.7978845608028654f * 1.4426950408889634f, c1 = c0 * 0.044715f;
  char* hdst = ws + OFF_HFF + ((size_t)(mt * 112 + fb * 7) * 64 + lane) * 16;
#pragma unroll
  for (int n = 0; n < 7; ++n) {
    float hh[8];
#pragma unroll
    for (int j = 0; j < 8; ++j) {
      const float g = acc[n][j], u = acc[n][8 + j];
      const float e = __builtin_amdgcn_exp2f(g * fmaf(g * g, c1, c0));
      hh[j] = g * u * __builtin_amdgcn_rcpf(1.f + e);
    }
    u32x4 o = {pack2(hh[0], hh[1]), pack2(hh[2], hh[3]), pack2(hh[4], hh[5]), pack2(hh[6], hh[7])};
    *(u32x4*)(hdst + n * 1024) = o;
  }
  PFLUSH(4);
}

__global__ __launch_bounds__(256) __attribute__((amdgpu_waves_per_eu(1, 1))) void k5_down(char* __restrict__ ws,
                                                                                          uint16_t* __restrict__ out) {
  __shared__ __attribute__((aligned(16))) char lds[48 * 1024];
  const int t = threadIdx.x, lane = t & 63, w = __builtin_amdgcn_readfirstlane(t >> 6);
  PDECL; STAMP(0);
  const int xc = blockIdx.x & 7, bi = blockIdx.x >> 3;
  const int mb = xc * 4 + (bi & 3), nb = bi >> 2;
  const char* A = ws + OFF_HFF + ((size_t)mb * 2 * 112 + 28 * w) * 1024 + lane * 16;
  const char* B = ws + OFF_WD + ((size_t)nb * 2 * 112 + 28 * w) * 1024 + lane * 16;
  const int wm = w & 1, wn = w >> 1;
  const int hi = lane >> 5, l31 = lane & 31;
  const int mt = mb * 2 + wm, nt = nb * 2 + wn;
  constexpr int D = 12;
  bf16x8 a[D][2], bw[D][2];
  auto ld = [&](int k) {
    a[k % D][0] = *(const bf16x8*)(A + k * 1024);
    bw[k % D][0] = *(const bf16x8*)(B + k * 1024);
    a[k % D][1] = *(const bf16x8*)(A + (112 + k) * 1024);
    bw[k % D][1] = *(const bf16x8*)(B + (112 + k) * 1024);
  };
#pragma unroll
  for (int k = 0; k < D; ++k) ld(k);
  u32x4 xv[2];
  f32x4 gv[4];
  f32x16 acc[4];
#pragma unroll
  for (int i = 0; i < 4; ++i)
#pragma unroll
    for (int r = 0; r < 16; ++r) acc[i][r] = 0.f;
  __builtin_amdgcn_sched_barrier(0);
#pragma unroll
  for (int k = 0; k < 28; ++k) {
    acc[0] = MFMA32(bw[k % D][0], a[k % D][0], acc[0]);
    acc[1] = MFMA32(bw[k % D][0], a[k % D][1], acc[1]);
    acc[2] = MFMA32(bw[k % D][1], a[k % D][0], acc[2]);
    acc[3] = MFMA32(bw[k % D][1], a[k % D][1], acc[3]);
    if (k + D < 28) ld(k + D);
    if (k + D == 28) {
      const char* xs = ws + OFF_X1 + ((size_t)(mt * 28 + nt * 2) * 64 + lane) * 16;
      xv[0] = *(const u32x4*)xs;
      xv[1] = *(const u32x4*)(xs + 1024);
      const float* gp = (const float*)(ws + OFF_GAB) + 448 + nt * 32 + hi * 8;
      gv[0] = *(const f32x4*)gp; gv[1] = *(const f32x4*)(gp + 4);
      gv[2] = *(const f32x4*)(gp + 16); gv[3] = *(const f32x4*)(gp + 20);
    }
    __builtin_amdgcn_sched_barrier(0);
  }
  STAMP(1);
  const f32x16 r = ksplit_reduce(acc, lds, w, lane);
  STAMP(2);
#pragma unroll
  for (int s = 0; s < 2; ++s) {
    u32x4 o;
#pragma unroll
    for (int j = 0; j < 4; ++j) {
      const float ga0 = gv[2 * s + (j >> 1)][(2 * j) & 3], ga1 = gv[2 * s + (j >> 1)][(2 * j + 1) & 3];
      o[j] = pack2(bflo(xv[s][j]) + ga0 * r[8 * s + 2 * j], bfhi(xv[s][j]) + ga1 * r[8 * s + 2 * j + 1]);
    }
    *(u32x4*)(out + (size_t)(mt * 32 + l31) * 448 + nt * 32 + s * 16 + hi * 8) = o;
  }
  PFLUSH(5);
}

constexpr int LDS_K1 = 84 * 1024 + 4096;
constexpr int LDS_K2 = 56 * 1024;
constexpr int LDS_K4 = 140 * 1024 + 4096 + 16384;

extern "C" void kernel_launch(void* const* d_in, const int* in_sizes, int n_in, void* d_out, int out_size, void* d_ws,
                              size_t ws_size, hipStream_t stream) {
  const uint16_t* x = (const uint16_t*)d_in[0];
  const uint16_t* wqkv = (const uint16_t*)d_in[1];
  const uint16_t* wo = (const uint16_t*)d_in[2];
  const uint16_t* wg = (const uint16_t*)d_in[4];
  const uint16_t* wu = (const uint16_t*)d_in[5];
  const uint16_t* wd = (const uint16_t*)d_in[6];
  const uint16_t* cv = (const uint16_t*)d_in[7];
  const uint16_t* wmod = (const uint16_t*)d_in[8];
  char* ws = (char*)d_ws;
  (void)hipFuncSetAttribute((const void*)k1_qkv, hipFuncAttributeMaxDynamicSharedMemorySize, LDS_K1);
  (void)hipFuncSetAttribute((const void*)k2_attn, hipFuncAttributeMaxDynamicSharedMemorySize, LDS_K2);
  (void)hipFuncSetAttribute((const void*)k4_gu, hipFuncAttributeMaxDynamicSharedMemorySize, LDS_K4);
  k0_prep<<<330, 256, 0, stream>>>(x, wqkv, wo, wg, wu, wd, cv, wmod, ws);
  k1_qkv<<<256, 256, LDS_K1, stream>>>(ws);
  k2_attn<<<256, 512, LDS_K2, stream>>>(ws);
  k3_wo<<<224, 256, 0, stream>>>(x, ws);
  k4_gu<<<256, 256, LDS_K4, stream>>>(ws);
  k5_down<<<224, 256, 0, stream>>>(ws, (uint16_t*)d_out);
}
```

```cpp
#include <hip/hip_runtime.h>
#include <stdint.h>

typedef __attribute__((ext_vector_type(8))) short bf16x8;
typedef __attribute__((ext_vector_type(16))) float f32x16;
typedef __attribute__((ext_vector_type(4))) float f32x4;
typedef __attribute__((ext_vector_type(2))) float f32x2;
typedef __attribute__((ext_vector_type(2))) __bf16 bf16x2_t;
typedef __attribute__((ext_vector_type(4))) unsigned u32x4;
typedef __attribute__((ext_vector_type(2))) unsigned u32x2;

#define DEVI __device__ __forceinline__

DEVI unsigned pack2(float a, float b) {
  f32x2 v = {a, b};
  bf16x2_t r = __builtin_convertvector(v, bf16x2_t);
  return __builtin_bit_cast(unsigned, r);
}
DEVI float bflo(unsigned w) { return __builtin_bit_cast(float, w << 16); }
DEVI float bfhi(unsigned w) { return __builtin_bit_cast(float, w & 0xffff0000u); }
DEVI float rbf(float a) { return bflo(pack2(a, a)); }
DEVI int swap23(int d) { return (d & ~12) | ((d & 4) << 1) | ((d & 8) >> 1); }

constexpr size_t OFF_XN = 0;
constexpr size_t OFF_WQKV = OFF_XN + 64 * 28 * 1024;
constexpr size_t OFF_WO = OFF_WQKV + 48 * 28 * 1024;
constexpr size_t OFF_WGU = OFF_WO + 14 * 28 * 1024;
constexpr size_t OFF_WD = OFF_WGU + 112 * 28 * 1024;
constexpr size_t OFF_Q = OFF_WD + 14 * 112 * 1024;
constexpr size_t OFF_K = OFF_Q + 16 * 128 * 1024;
constexpr size_t OFF_V = OFF_K + 16 * 128 * 1024;
constexpr size_t OFF_O = OFF_V + 16 * 128 * 1024;
constexpr size_t OFF_X1 = OFF_O + 64 * 28 * 1024;
constexpr size_t OFF_HFF = OFF_X1 + 64 * 28 * 1024;
constexpr size_t OFF_STATS = OFF_HFF + 64 * 112 * 1024;
constexpr size_t OFF_MODA = OFF_STATS + 64 * 4096;
constexpr size_t OFF_MODB = OFF_MODA + 4096;
constexpr size_t OFF_GAB = OFF_MODB + 4096;
constexpr size_t WS_END = OFF_GAB + 4096;

#define PDECL
#define STAMP(i)
#define PFLUSH(kid)
#define GLDS16(src, dst) __builtin_amdgcn_global_load_lds((const unsigned*)(src), (unsigned*)(dst), 16, 0, 0)
#define GLDS4(src, dst) __builtin_amdgcn_global_load_lds((const unsigned*)(src), (unsigned*)(dst), 4, 0, 0)

template <int N>
DEVI void wait_bar() {
  asm volatile("s_waitcnt vmcnt(%0) lgkmcnt(0)\n\ts_barrier" ::"n"(N) : "memory");
}
template <int N>
DEVI void wait_bar4(u32x4& a, u32x4& b, u32x4& c, u32x4& d) {
  asm volatile("s_waitcnt vmcnt(%4) lgkmcnt(0)\n\ts_barrier" : "+v"(a), "+v"(b), "+v"(c), "+v"(d) : "n"(N) : "memory");
}
DEVI void gload16(u32x4& r, const char* p) {
  asm volatile("global_load_dwordx4 %0, %1, off" : "=v"(r) : "v"(p) : "memory");
}

typedef __attribute__((address_space(3))) char lds_char;
DEVI unsigned lds_addr(const char* p) { return (unsigned)(size_t)(lds_char*)p; }
template <int OFF>
DEVI void lread16(u32x4& r, unsigned a) {
  asm volatile("ds_read_b128 %0, %1 offset:%2" : "=v"(r) : "v"(a), "n"(OFF) : "memory");
}
template <int OFF>
DEVI void lwrite16(const u32x4& r, unsigned a) {
  asm volatile("ds_write_b128 %1, %0 offset:%2" ::"v"(r), "v"(a), "n"(OFF) : "memory");
}
template <int N>
DEVI void lwait() { asm volatile("s_waitcnt lgkmcnt(%0)" ::"n"(N) : "memory"); }
template <int N>
DEVI void vwait() { asm volatile("s_waitcnt vmcnt(%0)" ::"n"(N) : "memory"); }
DEVI void rdyv(u32x4& a) { asm volatile("" : "+v"(a)); }
DEVI void rdya(u32x4& a) { asm volatile("" : "+a"(a)); }
DEVI void gload16a(u32x4& r, const char* p) {
  asm volatile("global_load_dwordx4 %0, %1, off" : "=a"(r) : "v"(p) : "memory");
}
DEVI float dot2(unsigned a, unsigned b, float c) {
  return __builtin_amdgcn_fdot2_f32_bf16(__builtin_bit_cast(bf16x2_t, a), __builtin_bit_cast(bf16x2_t, b), c, false);
}
#define SB() __builtin_amdgcn_sched_barrier(0)
#define BF8(x) __builtin_bit_cast(bf16x8, x)
#define IC(v) std::integral_constant<int, (v)>{}

template <int I, int N, class F>
DEVI void static_for(F&& f) {
  if constexpr (I < N) {
    f(std::integral_constant<int, I>{});
    static_for<I + 1, N>(f);
  }
}

#define MFMA32(a, b, c) __builtin_amdgcn_mfma_f32_32x32x16_bf16(a, b, c, 0, 0, 0)
#define MFMA16(a, b, c) __builtin_amdgcn_mfma_f32_16x16x32_bf16(a, b, c, 0, 0, 0)

__global__ __launch_bounds__(256) void k0_prep(const uint16_t* __restrict__ x, const uint16_t* __restrict__ wqkv,
                                               const uint16_t* __restrict__ wo, const uint16_t* __restrict__ wg,
                                               const uint16_t* __restrict__ wu, const uint16_t* __restrict__ wd,
                                               const uint16_t* __restrict__ cvec, const uint16_t* __restrict__ wmod,
                                               char* __restrict__ ws) {
  __shared__ __attribute__((aligned(16))) float sm[512];
  const int b = blockIdx.x, t = threadIdx.x, lane = t & 63, w = t >> 6;
  PDECL; STAMP(0);

  if (b < 42) {
    const int q8 = t & 7, rs = t >> 3;
    const uint16_t* mp = wmod + (size_t)rs * 2688 + b * 64 + q8 * 8;
    u32x4 mv[14];
    float cs[14];
#pragma unroll
    for (int i = 0; i < 14; ++i) mv[i] = *(const u32x4*)(mp + (size_t)i * 32 * 2688);
#pragma unroll
    for (int i = 0; i < 14; ++i) {
      float cv = bflo((unsigned)cvec[rs + 32 * i]);
      cs[i] = rbf(cv / (1.f + __expf(-cv)));
    }
    float acc[8];
#pragma unroll
    for (int j = 0; j < 8; ++j) acc[j] = 0.f;
#pragma unroll
    for (int i = 0; i < 14; ++i)
#pragma unroll
      for (int j = 0; j < 4; ++j) {
        acc[2 * j] += cs[i] * bflo(mv[i][j]);
        acc[2 * j + 1] += cs[i] * bfhi(mv[i][j]);
      }
#pragma unroll
    for (int j = 0; j < 8; ++j) {
      acc[j] += __shfl_xor(acc[j], 8);
      acc[j] += __shfl_xor(acc[j], 16);
      acc[j] += __shfl_xor(acc[j], 32);
    }
    if (lane < 8) {
#pragma unroll
      for (int j = 0; j < 8; ++j) sm[w * 64 + lane * 8 + j] = acc[j];
    }
    __syncthreads();
    STAMP(1);
    if (t < 64) {
      const float m = rbf(sm[t] + sm[64 + t] + sm[128 + t] + sm[192 + t]);
      const int part = b / 7, k = (b % 7) * 64 + t;
      if (part == 2 || part == 5) {
        ((float*)(ws + OFF_GAB))[(part == 5 ? 448 : 0) + k] = m;
      } else {
        uint16_t* dst = (uint16_t*)(ws + (part >= 3 ? OFF_MODB : OFF_MODA)) + (k >> 3) * 16 + (k & 7);
        if (part == 1 || part == 4) dst[0] = (uint16_t)pack2(1.f + m, 0.f);
        else dst[8] = (uint16_t)pack2(m, 0.f);
      }
    }
    PFLUSH(0);
    return;
  }
  if (b < 106) {
    const int mt = b - 42, tok = lane & 31, half = lane >> 5;
    const uint16_t* src = x + (size_t)(mt * 32 + tok) * 448 + (7 * w) * 16 + half * 8;
    u32x4 v[7];
#pragma unroll
    for (int i = 0; i < 7; ++i) v[i] = *(const u32x4*)(src + i * 16);
    float s = 0.f;
#pragma unroll
    for (int i = 0; i < 7; ++i)
#pragma unroll
      for (int j = 0; j < 4; ++j) s += bflo(v[i][j]) + bfhi(v[i][j]);
    const float lm = s * (1.f / 56.f);
    float m2 = 0.f;
#pragma unroll
    for (int i = 0; i < 7; ++i)
#pragma unroll
      for (int j = 0; j < 4; ++j) {
        float a = bflo(v[i][j]) - lm, c = bfhi(v[i][j]) - lm;
        m2 += a * a + c * c;
      }
    f32x2* part = (f32x2*)sm;
    part[(w * 2 + half) * 32 + tok] = f32x2{lm, m2};
    __syncthreads();
    STAMP(1);
    float mu = 0.f, M2 = 0.f;
    f32x2 p[8];
#pragma unroll
    for (int i = 0; i < 8; ++i) { p[i] = part[i * 32 + tok]; mu += p[i][0]; M2 += p[i][1]; }
    mu *= 0.125f;
#pragma unroll
    for (int i = 0; i < 8; ++i) { float d = p[i][0] - mu; M2 += 56.f * d * d; }
    const float rstd = rsqrtf(M2 * (1.f / 448.f) + 1e-5f);
    char* dst = ws + OFF_XN + ((size_t)(mt * 28 + 7 * w) * 64 + lane) * 16;
#pragma unroll
    for (int i = 0; i < 7; ++i) {
      u32x4 o;
#pragma unroll
      for (int j = 0; j < 4; ++j) o[j] = pack2((bflo(v[i][j]) - mu) * rstd, (bfhi(v[i][j]) - mu) * rstd);
      *(u32x4*)(dst + i * 1024) = o;
    }
    PFLUSH(0);
    return;
  }

  const int rb = b - 106;
  int role, kr, sub = 0;
  if (rb < 42) { role = 0; kr = rb / 3; sub = rb % 3; }
  else if (rb < 56) { role = 2; kr = rb - 42; }
  else if (rb < 168) { role = 1; kr = (rb - 56) >> 3; sub = (rb - 56) & 7; }
  else { role = 3; kr = rb - 168; }
  const int k0 = kr * 32;
  const int koct = t / 56, coct = t % 56;
  const uint16_t* wsrc; int ldw, c0;
  if (role == 0) { wsrc = wqkv; ldw = 1344; c0 = sub * 448; }
  else if (role == 1) { wsrc = (sub & 4) ? wu : wg; ldw = 1792; c0 = (sub & 3) * 448; }
  else if (role == 2) { wsrc = wo; ldw = 448; c0 = 0; }
  else { wsrc = wd; ldw = 448; c0 = 0; }
  if (t < 224) {
    u32x4 wv[8];
    const uint16_t* p = wsrc + (size_t)(k0 + koct * 8) * ldw + c0 + coct * 8;
#pragma unroll
    for (int i = 0; i < 8; ++i) wv[i] = *(const u32x4*)(p + (size_t)i * ldw);
    const int ksl = koct >> 1, half = koct & 1;
#pragma unroll
    for (int c = 0; c < 8; ++c) {
      u32x4 o;
#pragma unroll
      for (int i = 0; i < 4; ++i)
        o[i] = __builtin_amdgcn_perm(wv[2 * i + 1][c >> 1], wv[2 * i][c >> 1], (c & 1) ? 0x07060302u : 0x05040100u);
      const int n = coct * 8 + c;
      char* dst;
      if (role == 0) {
        const int head = n / 28, d = n % 28;
        const int idx = (sub < 2) ? swap23(d) : d;
        dst = ws + OFF_WQKV + ((size_t)((head * 3 + sub) * 28 + 2 * kr + ksl) * 64 + half * 32 + idx) * 16;
      } else if (role == 1) {
        const int f = (sub & 3) * 448 + n;
        const int idx = (sub >> 2) * 16 + swap23(f & 15);
        dst = ws + OFF_WGU + ((size_t)((f >> 4) * 28 + 2 * kr + ksl) * 64 + half * 32 + idx) * 16;
      } else if (role == 2) {
        dst = ws + OFF_WO + ((size_t)((n >> 5) * 28 + 2 * kr + ksl) * 64 + half * 32 + swap23(n & 31)) * 16;
      } else {
        dst = ws + OFF_WD + ((size_t)((n >> 5) * 112 + 2 * kr + ksl) * 64 + half * 32 + swap23(n & 31)) * 16;
      }
      *(u32x4*)dst = o;
    }
  } else if (role == 0) {
    const int z = t - 224;
#pragma unroll
    for (int i = 0; i < 8; ++i) {
      const int pc = z * 8 + i, head = pc >> 4, d = 28 + ((pc >> 2) & 3), ko = pc & 3;
      const int idx = (sub < 2) ? swap23(d) : d;
      char* dst = ws + OFF_WQKV + ((size_t)((head * 3 + sub) * 28 + 2 * kr + (ko >> 1)) * 64 + (ko & 1) * 32 + idx) * 16;
      *(u32x4*)dst = u32x4{0, 0, 0, 0};
    }
  }
  PFLUSH(0);
}

__global__ __launch_bounds__(256) __attribute__((amdgpu_waves_per_eu(1, 1))) void k1_qkv(char* __restrict__ ws) {
  extern __shared__ __attribute__((aligned(16))) char lds[];
  const int t = threadIdx.x, lane = t & 63, w = __builtin_amdgcn_readfirstlane(t >> 6);
  PDECL; STAMP(0);
  const int xc = blockIdx.x & 7, bi = blockIdx.x >> 3;
  const int mb = (xc & 3) * 4 + (bi & 3), head = (xc >> 2) * 8 + (bi >> 2);
  const int mt = mb * 4 + w;
  const int hi = lane >> 5, l31 = lane & 31;
  const char* A = ws + OFF_XN + (size_t)mt * 28 * 1024 + lane * 16;
  const char* B = ws + OFF_WQKV + ((size_t)head * 3 * 28 + w) * 1024 + lane * 16;
  char* modv = lds + 87 * 1024 + w * 2048;
  float* P = (float*)(lds + 95 * 1024);
  GLDS16(ws + OFF_MODA + lane * 16, modv);
  GLDS16(ws + OFF_MODA + 1024 + lane * 16, modv + 1024);
  u32x4 xr[29], sw[7][3];
#pragma unroll
  for (int c = 0; c < 7; ++c) {
#pragma unroll
    for (int i = 0; i < 3; ++i) gload16(sw[c][i], B + (size_t)(i * 28 + 4 * c) * 1024);
#pragma unroll
    for (int i = 0; i < 4; ++i) gload16a(xr[4 * c + i], A + (4 * c + i) * 1024);
  }
  f32x16 acc[3];
#pragma unroll
  for (int i = 0; i < 3; ++i)
#pragma unroll
    for (int r = 0; r < 16; ++r) acc[i][r] = 0.f;
  const unsigned wl0 = lds_addr(lds) + lane * 16, wl1 = wl0 + 49152;
  const unsigned sa0 = wl0 + w * 3072, sa1 = sa0 + 49152;
  const unsigned ml = lds_addr(modv) + w * 64 + hi * 32;
  u32x4 wb[3][3], mv[7][2];
  f32x2 scf[7][4];
  float bp[3] = {0.f, 0.f, 0.f};
  auto rdw = [&](auto kc) {
    constexpr int k = decltype(kc)::value;
    constexpr int o = (k & 15) * 3072;
    const unsigned a = (k >= 16) ? wl1 : wl0;
    lread16<o>(wb[k % 3][0], a); lread16<o + 1024>(wb[k % 3][1], a); lread16<o + 2048>(wb[k % 3][2], a);
  };
  auto rdm = [&](auto cc) {
    constexpr int c = decltype(cc)::value;
    lread16<c * 256>(mv[c][0], ml); lread16<c * 256 + 16>(mv[c][1], ml);
  };
  auto unp = [&](auto cc) {
    constexpr int c = decltype(cc)::value;
#pragma unroll
    for (int j = 0; j < 4; ++j) scf[c][j] = f32x2{bflo(mv[c][0][j]), bfhi(mv[c][0][j])};
  };
  auto word = [&](auto cc, auto qc) {
    constexpr int c = decltype(cc)::value, q = decltype(qc)::value, i = q >> 2, j = q & 3;
    const unsigned wv = sw[c][i][j];
    bp[i] = dot2(wv, mv[c][1][j], bp[i]);
    const f32x2 v = f32x2{bflo(wv), bfhi(wv)} * scf[c][j];
    sw[c][i][j] = pack2(v[0], v[1]);
    if constexpr (j == 3) lwrite16<(c & 3) * 12288 + i * 1024>(sw[c][i], c < 4 ? sa0 : sa1);
  };
  vwait<46>();
  rdyv(sw[0][0]); rdyv(sw[0][1]); rdyv(sw[0][2]);
  rdm(IC(0)); rdm(IC(1)); rdm(IC(2));
  lwait<0>();
  static_for<0, 3>([&](auto c) { rdyv(mv[c][0]); rdyv(mv[c][1]); });
  unp(IC(0)); unp(IC(1));
  static_for<0, 12>([&](auto q) { word(IC(0), q); });
  wait_bar<39>();
  STAMP(1);
  rdya(xr[0]); rdya(xr[1]); rdya(xr[2]); rdya(xr[3]);
  rdyv(sw[1][0]); rdyv(sw[1][1]); rdyv(sw[1][2]);
  rdw(IC(0)); rdw(IC(1));
  lwait<3>();
  rdyv(wb[0][0]); rdyv(wb[0][1]); rdyv(wb[0][2]);
  xr[28] = u32x4{0x3f803f80u, 0, 0, 0};
  SB();
  static_for<0, 29>([&](auto kc) {
    constexpr int k = decltype(kc)::value;
    constexpr int cn = (k < 2) ? 1 : (k + 6) / 4, r = (k + 6) % 4;
    constexpr bool PR = k < 22;
    auto gap = [&](auto gc) {
      constexpr int g = decltype(gc)::value;
      if constexpr (k < 2) { word(IC(1), IC(6 * k + 2 * g)); word(IC(1), IC(6 * k + 2 * g + 1)); }
      else if constexpr (PR) word(IC(cn), IC(3 * r + g));
    };
    acc[0] = MFMA32(BF8(wb[k % 3][0]), BF8(xr[k]), acc[0]);
    if constexpr ((k + 2) % 4 == 0) {
      constexpr int j = (k + 2) / 4;
      wait_bar<(j < 6) ? 4 + 7 * (5 - j) : 0>();
      if constexpr (j < 7) { rdya(xr[k + 2]); rdya(xr[k + 3]); rdya(xr[k + 4]); rdya(xr[k + 5]); }
      if constexpr (j < 6) { rdyv(sw[j + 1][0]); rdyv(sw[j + 1][1]); rdyv(sw[j + 1][2]); }
    }
    if constexpr (k >= 2 && r == 2 && cn + 1 < 7) rdm(IC(cn + 1));
    if constexpr (k + 2 < 29) rdw(IC(k + 2));
    gap(IC(0));
    SB();
    acc[1] = MFMA32(BF8(wb[k % 3][1]), BF8(xr[k]), acc[1]);
    gap(IC(1));
    if constexpr (k == 23) {
      if (w < 3) {
        float bs = P[(w * 4 + 0) * 64 + lane] + P[(w * 4 + 1) * 64 + lane] + P[(w * 4 + 2) * 64 + lane] + P[(w * 4 + 3) * 64 + lane];
        bs += __shfl_xor(bs, 32);
        const float bh = rbf(bs);
        *(u32x4*)(lds + (84 + w) * 1024 + lane * 16) = u32x4{hi ? 0u : pack2(bh, bs - bh), 0, 0, 0};
      }
    }
    SB();
    acc[2] = MFMA32(BF8(xr[k]), BF8(wb[k % 3][2]), acc[2]);
    gap(IC(2));
    if constexpr (r == 3 && cn + 1 < 7) unp(IC(cn + 1));
    if constexpr (k == 21) {
#pragma unroll
      for (int i = 0; i < 3; ++i) P[(i * 4 + w) * 64 + lane] = bp[i];
    }
    if constexpr (k + 1 < 29) {
      lwait<(k + 2 < 29) ? 3 : 0>();
      rdyv(wb[(k + 1) % 3][0]); rdyv(wb[(k + 1) % 3][1]); rdyv(wb[(k + 1) % 3][2]);
      if constexpr (k >= 2 && r == 2 && cn + 1 < 7) { rdyv(mv[cn + 1][0]); rdyv(mv[cn + 1][1]); }
    }
    SB();
  });
  STAMP(2);
  const float qs = 0.18898223650461362f * 1.4426950408889634f;
#pragma unroll
  for (int p = 0; p < 2; ++p) {
    char* base = ws + (p ? OFF_K : OFF_Q) + ((size_t)(head * 128 + mt * 2 + (l31 >> 4)) * 64 + (l31 & 15)) * 16;
    const float sc = p ? 1.f : qs;
#pragma unroll
    for (int s = 0; s < 2; ++s) {
      u32x4 o;
#pragma unroll
      for (int j = 0; j < 4; ++j) o[j] = pack2(acc[p][8 * s + 2 * j] * sc, acc[p][8 * s + 2 * j + 1] * sc);
      *(u32x4*)(base + (2 * s + hi) * 256) = o;
    }
  }
  {
    char* base = ws + OFF_V + ((size_t)((head * 64 + mt) * 2 + (l31 >> 4)) * 64 + (l31 & 15)) * 16;
#pragma unroll
    for (int s = 0; s < 2; ++s) {
      u32x4 o = {pack2(acc[2][4 * s], acc[2][4 * s + 1]), pack2(acc[2][4 * s + 2], acc[2][4 * s + 3]),
                 pack2(acc[2][8 + 4 * s], acc[2][8 + 4 * s + 1]), pack2(acc[2][8 + 4 * s + 2], acc[2][8 + 4 * s + 3])};
      if (l31 == 28) o = u32x4{0x3f803f80u, 0x3f803f80u, 0x3f803f80u, 0x3f803f80u};
      *(u32x4*)(base + (2 * s + hi) * 256) = o;
    }
  }
  PFLUSH(1);
}

template <bool ODD, bool EDGE>
DEVI void attn_wave(const char* ldsK, const char* ldsV, const char* ldsQ, int w, int lane, int nneg, char* odst, int kbase) {
  const int g = lane >> 4, qq = lane & 15;
  const int s0 = (w >> 1) * 2;
  const bf16x8 q = *(const bf16x8*)(ldsQ + w * 1024 + lane * 16);
  const float NEG = -1e30f;
  f32x4 mlo, mhi;
#pragma unroll
  for (int r = 0; r < 4; ++r) {
    mlo[r] = (4 * g + r > qq) ? 0.f : NEG;
    mhi[r] = (4 * g + r <= qq) ? 0.f : NEG;
  }
  f32x4 s[17];
  const char* kp = ldsK + (s0 + (ODD ? 1 : 0)) * 1024 + lane * 16;
#pragma unroll
  for (int tt = 0; tt < 17; ++tt) {
    f32x4 ci = (tt == 0) ? mlo : (tt == 16) ? mhi : f32x4{0.f, 0.f, 0.f, 0.f};
    if (EDGE) {
      if (w + tt < nneg) ci = f32x4{NEG, NEG, NEG, NEG};
    }
    const bf16x8 kf = *(const bf16x8*)(kp + tt * 1024);
    s[tt] = MFMA16(kf, q, ci);
  }
  float m = NEG;
#pragma unroll
  for (int tt = 0; tt < 17; ++tt) m = fmaxf(fmaxf(m, fmaxf(s[tt][0], s[tt][1])), fmaxf(s[tt][2], s[tt][3]));
  m = fmaxf(m, __shfl_xor(m, 16));
  m = fmaxf(m, __shfl_xor(m, 32));
  u32x2 pb[18];
  pb[ODD ? 0 : 17] = u32x2{0, 0};
#pragma unroll
  for (int tt = 0; tt < 17; ++tt) {
    const float e0 = __builtin_amdgcn_exp2f(s[tt][0] - m), e1 = __builtin_amdgcn_exp2f(s[tt][1] - m);
    const float e2 = __builtin_amdgcn_exp2f(s[tt][2] - m), e3 = __builtin_amdgcn_exp2f(s[tt][3] - m);
    pb[tt + (ODD ? 1 : 0)] = u32x2{pack2(e0, e1), pack2(e2, e3)};
  }
  wait_bar<0>();
  f32x4 o0 = {0.f, 0.f, 0.f, 0.f}, o1 = {0.f, 0.f, 0.f, 0.f};
  const char* vp = ldsV + (w >> 1) * 2048 + lane * 16;
#pragma unroll
  for (int v = 0; v < 9; ++v) {
    const u32x4 pw = {pb[2 * v][0], pb[2 * v][1], pb[2 * v + 1][0], pb[2 * v + 1][1]};
    const bf16x8 pf = __builtin_bit_cast(bf16x8, pw);
    const bf16x8 v0 = *(const bf16x8*)(vp + v * 2048);
    const bf16x8 v1 = *(const bf16x8*)(vp + v * 2048 + 1024);
    o0 = MFMA16(v0, pf, o0);
    o1 = MFMA16(v1, pf, o1);
  }
  const float sum = __shfl(o1[0], 48 + qq);
  const float inv = 1.f / sum;
  {
    const int k = kbase + 4 * g;
    u32x2 o = {pack2(o0[0] * inv, o0[1] * inv), pack2(o0[2] * inv, o0[3] * inv)};
    *(u32x2*)(odst + (size_t)(k >> 4) * 1024 + ((k >> 3) & 1) * 512 + (k & 4) * 2) = o;
  }
  if (g < 3) {
    const int k = kbase + 16 + 4 * g;
    u32x2 o = {pack2(o1[0] * inv, o1[1] * inv), pack2(o1[2] * inv, o1[3] * inv)};
    *(u32x2*)(odst + (size_t)(k >> 4) * 1024 + ((k >> 3) & 1) * 512 + (k & 4) * 2) = o;
  }
}

__global__ __launch_bounds__(512) void k2_attn(char* __restrict__ ws) {
  extern __shared__ __attribute__((aligned(16))) char lds[];
  const int t = threadIdx.x, lane = t & 63, w = __builtin_amdgcn_readfirstlane(t >> 6);
  PDECL; STAMP(0);
  const int xc = blockIdx.x & 7, bi = blockIdx.x >> 3;
  const int head = xc * 2 + (bi & 1), qb = bi >> 1;
  char* ldsK = lds;
  char* ldsV = lds + 24 * 1024;
  char* ldsQ = lds + 48 * 1024;
  const int sub0 = qb * 8 - 16;
  GLDS16(ws + OFF_Q + (size_t)(head * 128 + qb * 8 + w) * 1024 + lane * 16, ldsQ + w * 1024);
#pragma unroll
  for (int i = 0; i < 3; ++i) {
    const int p = w * 3 + i;
    const int sidx = max(sub0 + p, 0);
    GLDS16(ws + OFF_K + (size_t)(head * 128 + sidx) * 1024 + lane * 16, ldsK + p * 1024);
  }
#pragma unroll
  for (int i = 0; i < 3; ++i) {
    const int p = w * 3 + i;
    const int sidx = max(sub0 + p, 0);
    GLDS16(ws + OFF_V + (size_t)(head * 128 + sidx) * 1024 + lane * 16, ldsV + p * 1024);
  }
  wait_bar<3>();
  STAMP(1);
  const int tok = qb * 128 + w * 16 + (lane & 15);
  char* odst = ws + OFF_O + (size_t)(tok >> 5) * 28 * 1024 + (tok & 31) * 16;
  const int kbase = head * 28;
  const int nneg = -sub0;
  if (qb >= 2) {
    if (w & 1) attn_wave<true, false>(ldsK, ldsV, ldsQ, w, lane, nneg, odst, kbase);
    else attn_wave<false, false>(ldsK, ldsV, ldsQ, w, lane, nneg, odst, kbase);
  } else {
    if (w & 1) attn_wave<true, true>(ldsK, ldsV, ldsQ, w, lane, nneg, odst, kbase);
    else attn_wave<false, true>(ldsK, ldsV, ldsQ, w, lane, nneg, odst, kbase);
  }
  PFLUSH(2);
}

DEVI f32x16 ksplit_reduce(f32x16 (&acc)[4], char* lds, int w, int lane) {
#pragma unroll
  for (int tl = 0; tl < 4; ++tl) {
    if (tl != w) {
      const int slot = (w > tl) ? w - 1 : w;
      char* p = lds + (tl * 3 + slot) * 4096 + lane * 16;
#pragma unroll
      for (int q = 0; q < 4; ++q)
        *(f32x4*)(p + q * 1024) = f32x4{acc[tl][4 * q], acc[tl][4 * q + 1], acc[tl][4 * q + 2], acc[tl][4 * q + 3]};
    }
  }
  f32x16 r = (w == 0) ? acc[0] : (w == 1) ? acc[1] : (w == 2) ? acc[2] : acc[3];
  __syncthreads();
#pragma unroll
  for (int s = 0; s < 3; ++s) {
    const char* p = lds + (w * 3 + s) * 4096 + lane * 16;
#pragma unroll
    for (int q = 0; q < 4; ++q) {
      const f32x4 v = *(const f32x4*)(p + q * 1024);
#pragma unroll
      for (int j = 0; j < 4; ++j) r[4 * q + j] += v[j];
    }
  }
  return r;
}

__global__ __launch_bounds__(256) __attribute__((amdgpu_waves_per_eu(1, 1))) void k3_wo(const uint16_t* __restrict__ x,
                                                                                        char* __restrict__ ws) {
  __shared__ __attribute__((aligned(16))) char lds[48 * 1024];
  const int t = threadIdx.x, lane = t & 63, w = __builtin_amdgcn_readfirstlane(t >> 6);
  PDECL; STAMP(0);
  const int xc = blockIdx.x & 7, bi = blockIdx.x >> 3;
  const int mb = xc * 4 + (bi & 3), nb = bi >> 2;
  const char* A = ws + OFF_O + ((size_t)mb * 2 * 28 + 7 * w) * 1024 + lane * 16;
  const char* B = ws + OFF_WO + ((size_t)nb * 2 * 28 + 7 * w) * 1024 + lane * 16;
  const int wm = w & 1, wn = w >> 1;
  const int hi = lane >> 5, l31 = lane & 31;
  const int mt = mb * 2 + wm, nt = nb * 2 + wn;
  bf16x8 a[7][2], bw[7][2];
#pragma unroll
  for (int k = 0; k < 7; ++k) {
    a[k][0] = *(const bf16x8*)(A + k * 1024);
    bw[k][0] = *(const bf16x8*)(B + k * 1024);
    a[k][1] = *(const bf16x8*)(A + (28 + k) * 1024);
    bw[k][1] = *(const bf16x8*)(B + (28 + k) * 1024);
  }
  u32x4 xv[2];
  f32x4 gv[4];
  {
    const uint16_t* xs = x + (size_t)(mt * 32 + l31) * 448 + nt * 32 + hi * 8;
    xv[0] = *(const u32x4*)xs;
    xv[1] = *(const u32x4*)(xs + 16);
    const float* gp = (const float*)(ws + OFF_GAB) + nt * 32 + hi * 8;
    gv[0] = *(const f32x4*)gp; gv[1] = *(const f32x4*)(gp + 4);
    gv[2] = *(const f32x4*)(gp + 16); gv[3] = *(const f32x4*)(gp + 20);
  }
  __builtin_amdgcn_sched_barrier(0);
  f32x16 acc[4];
#pragma unroll
  for (int i = 0; i < 4; ++i)
#pragma unroll
    for (int r = 0; r < 16; ++r) acc[i][r] = 0.f;
#pragma unroll
  for (int k = 0; k < 7; ++k) {
    acc[0] = MFMA32(bw[k][0], a[k][0], acc[0]);
    acc[1] = MFMA32(bw[k][0], a[k][1], acc[1]);
    acc[2] = MFMA32(bw[k][1], a[k][0], acc[2]);
    acc[3] = MFMA32(bw[k][1], a[k][1], acc[3]);
  }
  STAMP(1);
  const f32x16 r = ksplit_reduce(acc, lds, w, lane);
  STAMP(2);
  float v[16];
#pragma unroll
  for (int s = 0; s < 2; ++s) {
    u32x4 o;
#pragma unroll
    for (int j = 0; j < 4; ++j) {
      const float ga0 = gv[2 * s + (j >> 1)][(2 * j) & 3], ga1 = gv[2 * s + (j >> 1)][(2 * j + 1) & 3];
      o[j] = pack2(bflo(xv[s][j]) + ga0 * r[8 * s + 2 * j], bfhi(xv[s][j]) + ga1 * r[8 * s + 2 * j + 1]);
      v[8 * s + 2 * j] = bflo(o[j]);
      v[8 * s + 2 * j + 1] = bfhi(o[j]);
    }
    *(u32x4*)(ws + OFF_X1 + ((size_t)(mt * 28 + nt * 2 + s) * 64 + lane) * 16) = o;
  }
  float sm = 0.f;
#pragma unroll
  for (int j = 0; j < 16; ++j) sm += v[j];
  sm += __shfl_xor(sm, 32);
  const float mean = sm * (1.f / 32.f);
  float m2 = 0.f;
#pragma unroll
  for (int j = 0; j < 16; ++j) { const float d = v[j] - mean; m2 += d * d; }
  m2 += __shfl_xor(m2, 32);
  if (hi == 0) *(f32x2*)(ws + OFF_STATS + ((size_t)(mt * 16 + nt) * 32 + l31) * 8) = f32x2{mean, m2};
  PFLUSH(3);
}

__global__ __launch_bounds__(256) __attribute__((amdgpu_waves_per_eu(1, 1))) void k4_gu(char* __restrict__ ws) {
  extern __shared__ __attribute__((aligned(16))) char lds[];
  const int t = threadIdx.x, lane = t & 63, w = __builtin_amdgcn_readfirstlane(t >> 6);
  PDECL; STAMP(0);
  const int xc = blockIdx.x & 7, bi = blockIdx.x >> 3;
  const int mb = (xc & 1) * 4 + (bi & 3), fb = (xc >> 1) * 7 + (bi >> 2);
  const int mt = mb * 8 + 2 * w;
  const char* A = ws + OFF_X1 + (size_t)mt * 28 * 1024 + lane * 16;
  const char* B = ws + OFF_WGU + ((size_t)fb * 4 * 28 + w) * 1024 + lane * 16;
  const int hi = lane >> 5, l31 = lane & 31;
  char* modv = lds + 116 * 1024 + w * 2048;
  char* auxS = lds + w * 8192;
  f32x2* P = (f32x2*)(lds + 32 * 1024);
  GLDS16(ws + OFF_MODB + lane * 16, modv);
  GLDS16(ws + OFF_MODB + 1024 + lane * 16, modv + 1024);
  u32x4 xr[29][2], sw[7][4];
  auto issW = [&](auto cc) {
    constexpr int c = decltype(cc)::value;
#pragma unroll
    for (int i = 0; i < 4; ++i) gload16(sw[c][i], B + (size_t)(i * 28 + 4 * c) * 1024);
  };
  auto issX = [&](auto cc) {
    constexpr int c = decltype(cc)::value;
#pragma unroll
    for (int i = 0; i < 4; ++i) {
      gload16a(xr[4 * c + i][0], A + (4 * c + i) * 1024);
      gload16a(xr[4 * c + i][1], A + (28 + 4 * c + i) * 1024);
    }
  };
  static_for<0, 3>([&](auto cc) { issW(cc); issX(cc); });
  static_for<3, 7>(issW);
  f32x16 acc[2][4];
#pragma unroll
  for (int i = 0; i < 8; ++i)
#pragma unroll
    for (int r = 0; r < 16; ++r) acc[i >> 2][i & 3][r] = 0.f;
  const unsigned wa0 = lds_addr(lds) + lane * 16, wa1 = wa0 + 65536;
  const unsigned sa0 = wa0 + w * 4096, sa1 = sa0 + 65536;
  const unsigned ml = lds_addr(modv) + w * 64 + hi * 32;
  u32x4 wb[3][4], mv[7][2];
  f32x2 scf[7][4];
  float bp[4] = {0.f, 0.f, 0.f, 0.f}, cp[4] = {0.f, 0.f, 0.f, 0.f};
  float rstd[2];
  auto rdw = [&](auto kc) {
    constexpr int k = decltype(kc)::value;
    constexpr int o = (k & 15) * 4096;
    const unsigned a = (k >= 16) ? wa1 : wa0;
    lread16<o>(wb[k % 3][0], a); lread16<o + 1024>(wb[k % 3][1], a);
    lread16<o + 2048>(wb[k % 3][2], a); lread16<o + 3072>(wb[k % 3][3], a);
  };
  auto rdm = [&](auto cc) {
    constexpr int c = decltype(cc)::value;
    lread16<c * 256>(mv[c][0], ml); lread16<c * 256 + 16>(mv[c][1], ml);
  };
  auto unp = [&](auto cc) {
    constexpr int c = decltype(cc)::value;
#pragma unroll
    for (int j = 0; j < 4; ++j) scf[c][j] = f32x2{bflo(mv[c][0][j]), bfhi(mv[c][0][j])};
  };
  auto word = [&](auto cc, auto qc) {
    constexpr int c = decltype(cc)::value, q = decltype(qc)::value, i = q >> 2, j = q & 3;
    const unsigned wv = sw[c][i][j];
    bp[i] = dot2(wv, mv[c][1][j], bp[i]);
    const f32x2 v = f32x2{bflo(wv), bfhi(wv)} * scf[c][j];
    const unsigned o = pack2(v[0], v[1]);
    cp[i] = dot2(o, 0x3f803f80u, cp[i]);
    sw[c][i][j] = o;
    if constexpr (j == 3) lwrite16<(c & 3) * 16384 + i * 1024>(sw[c][i], c < 4 ? sa0 : sa1);
  };
  auto rdyW = [&](auto cc) {
    constexpr int c = decltype(cc)::value;
    rdyv(sw[c][0]); rdyv(sw[c][1]); rdyv(sw[c][2]); rdyv(sw[c][3]);
  };
  auto rdyX = [&](auto cc) {
    constexpr int c = decltype(cc)::value;
#pragma unroll
    for (int i = 0; i < 4; ++i) { rdya(xr[4 * c + i][0]); rdya(xr[4 * c + i][1]); }
  };
  vwait<48>();
  rdyW(IC(0));
  rdm(IC(0)); rdm(IC(1)); rdm(IC(2));
  lwait<0>();
  static_for<0, 3>([&](auto c) { rdyv(mv[c][0]); rdyv(mv[c][1]); });
  unp(IC(0)); unp(IC(1));
  static_for<0, 16>([&](auto q) { word(IC(0), q); });
  wait_bar<36>();
  STAMP(1);
  rdyX(IC(0)); rdyW(IC(1));
  rdw(IC(0)); rdw(IC(1));
  lwait<4>();
  rdyv(wb[0][0]); rdyv(wb[0][1]); rdyv(wb[0][2]); rdyv(wb[0][3]);
  SB();
  static_for<0, 29>([&](auto kc) {
    constexpr int k = decltype(kc)::value;
    constexpr int cn = (k < 2) ? 1 : (k + 6) / 4, r = (k + 6) % 4;
    constexpr bool PR = k < 22;
    static_for<0, 8>([&](auto gc) {
      constexpr int g = decltype(gc)::value;
      acc[g & 1][g >> 1] = MFMA32(BF8(wb[k % 3][g >> 1]), BF8(xr[k][g & 1]), acc[g & 1][g >> 1]);
      if constexpr (g == 0) {
        if constexpr ((k + 2) % 4 == 0) {
          constexpr int j = (k + 2) / 4;
          constexpr int NV[8] = {0, 24, 20, 8, 8, 16, 8, 0};
          wait_bar<NV[j]>();
          if constexpr (j < 7) rdyX(IC(j < 7 ? j : 0));
          if constexpr (j < 6) rdyW(IC(j < 6 ? j + 1 : 0));
        }
        if constexpr (k % 4 == 0 && k >= 4 && k <= 16) issX(IC(k / 4 + 2));
        if constexpr (k == 16) {
          const char* ss = ws + OFF_STATS + (size_t)mt * 4096 + lane * 16;
#pragma unroll
          for (int i = 0; i < 8; ++i) GLDS16(ss + i * 1024, auxS + i * 1024);
        }
        if constexpr (k >= 2 && r == 2 && cn + 1 < 7) rdm(IC(cn + 1));
        if constexpr (k + 2 < 29) rdw(IC(k + 2));
      }
      if constexpr (k < 2) word(IC(1), IC(8 * k + g));
      else if constexpr (PR && g >= 1 && g <= 4) word(IC(cn), IC(4 * r + g - 1));
      if constexpr (g == 6 && r == 3 && cn + 1 < 7) unp(IC(cn + 1));
      if constexpr (k == 21 && g == 5) {
#pragma unroll
        for (int i = 0; i < 4; ++i) P[(i * 4 + w) * 64 + lane] = f32x2{cp[i], bp[i]};
      }
      if constexpr (k == 23 && g == 1) {
        f32x2 v = P[(w * 4 + 0) * 64 + lane] + P[(w * 4 + 1) * 64 + lane] + P[(w * 4 + 2) * 64 + lane] + P[(w * 4 + 3) * 64 + lane];
        v[0] += __shfl_xor(v[0], 32);
        v[1] += __shfl_xor(v[1], 32);
        const float ch = rbf(v[0]), cl = v[0] - ch, bh = rbf(v[1]), bl = v[1] - bh;
        u32x4 f = {pack2(ch, ch), pack2(cl, cl), pack2(bh, bh), pack2(bl, bl)};
        if (hi) f = u32x4{0, 0, 0, 0};
        *(u32x4*)(lds + (112 + w) * 1024 + lane * 16) = f;
      }
      if constexpr ((k == 26 || k == 27) && g == 1) {
        constexpr int tt = k - 26;
        float mu = 0.f, M2 = 0.f;
        f32x2 p[14];
#pragma unroll
        for (int i = 0; i < 14; ++i) { p[i] = *(const f32x2*)(auxS + tt * 4096 + (i * 32 + l31) * 8); mu += p[i][0]; M2 += p[i][1]; }
        mu *= (1.f / 14.f);
#pragma unroll
        for (int i = 0; i < 14; ++i) { const float d = p[i][0] - mu; M2 += 32.f * d * d; }
        const float var = M2 * (1.f / 448.f) + 1e-5f;
        rstd[tt] = rsqrtf(var);
        const float sg = var * rstd[tt];
        const float mh = rbf(mu), sh = rbf(sg);
        const unsigned pm = pack2(-mh, mh - mu), ps = pack2(sh, sg - sh);
        xr[28][tt] = u32x4{pm, pm, ps, ps};
      }
      if constexpr (g == 7 && k + 1 < 29) {
        lwait<(k + 2 < 29) ? 4 : 0>();
        rdyv(wb[(k + 1) % 3][0]); rdyv(wb[(k + 1) % 3][1]); rdyv(wb[(k + 1) % 3][2]); rdyv(wb[(k + 1) % 3][3]);
        if constexpr (k >= 2 && r == 2 && cn + 1 < 7) { rdyv(mv[cn + 1][0]); rdyv(mv[cn + 1][1]); }
      }
      SB();
    });
  });
  STAMP(2);
  const float c0 = -2.f * 0.7978845608028654f * 1.4426950408889634f, c1 = c0 * 0.044715f;
  char* hdst = ws + OFF_HFF + ((size_t)(mt * 112 + fb * 4) * 64 + lane) * 16;
#pragma unroll
  for (int tt = 0; tt < 2; ++tt)
#pragma unroll
    for (int n = 0; n < 4; ++n) {
      float hh[8];
#pragma unroll
      for (int j = 0; j < 8; ++j) {
        const float g = acc[tt][n][j] * rstd[tt], u = acc[tt][n][8 + j] * rstd[tt];
        const float e = __builtin_amdgcn_exp2f(g * fmaf(g * g, c1, c0));
        hh[j] = g * u * __builtin_amdgcn_rcpf(1.f + e);
      }
      u32x4 o = {pack2(hh[0], hh[1]), pack2(hh[2], hh[3]), pack2(hh[4], hh[5]), pack2(hh[6], hh[7])};
      *(u32x4*)(hdst + (tt * 112 + n) * 1024) = o;
    }
  PFLUSH(4);
}

__global__ __launch_bounds__(256) __attribute__((amdgpu_waves_per_eu(1, 1))) void k5_down(char* __restrict__ ws,
                                                                                          uint16_t* __restrict__ out) {
  __shared__ __attribute__((aligned(16))) char lds[48 * 1024];
  const int t = threadIdx.x, lane = t & 63, w = __builtin_amdgcn_readfirstlane(t >> 6);
  PDECL; STAMP(0);
  const int xc = blockIdx.x & 7, bi = blockIdx.x >> 3;
  const int mb = xc * 4 + (bi & 3), nb = bi >> 2;
  const char* A = ws + OFF_HFF + ((size_t)mb * 2 * 112 + 28 * w) * 1024 + lane * 16;
  const char* B = ws + OFF_WD + ((size_t)nb * 2 * 112 + 28 * w) * 1024 + lane * 16;
  const int wm = w & 1, wn = w >> 1;
  const int hi = lane >> 5, l31 = lane & 31;
  const int mt = mb * 2 + wm, nt = nb * 2 + wn;
  constexpr int D = 12;
  bf16x8 a[D][2], bw[D][2];
  auto ld = [&](int k) {
    a[k % D][0] = *(const bf16x8*)(A + k * 1024);
    bw[k % D][0] = *(const bf16x8*)(B + k * 1024);
    a[k % D][1] = *(const bf16x8*)(A + (112 + k) * 1024);
    bw[k % D][1] = *(const bf16x8*)(B + (112 + k) * 1024);
  };
#pragma unroll
  for (int k = 0; k < D; ++k) ld(k);
  u32x4 xv[2];
  f32x4 gv[4];
  f32x16 acc[4];
#pragma unroll
  for (int i = 0; i < 4; ++i)
#pragma unroll
    for (int r = 0; r < 16; ++r) acc[i][r] = 0.f;
  __builtin_amdgcn_sched_barrier(0);
#pragma unroll
  for (int k = 0; k < 28; ++k) {
    acc[0] = MFMA32(bw[k % D][0], a[k % D][0], acc[0]);
    acc[1] = MFMA32(bw[k % D][0], a[k % D][1], acc[1]);
    acc[2] = MFMA32(bw[k % D][1], a[k % D][0], acc[2]);
    acc[3] = MFMA32(bw[k % D][1], a[k % D][1], acc[3]);
    if (k + D < 28) ld(k + D);
    if (k + D == 28) {
      const char* xs = ws + OFF_X1 + ((size_t)(mt * 28 + nt * 2) * 64 + lane) * 16;
      xv[0] = *(const u32x4*)xs;
      xv[1] = *(const u32x4*)(xs + 1024);
      const float* gp = (const float*)(ws + OFF_GAB) + 448 + nt * 32 + hi * 8;
      gv[0] = *(const f32x4*)gp; gv[1] = *(const f32x4*)(gp + 4);
      gv[2] = *(const f32x4*)(gp + 16); gv[3] = *(const f32x4*)(gp + 20);
    }
    __builtin_amdgcn_sched_barrier(0);
  }
  STAMP(1);
  const f32x16 r = ksplit_reduce(acc, lds, w, lane);
  STAMP(2);
#pragma unroll
  for (int s = 0; s < 2; ++s) {
    u32x4 o;
#pragma unroll
    for (int j = 0; j < 4; ++j) {
      const float ga0 = gv[2 * s + (j >> 1)][(2 * j) & 3], ga1 = gv[2 * s + (j >> 1)][(2 * j + 1) & 3];
      o[j] = pack2(bflo(xv[s][j]) + ga0 * r[8 * s + 2 * j], bfhi(xv[s][j]) + ga1 * r[8 * s + 2 * j + 1]);
    }
    *(u32x4*)(out + (size_t)(mt * 32 + l31) * 448 + nt * 32 + s * 16 + hi * 8) = o;
  }
  PFLUSH(5);
}

constexpr int LDS_K1 = 98 * 1024;
constexpr int LDS_K2 = 56 * 1024;
constexpr int LDS_K4 = 124 * 1024;

extern "C" void kernel_launch(void* const* d_in, const int* in_sizes, int n_in, void* d_out, int out_size, void* d_ws,
                              size_t ws_size, hipStream_t stream) {
  const uint16_t* x = (const uint16_t*)d_in[0];
  const uint16_t* wqkv = (const uint16_t*)d_in[1];
  const uint16_t* wo = (const uint16_t*)d_in[2];
  const uint16_t* wg = (const uint16_t*)d_in[4];
  const uint16_t* wu = (const uint16_t*)d_in[5];
  const uint16_t* wd = (const uint16_t*)d_in[6];
  const uint16_t* cv = (const uint16_t*)d_in[7];
  const uint16_t* wmod = (const uint16_t*)d_in[8];
  char* ws = (char*)d_ws;
  (void)hipFuncSetAttribute((const void*)k1_qkv, hipFuncAttributeMaxDynamicSharedMemorySize, LDS_K1);
  (void)hipFuncSetAttribute((const void*)k2_attn, hipFuncAttributeMaxDynamicSharedMemorySize, LDS_K2);
  (void)hipFuncSetAttribute((const void*)k4_gu, hipFuncAttributeMaxDynamicSharedMemorySize, LDS_K4);
  k0_prep<<<330, 256, 0, stream>>>(x, wqkv, wo, wg, wu, wd, cv, wmod, ws);
  k1_qkv<<<256, 256, LDS_K1, stream>>>(ws);
  k2_attn<<<256, 512, LDS_K2, stream>>>(ws);
  k3_wo<<<224, 256, 0, stream>>>(x, ws);
  k4_gu<<<224, 256, LDS_K4, stream>>>(ws);
  k5_down<<<224, 256, 0, stream>>>(ws, (uint16_t*)d_out);
}
```

```cpp
#include <hip/hip_runtime.h>
#include <stdint.h>

typedef __attribute__((ext_vector_type(8))) short bf16x8;
typedef __attribute__((ext_vector_type(16))) float f32x16;
typedef __attribute__((ext_vector_type(4))) float f32x4;
typedef __attribute__((ext_vector_type(2))) float f32x2;
typedef __attribute__((ext_vector_type(2))) __bf16 bf16x2_t;
typedef __attribute__((ext_vector_type(4))) unsigned u32x4;
typedef __attribute__((ext_vector_type(2))) unsigned u32x2;

#define DEVI __device__ __forceinline__

DEVI unsigned pack2(float a, float b) {
  f32x2 v = {a, b};
  bf16x2_t r = __builtin_convertvector(v, bf16x2_t);
  return __builtin_bit_cast(unsigned, r);
}
DEVI float bflo(unsigned w) { return __builtin_bit_cast(float, w << 16); }
DEVI float bfhi(unsigned w) { return __builtin_bit_cast(float, w & 0xffff0000u); }
DEVI float rbf(float a) { return bflo(pack2(a, a)); }
DEVI int swap23(int d) { return (d & ~12) | ((d & 4) << 1) | ((d & 8) >> 1); }

constexpr size_t OFF_XN = 0;
constexpr size_t OFF_WQKV = OFF_XN + 64 * 28 * 1024;
constexpr size_t OFF_WO = OFF_WQKV + 48 * 28 * 1024;
constexpr size_t OFF_WGU = OFF_WO + 14 * 28 * 1024;
constexpr size_t OFF_WD = OFF_WGU + 112 * 29 * 1024;
constexpr size_t OFF_Q = OFF_WD + 14 * 112 * 1024;
constexpr size_t OFF_K = OFF_Q + 16 * 128 * 1024;
constexpr size_t OFF_V = OFF_K + 16 * 128 * 1024;
constexpr size_t OFF_O = OFF_V + 16 * 128 * 1024;
constexpr size_t OFF_X1 = OFF_O + 64 * 28 * 1024;
constexpr size_t OFF_HFF = OFF_X1 + 64 * 28 * 1024;
constexpr size_t OFF_STATS = OFF_HFF + 64 * 112 * 1024;
constexpr size_t OFF_MODA = OFF_STATS + 64 * 4096;
constexpr size_t OFF_MODB = OFF_MODA + 4096;
constexpr size_t OFF_GAB = OFF_MODB + 4096;
constexpr size_t WS_END = OFF_GAB + 4096;

#define PDECL
#define STAMP(i)
#define PFLUSH(kid)
#define PDECL2
#define BST(i)
#define PFLUSH2(kid)
#define GLDS16(src, dst) __builtin_amdgcn_global_load_lds((const unsigned*)(src), (unsigned*)(dst), 16, 0, 0)
#define GLDS4(src, dst) __builtin_amdgcn_global_load_lds((const unsigned*)(src), (unsigned*)(dst), 4, 0, 0)

template <int N>
DEVI void wait_bar() {
  asm volatile("s_waitcnt vmcnt(%0) lgkmcnt(0)\n\ts_barrier" ::"n"(N) : "memory");
}
template <int N>
DEVI void wait_bar4(u32x4& a, u32x4& b, u32x4& c, u32x4& d) {
  asm volatile("s_waitcnt vmcnt(%4) lgkmcnt(0)\n\ts_barrier" : "+v"(a), "+v"(b), "+v"(c), "+v"(d) : "n"(N) : "memory");
}
DEVI void gload16(u32x4& r, const char* p) {
  asm volatile("global_load_dwordx4 %0, %1, off" : "=v"(r) : "v"(p) : "memory");
}

typedef __attribute__((address_space(3))) char lds_char;
DEVI unsigned lds_addr(const char* p) { return (unsigned)(size_t)(lds_char*)p; }
template <int OFF>
DEVI void lread16(u32x4& r, unsigned a) {
  asm volatile("ds_read_b128 %0, %1 offset:%2" : "=v"(r) : "v"(a), "n"(OFF) : "memory");
}
template <int OFF>
DEVI void lwrite16(const u32x4& r, unsigned a) {
  asm volatile("ds_write_b128 %1, %0 offset:%2" ::"v"(r), "v"(a), "n"(OFF) : "memory");
}
template <int N>
DEVI void lwait() { asm volatile("s_waitcnt lgkmcnt(%0)" ::"n"(N) : "memory"); }
template <int N>
DEVI void vwait() { asm volatile("s_waitcnt vmcnt(%0)" ::"n"(N) : "memory"); }
DEVI void rdyv(u32x4& a) { asm volatile("" : "+v"(a)); }
DEVI void rdya(u32x4& a) { asm volatile("" : "+a"(a)); }
DEVI void gload16a(u32x4& r, const char* p) {
  asm volatile("global_load_dwordx4 %0, %1, off" : "=a"(r) : "v"(p) : "memory");
}
DEVI float dot2(unsigned a, unsigned b, float c) {
  return __builtin_amdgcn_fdot2_f32_bf16(__builtin_bit_cast(bf16x2_t, a), __builtin_bit_cast(bf16x2_t, b), c, false);
}
DEVI float fmul1(float a, float b) { float r; asm("v_mul_f32 %0, %1, %2" : "=v"(r) : "v"(a), "v"(b)); return r; }
DEVI float fadd1(float a, float b) { float r; asm("v_add_f32 %0, %1, %2" : "=v"(r) : "v"(a), "v"(b)); return r; }
DEVI float fsub1(float a, float b) { float r; asm("v_sub_f32 %0, %1, %2" : "=v"(r) : "v"(a), "v"(b)); return r; }
DEVI void fmac1(float& c, float a, float b) { asm("v_fmac_f32 %0, %1, %2" : "+v"(c) : "v"(a), "v"(b)); }
template <int NV, int NL>
DEVI void vlwait() { asm volatile("s_waitcnt vmcnt(%0) lgkmcnt(%1)" ::"n"(NV), "n"(NL) : "memory"); }
template <int N>
DEVI void vbar() { asm volatile("s_waitcnt vmcnt(%0)\n\ts_barrier" ::"n"(N) : "memory"); }
DEVI float xhalf_sum(float v) {
  const unsigned u = __builtin_bit_cast(unsigned, v);
  const f32x2 r = __builtin_bit_cast(f32x2, __builtin_amdgcn_permlane32_swap(u, u, false, false));
  return fadd1(r[0], r[1]);
}
#define F4(x) __builtin_bit_cast(f32x4, x)
#define SB() __builtin_amdgcn_sched_barrier(0)
#define BF8(x) __builtin_bit_cast(bf16x8, x)
#define IC(v) std::integral_constant<int, (v)>{}

template <int I, int N, class F>
DEVI void static_for(F&& f) {
  if constexpr (I < N) {
    f(std::integral_constant<int, I>{});
    static_for<I + 1, N>(f);
  }
}

#define MFMA32(a, b, c) __builtin_amdgcn_mfma_f32_32x32x16_bf16(a, b, c, 0, 0, 0)
#define MFMA16(a, b, c) __builtin_amdgcn_mfma_f32_16x16x32_bf16(a, b, c, 0, 0, 0)

DEVI void mod_unit(const uint16_t* __restrict__ cvec, const uint16_t* __restrict__ wmod, char* __restrict__ ws, int col0,
                   int t, float* sm) {
  const int lane = t & 63, w = t >> 6, q4 = t & 3, rs = t >> 2;
  const uint16_t* mp = wmod + (size_t)rs * 2688 + col0 + q4 * 8;
  u32x4 mv[7];
  float cs[7];
#pragma unroll
  for (int i = 0; i < 7; ++i) mv[i] = *(const u32x4*)(mp + (size_t)i * 64 * 2688);
#pragma unroll
  for (int i = 0; i < 7; ++i) {
    float cv = bflo((unsigned)cvec[rs + 64 * i]);
    cs[i] = rbf(cv / (1.f + __expf(-cv)));
  }
  float acc[8];
#pragma unroll
  for (int j = 0; j < 8; ++j) acc[j] = 0.f;
#pragma unroll
  for (int i = 0; i < 7; ++i)
#pragma unroll
    for (int j = 0; j < 4; ++j) {
      acc[2 * j] += cs[i] * bflo(mv[i][j]);
      acc[2 * j + 1] += cs[i] * bfhi(mv[i][j]);
    }
#pragma unroll
  for (int j = 0; j < 8; ++j) {
    acc[j] += __shfl_xor(acc[j], 4);
    acc[j] += __shfl_xor(acc[j], 8);
    acc[j] += __shfl_xor(acc[j], 16);
    acc[j] += __shfl_xor(acc[j], 32);
  }
  if (lane < 4) {
#pragma unroll
    for (int j = 0; j < 8; ++j) sm[w * 32 + lane * 8 + j] = acc[j];
  }
  __syncthreads();
  if (t < 32) {
    const float m = rbf(sm[t] + sm[32 + t] + sm[64 + t] + sm[96 + t]);
    const int part = col0 / 448, k = col0 % 448 + t;
    if (part == 2 || part == 5) {
      ((float*)(ws + OFF_GAB))[(part == 5 ? 448 : 0) + k] = m;
    } else {
      float* dst = (float*)(ws + (part >= 3 ? OFF_MODB : OFF_MODA)) + (k >> 3) * 16 + (k & 7);
      if (part == 1 || part == 4) dst[0] = rbf(1.f + m);
      else dst[8] = m;
    }
  }
}

DEVI void retile_unit(const uint16_t* __restrict__ wsrc, char* __restrict__ ws, int role, int kr, int sub, int t) {
  const int k0 = kr * 32;
  const int koct = t / 56, coct = t % 56;
  const int ldw = role == 0 ? 1344 : 448, c0 = role == 0 ? sub * 448 : 0;
  if (t < 224) {
    u32x4 wv[8];
    const uint16_t* p = wsrc + (size_t)(k0 + koct * 8) * ldw + c0 + coct * 8;
#pragma unroll
    for (int i = 0; i < 8; ++i) wv[i] = *(const u32x4*)(p + (size_t)i * ldw);
    const int ksl = koct >> 1, half = koct & 1;
#pragma unroll
    for (int c = 0; c < 8; ++c) {
      u32x4 o;
#pragma unroll
      for (int i = 0; i < 4; ++i)
        o[i] = __builtin_amdgcn_perm(wv[2 * i + 1][c >> 1], wv[2 * i][c >> 1], (c & 1) ? 0x07060302u : 0x05040100u);
      const int n = coct * 8 + c;
      char* dst;
      if (role == 0) {
        const int head = n / 28, d = n % 28;
        const int idx = (sub < 2) ? swap23(d) : d;
        dst = ws + OFF_WQKV + ((size_t)((head * 3 + sub) * 28 + 2 * kr + ksl) * 64 + half * 32 + idx) * 16;
      } else if (role == 2) {
        dst = ws + OFF_WO + ((size_t)((n >> 5) * 28 + 2 * kr + ksl) * 64 + half * 32 + swap23(n & 31)) * 16;
      } else {
        dst = ws + OFF_WD + ((size_t)((n >> 5) * 112 + 2 * kr + ksl) * 64 + half * 32 + swap23(n & 31)) * 16;
      }
      *(u32x4*)dst = o;
    }
  } else if (role == 0) {
    const int z = t - 224;
#pragma unroll
    for (int i = 0; i < 8; ++i) {
      const int pc = z * 8 + i, head = pc >> 4, d = 28 + ((pc >> 2) & 3), ko = pc & 3;
      const int idx = (sub < 2) ? swap23(d) : d;
      char* dst = ws + OFF_WQKV + ((size_t)((head * 3 + sub) * 28 + 2 * kr + (ko >> 1)) * 64 + (ko & 1) * 32 + idx) * 16;
      *(u32x4*)dst = u32x4{0, 0, 0, 0};
    }
  }
}

DEVI void fold_unit(const uint16_t* __restrict__ wsrc, char* __restrict__ ws, int mat, int cg, int t, char* red) {
  const int oct = t >> 2, c4 = t & 3;
  if (oct < 56) {
    u32x4 wv[8];
    const uint16_t* p = wsrc + (size_t)(oct * 8) * 1792 + cg * 32 + c4 * 8;
#pragma unroll
    for (int i = 0; i < 8; ++i) wv[i] = *(const u32x4*)(p + (size_t)i * 1792);
    const f32x4* tp = (const f32x4*)(ws + OFF_MODB + oct * 64);
    const f32x4 t0 = tp[0], t1 = tp[1], t2 = tp[2], t3 = tp[3];
    const float sc[8] = {t0[0], t0[1], t0[2], t0[3], t1[0], t1[1], t1[2], t1[3]};
    const float sh[8] = {t2[0], t2[1], t2[2], t2[3], t3[0], t3[1], t3[2], t3[3]};
    f32x2 pr[8];
#pragma unroll
    for (int c = 0; c < 8; ++c) {
      u32x4 o;
      float bs = 0.f, cs = 0.f;
#pragma unroll
      for (int i = 0; i < 4; ++i) {
        const float a = (c & 1) ? bfhi(wv[2 * i][c >> 1]) : bflo(wv[2 * i][c >> 1]);
        const float b = (c & 1) ? bfhi(wv[2 * i + 1][c >> 1]) : bflo(wv[2 * i + 1][c >> 1]);
        bs += a * sh[2 * i] + b * sh[2 * i + 1];
        o[i] = pack2(a * sc[2 * i], b * sc[2 * i + 1]);
        cs += bflo(o[i]) + bfhi(o[i]);
      }
      pr[c] = f32x2{cs, bs};
      const int f = cg * 32 + c4 * 8 + c;
      *(u32x4*)(ws + OFF_WGU + ((size_t)((f >> 4) * 29 + (oct >> 1)) * 64 + (oct & 1) * 32 + mat * 16 + swap23(f & 15)) * 16) = o;
    }
#pragma unroll
    for (int c = 0; c < 4; ++c)
      *(f32x4*)(red + oct * 256 + c4 * 64 + c * 16) = f32x4{pr[2 * c][0], pr[2 * c][1], pr[2 * c + 1][0], pr[2 * c + 1][1]};
  }
  __syncthreads();
  {
    const int col = t & 31, p = t >> 5;
    f32x2 v = {0.f, 0.f};
#pragma unroll
    for (int i = 0; i < 7; ++i) v += *(const f32x2*)(red + (p * 7 + i) * 256 + col * 8);
    *(f32x2*)(red + 14336 + p * 256 + col * 8) = v;
  }
  __syncthreads();
  if (t < 64) {
    const int col = t & 31;
    f32x2 v = {0.f, 0.f};
#pragma unroll
    for (int i = 0; i < 8; ++i) v += *(const f32x2*)(red + 14336 + i * 256 + col * 8);
    const float ch = rbf(v[0]), cl = v[0] - ch, bh = rbf(v[1]), bl = v[1] - bh;
    u32x4 o = {pack2(ch, ch), pack2(cl, cl), pack2(bh, bh), pack2(bl, bl)};
    if (t >= 32) o = u32x4{0, 0, 0, 0};
    const int f = cg * 32 + col;
    *(u32x4*)(ws + OFF_WGU + ((size_t)((f >> 4) * 29 + 28) * 64 + (t >> 5) * 32 + mat * 16 + swap23(f & 15)) * 16) = o;
  }
}

__global__ __launch_bounds__(256) void k0_prep(const uint16_t* __restrict__ x, const uint16_t* __restrict__ wqkv,
                                               const uint16_t* __restrict__ cvec, const uint16_t* __restrict__ wmod,
                                               char* __restrict__ ws) {
  __shared__ __attribute__((aligned(16))) float sm[512];
  const int b = blockIdx.x, t = threadIdx.x, lane = t & 63, w = t >> 6;
  PDECL; STAMP(0);
  if (b < 56) {
    const int u = (b & 7) * 7 + (b >> 3), pi = u / 14;
    mod_unit(cvec, wmod, ws, (pi + (pi >= 2 ? 1 : 0)) * 448 + (u % 14) * 32, t, sm);
  } else if (b < 120) {
    const int mt = b - 56, tok = lane & 31, half = lane >> 5;
    const uint16_t* src = x + (size_t)(mt * 32 + tok) * 448 + (7 * w) * 16 + half * 8;
    u32x4 v[7];
#pragma unroll
    for (int i = 0; i < 7; ++i) v[i] = *(const u32x4*)(src + i * 16);
    float s = 0.f;
#pragma unroll
    for (int i = 0; i < 7; ++i)
#pragma unroll
      for (int j = 0; j < 4; ++j) s += bflo(v[i][j]) + bfhi(v[i][j]);
    const float lm = s * (1.f / 56.f);
    float m2 = 0.f;
#pragma unroll
    for (int i = 0; i < 7; ++i)
#pragma unroll
      for (int j = 0; j < 4; ++j) {
        float a = bflo(v[i][j]) - lm, c = bfhi(v[i][j]) - lm;
        m2 += a * a + c * c;
      }
    f32x2* part = (f32x2*)sm;
    part[(w * 2 + half) * 32 + tok] = f32x2{lm, m2};
    __syncthreads();
    STAMP(1);
    float mu = 0.f, M2 = 0.f;
    f32x2 p[8];
#pragma unroll
    for (int i = 0; i < 8; ++i) { p[i] = part[i * 32 + tok]; mu += p[i][0]; M2 += p[i][1]; }
    mu *= 0.125f;
#pragma unroll
    for (int i = 0; i < 8; ++i) { float d = p[i][0] - mu; M2 += 56.f * d * d; }
    const float rstd = rsqrtf(M2 * (1.f / 448.f) + 1e-5f);
    char* dst = ws + OFF_XN + ((size_t)(mt * 28 + 7 * w) * 64 + lane) * 16;
#pragma unroll
    for (int i = 0; i < 7; ++i) {
      u32x4 o;
#pragma unroll
      for (int j = 0; j < 4; ++j) o[j] = pack2((bflo(v[i][j]) - mu) * rstd, (bfhi(v[i][j]) - mu) * rstd);
      *(u32x4*)(dst + i * 1024) = o;
    }
  } else {
    const int rb = b - 120;
    retile_unit(wqkv, ws, 0, rb / 3, rb % 3, t);
  }
  PFLUSH(0);
}

constexpr int k1_posW(int c) { return c == 0 ? 3 : c == 1 ? 10 : c == 2 ? 13 : 20 + 7 * (c - 3); }
constexpr int k1_posX(int k) { return (k < 4 ? 4 : k < 8 ? 10 : k < 24 ? 13 + 3 * (k / 4 - 2) : 22) + k; }
constexpr int k1_iss(int k) { return 18 + (2 * k < 32 ? 2 * k : 32); }
__global__ __launch_bounds__(256) __attribute__((amdgpu_waves_per_eu(1, 1))) void k1_qkv(char* __restrict__ ws) {
  extern __shared__ __attribute__((aligned(16))) char lds[];
  const int t = threadIdx.x, lane = t & 63, w = __builtin_amdgcn_readfirstlane(t >> 6);
  PDECL; STAMP(0);
  const int xc = blockIdx.x & 7, bi = blockIdx.x >> 3;
  const int mb = (xc & 3) * 4 + (bi & 3), head = (xc >> 2) * 8 + (bi >> 2);
  const int mt = mb * 4 + w;
  const int hi = lane >> 5, l31 = lane & 31;
  const char* A = ws + OFF_XN + (size_t)mt * 28 * 1024 + lane * 16;
  const char* B = ws + OFF_WQKV + ((size_t)head * 3 * 28 + w) * 1024 + lane * 16;
  char* modv = lds + 87 * 1024 + w * 1024;
  float* P = (float*)(lds + 91 * 1024);
  GLDS16(ws + OFF_MODA + (4 * (lane >> 3) + w) * 128 + (lane & 7) * 16, modv);
  u32x4 xr[29], sw[7][3];
  auto ldW = [&](auto cc, auto ic) {
    constexpr int c = decltype(cc)::value, i = decltype(ic)::value;
    gload16(sw[c][i], B + (size_t)(i * 28 + 4 * c) * 1024);
  };
  auto ldX = [&](auto kc) {
    constexpr int k = decltype(kc)::value;
    gload16a(xr[k], A + k * 1024);
  };
  auto ldN = [&](auto nc) {
    constexpr int n = decltype(nc)::value, g = n / 7, r = n % 7;
    if constexpr (n >= 35) { if constexpr (n < 39) ldX(IC(24 + n - 35)); }
    else if constexpr (r < 3) ldW(IC(g + 2), IC(r));
    else ldX(IC(4 * (g + 1) + r - 3));
  };
  static_for<0, 3>([&](auto i) { ldW(IC(0), i); });
  static_for<0, 4>(ldX);
  static_for<0, 3>([&](auto i) { ldW(IC(1), i); });
  f32x16 acc[3];
#pragma unroll
  for (int i = 0; i < 3; ++i)
#pragma unroll
    for (int r = 0; r < 16; ++r) acc[i][r] = 0.f;
  const unsigned wl0 = lds_addr(lds) + lane * 16, wl1 = wl0 + 49152;
  const unsigned sa0 = wl0 + w * 3072, sa1 = sa0 + 49152;
  const unsigned ml = lds_addr(modv) + hi * 64;
  u32x4 wb[3][3], mv[7][4];
  float bp[3] = {0.f, 0.f, 0.f};
  auto rdw = [&](auto kc) {
    constexpr int k = decltype(kc)::value;
    constexpr int o = (k & 15) * 3072;
    const unsigned a = (k >= 16) ? wl1 : wl0;
    lread16<o>(wb[k % 3][0], a); lread16<o + 1024>(wb[k % 3][1], a); lread16<o + 2048>(wb[k % 3][2], a);
  };
  auto rdm = [&](auto cc) {
    constexpr int c = decltype(cc)::value;
    lread16<c * 128>(mv[c][0], ml); lread16<c * 128 + 16>(mv[c][1], ml);
    lread16<c * 128 + 32>(mv[c][2], ml); lread16<c * 128 + 48>(mv[c][3], ml);
  };
  auto rdym = [&](auto cc) {
    constexpr int c = decltype(cc)::value;
    rdyv(mv[c][0]); rdyv(mv[c][1]); rdyv(mv[c][2]); rdyv(mv[c][3]);
  };
  auto word = [&](auto cc, auto qc) {
    constexpr int c = decltype(cc)::value, q = decltype(qc)::value, i = q >> 2, j = q & 3;
    const unsigned wv = sw[c][i][j];
    const float lo = bflo(wv), hh = bfhi(wv);
    const f32x4 sc = F4(mv[c][j >> 1]), sh = F4(mv[c][2 + (j >> 1)]);
    fmac1(bp[i], lo, sh[(2 * j) & 3]);
    fmac1(bp[i], hh, sh[(2 * j + 1) & 3]);
    sw[c][i][j] = pack2(fmul1(lo, sc[(2 * j) & 3]), fmul1(hh, sc[(2 * j + 1) & 3]));
    if constexpr (j == 3) lwrite16<(c & 3) * 12288 + i * 1024>(sw[c][i], c < 4 ? sa0 : sa1);
  };
  PDECL2;
  BST(0);
  vwait<7>();
  BST(8);
  rdyv(sw[0][0]); rdyv(sw[0][1]); rdyv(sw[0][2]);
  rdm(IC(0)); rdm(IC(1)); rdm(IC(2));
  ldN(IC(0));
  lwait<0>();
  static_for<0, 3>(rdym);
  static_for<0, 12>([&](auto q) {
    word(IC(0), q);
    if constexpr (q % 2 == 0) ldN(IC(1 + q / 2));
    SB();
  });
  wait_bar<7>();
  STAMP(1);
  rdya(xr[0]);
  rdyv(sw[1][0]); rdyv(sw[1][1]); rdyv(sw[1][2]);
  rdw(IC(0)); rdw(IC(1));
  lwait<3>();
  rdyv(wb[0][0]); rdyv(wb[0][1]); rdyv(wb[0][2]);
  xr[28] = u32x4{0x3f803f80u, 0, 0, 0};
  SB();
  static_for<0, 29>([&](auto kc) {
    constexpr int k = decltype(kc)::value;
    constexpr int cn = (k < 2) ? 1 : (k + 6) / 4, r = (k + 6) % 4;
    constexpr bool PR = k < 22;
    auto gap = [&](auto gc) {
      constexpr int g = decltype(gc)::value;
      if constexpr (k < 2) { word(IC(1), IC(6 * k + 2 * g)); word(IC(1), IC(6 * k + 2 * g + 1)); }
      else if constexpr (PR) word(IC(cn), IC(3 * r + g));
    };
    acc[0] = MFMA32(BF8(wb[k % 3][0]), BF8(xr[k]), acc[0]);
    if constexpr ((k + 2) % 4 == 0) {
      constexpr int j = (k + 2) / 4;
      BST(j);
      wait_bar<(j < 6) ? k1_iss(k) - 1 - k1_posW(j + 1) : 63>();
      BST(8 + j);
      if constexpr (j < 6) { rdyv(sw[j + 1][0]); rdyv(sw[j + 1][1]); rdyv(sw[j + 1][2]); }
    }
    if constexpr (k >= 2 && r == 2 && cn + 1 < 7) rdm(IC(cn + 1));
    if constexpr (k + 2 < 29) rdw(IC(k + 2));
    gap(IC(0));
    SB();
    acc[1] = MFMA32(BF8(wb[k % 3][1]), BF8(xr[k]), acc[1]);
    ldN(IC(7 + 2 * k));
    gap(IC(1));
    if constexpr (k == 23) {
      if (w < 3) {
        float bs = P[(w * 4 + 0) * 64 + lane] + P[(w * 4 + 1) * 64 + lane] + P[(w * 4 + 2) * 64 + lane] + P[(w * 4 + 3) * 64 + lane];
        bs = xhalf_sum(bs);
        const float bh = rbf(bs);
        *(u32x4*)(lds + (84 + w) * 1024 + lane * 16) = u32x4{hi ? 0u : pack2(bh, bs - bh), 0, 0, 0};
      }
    }
    SB();
    acc[2] = MFMA32(BF8(xr[k]), BF8(wb[k % 3][2]), acc[2]);
    ldN(IC(8 + 2 * k));
    gap(IC(2));
    if constexpr (k == 21) {
#pragma unroll
      for (int i = 0; i < 3; ++i) P[(i * 4 + w) * 64 + lane] = bp[i];
    }
    if constexpr (k + 1 < 29) {
      vlwait<(k + 1 < 28) ? k1_iss(k + 1) - 1 - k1_posX(k + 1) : 63, (k + 2 < 29) ? 3 : 0>();
      if constexpr (k + 1 < 28) rdya(xr[k + 1]);
      rdyv(wb[(k + 1) % 3][0]); rdyv(wb[(k + 1) % 3][1]); rdyv(wb[(k + 1) % 3][2]);
      if constexpr (k >= 2 && r == 2 && cn + 1 < 7) rdym(IC(cn + 1));
    }
    SB();
  });
  STAMP(2);
  const float qs = 0.18898223650461362f * 1.4426950408889634f;
#pragma unroll
  for (int p = 0; p < 2; ++p) {
    char* base = ws + (p ? OFF_K : OFF_Q) + ((size_t)(head * 128 + mt * 2 + (l31 >> 4)) * 64 + (l31 & 15)) * 16;
    const float sc = p ? 1.f : qs;
#pragma unroll
    for (int s = 0; s < 2; ++s) {
      u32x4 o;
#pragma unroll
      for (int j = 0; j < 4; ++j) o[j] = pack2(acc[p][8 * s + 2 * j] * sc, acc[p][8 * s + 2 * j + 1] * sc);
      *(u32x4*)(base + (2 * s + hi) * 256) = o;
    }
  }
  {
    char* base = ws + OFF_V + ((size_t)((head * 64 + mt) * 2 + (l31 >> 4)) * 64 + (l31 & 15)) * 16;
#pragma unroll
    for (int s = 0; s < 2; ++s) {
      u32x4 o = {pack2(acc[2][4 * s], acc[2][4 * s + 1]), pack2(acc[2][4 * s + 2], acc[2][4 * s + 3]),
                 pack2(acc[2][8 + 4 * s], acc[2][8 + 4 * s + 1]), pack2(acc[2][8 + 4 * s + 2], acc[2][8 + 4 * s + 3])};
      if (l31 == 28) o = u32x4{0x3f803f80u, 0x3f803f80u, 0x3f803f80u, 0x3f803f80u};
      *(u32x4*)(base + (2 * s + hi) * 256) = o;
    }
  }
  PFLUSH(1);
  PFLUSH2(6);
}

template <bool ODD>
DEVI void attn_pv(const char* vp, const u32x2 (&p)[17], f32x4& o0, f32x4& o1) {
  bf16x8 vf[9][2];
#pragma unroll
  for (int v = 0; v < 3; ++v) { vf[v][0] = *(const bf16x8*)(vp + v * 2048); vf[v][1] = *(const bf16x8*)(vp + v * 2048 + 1024); }
#pragma unroll
  for (int v = 0; v < 9; ++v) {
    if (v + 3 < 9) {
      vf[v + 3][0] = *(const bf16x8*)(vp + (v + 3) * 2048);
      vf[v + 3][1] = *(const bf16x8*)(vp + (v + 3) * 2048 + 1024);
    }
    const int i0 = 2 * v - (ODD ? 1 : 0), i1 = i0 + 1;
    const u32x2 z = {0, 0};
    const u32x2 pa = i0 >= 0 ? p[i0 >= 0 ? i0 : 0] : z, pc = i1 < 17 ? p[i1 < 17 ? i1 : 0] : z;
    const u32x4 pw = {pa[0], pa[1], pc[0], pc[1]};
    const bf16x8 pf = __builtin_bit_cast(bf16x8, pw);
    o0 = MFMA16(vf[v][0], pf, o0);
    o1 = MFMA16(vf[v][1], pf, o1);
    SB();
  }
}

DEVI void attn_wave(const char* ldsK, const char* ldsV, const char* ldsQ, int w, int lane, int nneg, char* odst, int kbase) {
  const int g = lane >> 4, qq = lane & 15;
  const bf16x8 q = *(const bf16x8*)(ldsQ + w * 1024 + lane * 16);
  const float NEG = -1e30f;
  f32x4 s[17];
  const char* kp = ldsK + w * 1024 + lane * 16;
  bf16x8 kf[17];
#pragma unroll
  for (int tt = 0; tt < 4; ++tt) kf[tt] = *(const bf16x8*)(kp + tt * 1024);
#pragma unroll
  for (int tt = 0; tt < 17; ++tt) {
    if (tt + 4 < 17) kf[tt + 4] = *(const bf16x8*)(kp + (tt + 4) * 1024);
    s[tt] = MFMA16(kf[tt], q, (f32x4{0.f, 0.f, 0.f, 0.f}));
    SB();
  }
#pragma unroll
  for (int r = 0; r < 4; ++r) {
    s[0][r] = (4 * g + r > qq) ? s[0][r] : NEG;
    s[16][r] = (4 * g + r <= qq) ? s[16][r] : NEG;
  }
  if (nneg > 0) {
#pragma unroll
    for (int tt = 0; tt < 16; ++tt)
      if (w + tt < nneg) s[tt] = f32x4{NEG, NEG, NEG, NEG};
  }
  float m = NEG;
#pragma unroll
  for (int tt = 0; tt < 17; ++tt) m = fmaxf(fmaxf(m, fmaxf(s[tt][0], s[tt][1])), fmaxf(s[tt][2], s[tt][3]));
  m = fmaxf(m, __shfl_xor(m, 16));
  m = fmaxf(m, __shfl_xor(m, 32));
  u32x2 pb[17];
#pragma unroll
  for (int tt = 0; tt < 17; ++tt) {
    const float e0 = __builtin_amdgcn_exp2f(s[tt][0] - m), e1 = __builtin_amdgcn_exp2f(s[tt][1] - m);
    const float e2 = __builtin_amdgcn_exp2f(s[tt][2] - m), e3 = __builtin_amdgcn_exp2f(s[tt][3] - m);
    pb[tt] = u32x2{pack2(e0, e1), pack2(e2, e3)};
  }
  wait_bar<0>();
  f32x4 o0 = {0.f, 0.f, 0.f, 0.f}, o1 = {0.f, 0.f, 0.f, 0.f};
  const char* vp = ldsV + (w >> 1) * 2048 + lane * 16;
  if (w & 1) attn_pv<true>(vp, pb, o0, o1);
  else attn_pv<false>(vp, pb, o0, o1);
  const float sum = __shfl(o1[0], 48 + qq);
  const float inv = 1.f / sum;
  {
    const int k = kbase + 4 * g;
    u32x2 o = {pack2(o0[0] * inv, o0[1] * inv), pack2(o0[2] * inv, o0[3] * inv)};
    *(u32x2*)(odst + (size_t)(k >> 4) * 1024 + ((k >> 3) & 1) * 512 + (k & 4) * 2) = o;
  }
  if (g < 3) {
    const int k = kbase + 16 + 4 * g;
    u32x2 o = {pack2(o1[0] * inv, o1[1] * inv), pack2(o1[2] * inv, o1[3] * inv)};
    *(u32x2*)(odst + (size_t)(k >> 4) * 1024 + ((k >> 3) & 1) * 512 + (k & 4) * 2) = o;
  }
}

__global__ __launch_bounds__(512) __attribute__((amdgpu_waves_per_eu(4, 4))) void k2_attn(
    const uint16_t* __restrict__ wo, const uint16_t* __restrict__ wg, const uint16_t* __restrict__ wu,
    const uint16_t* __restrict__ wd, const uint16_t* __restrict__ cvec, const uint16_t* __restrict__ wmod,
    char* __restrict__ ws) {
  extern __shared__ __attribute__((aligned(16))) char lds[];
  const int t = threadIdx.x, lane = t & 63, w = __builtin_amdgcn_readfirstlane(t >> 6);
  PDECL; STAMP(0);
  if (blockIdx.x >= 256) {
    const int u = (blockIdx.x - 256) * 2 + (w >> 2), tt = t & 255;
    char* red = lds + (w >> 2) * 16384;
    if (u < 112) fold_unit(u < 56 ? wg : wu, ws, u < 56 ? 0 : 1, u % 56, tt, red);
    else if (u < 168) retile_unit(wd, ws, 3, u - 112, 0, tt);
    else if (u < 182) retile_unit(wo, ws, 2, u - 168, 0, tt);
    else mod_unit(cvec, wmod, ws, (u < 196 ? 2 : 5) * 448 + ((u - 182) % 14) * 32, tt, (float*)red);
    PFLUSH(2);
    return;
  }
  const int xc = blockIdx.x & 7, bi = blockIdx.x >> 3;
  const int head = xc * 2 + (bi & 1), qb = bi >> 1;
  char* ldsK = lds;
  char* ldsV = lds + 24 * 1024;
  char* ldsQ = lds + 48 * 1024;
  const int sub0 = qb * 8 - 16;
  GLDS16(ws + OFF_Q + (size_t)(head * 128 + qb * 8 + w) * 1024 + lane * 16, ldsQ + w * 1024);
#pragma unroll
  for (int i = 0; i < 3; ++i) {
    const int p = w * 3 + i;
    const int sidx = max(sub0 + p, 0);
    GLDS16(ws + OFF_K + (size_t)(head * 128 + sidx) * 1024 + lane * 16, ldsK + p * 1024);
  }
#pragma unroll
  for (int i = 0; i < 3; ++i) {
    const int p = w * 3 + i;
    const int sidx = max(sub0 + p, 0);
    GLDS16(ws + OFF_V + (size_t)(head * 128 + sidx) * 1024 + lane * 16, ldsV + p * 1024);
  }
  wait_bar<3>();
  STAMP(1);
  const int tok = qb * 128 + w * 16 + (lane & 15);
  char* odst = ws + OFF_O + (size_t)(tok >> 5) * 28 * 1024 + (tok & 31) * 16;
  const int kbase = head * 28;
  const int nneg = -sub0;
  attn_wave(ldsK, ldsV, ldsQ, w, lane, nneg, odst, kbase);
  PFLUSH(2);
}

DEVI f32x16 ksplit_reduce(f32x16 (&acc)[4], char* lds, int w, int lane) {
#pragma unroll
  for (int tl = 0; tl < 4; ++tl) {
    if (tl != w) {
      const int slot = (w > tl) ? w - 1 : w;
      char* p = lds + (tl * 3 + slot) * 4096 + lane * 16;
#pragma unroll
      for (int q = 0; q < 4; ++q)
        *(f32x4*)(p + q * 1024) = f32x4{acc[tl][4 * q], acc[tl][4 * q + 1], acc[tl][4 * q + 2], acc[tl][4 * q + 3]};
    }
  }
  f32x16 r = (w == 0) ? acc[0] : (w == 1) ? acc[1] : (w == 2) ? acc[2] : acc[3];
  __syncthreads();
#pragma unroll
  for (int s = 0; s < 3; ++s) {
    const char* p = lds + (w * 3 + s) * 4096 + lane * 16;
#pragma unroll
    for (int q = 0; q < 4; ++q) {
      const f32x4 v = *(const f32x4*)(p + q * 1024);
#pragma unroll
      for (int j = 0; j < 4; ++j) r[4 * q + j] += v[j];
    }
  }
  return r;
}

__global__ __launch_bounds__(256) __attribute__((amdgpu_waves_per_eu(1, 1))) void k3_wo(const uint16_t* __restrict__ x,
                                                                                        char* __restrict__ ws) {
  __shared__ __attribute__((aligned(16))) char lds[48 * 1024];
  const int t = threadIdx.x, lane = t & 63, w = __builtin_amdgcn_readfirstlane(t >> 6);
  PDECL; STAMP(0);
  const int xc = blockIdx.x & 7, bi = blockIdx.x >> 3;
  const int mb = xc * 4 + (bi & 3), nb = bi >> 2;
  const char* A = ws + OFF_O + ((size_t)mb * 2 * 28 + 7 * w) * 1024 + lane * 16;
  const char* B = ws + OFF_WO + ((size_t)nb * 2 * 28 + 7 * w) * 1024 + lane * 16;
  const int wm = w & 1, wn = w >> 1;
  const int hi = lane >> 5, l31 = lane & 31;
  const int mt = mb * 2 + wm, nt = nb * 2 + wn;
  bf16x8 a[7][2], bw[7][2];
#pragma unroll
  for (int k = 0; k < 7; ++k) {
    a[k][0] = *(const bf16x8*)(A + k * 1024);
    bw[k][0] = *(const bf16x8*)(B + k * 1024);
    a[k][1] = *(const bf16x8*)(A + (28 + k) * 1024);
    bw[k][1] = *(const bf16x8*)(B + (28 + k) * 1024);
  }
  u32x4 xv[2];
  f32x4 gv[4];
  {
    const uint16_t* xs = x + (size_t)(mt * 32 + l31) * 448 + nt * 32 + hi * 8;
    xv[0] = *(const u32x4*)xs;
    xv[1] = *(const u32x4*)(xs + 16);
    const float* gp = (const float*)(ws + OFF_GAB) + nt * 32 + hi * 8;
    gv[0] = *(const f32x4*)gp; gv[1] = *(const f32x4*)(gp + 4);
    gv[2] = *(const f32x4*)(gp + 16); gv[3] = *(const f32x4*)(gp + 20);
  }
  __builtin_amdgcn_sched_barrier(0);
  f32x16 acc[4];
#pragma unroll
  for (int i = 0; i < 4; ++i)
#pragma unroll
    for (int r = 0; r < 16; ++r) acc[i][r] = 0.f;
#pragma unroll
  for (int k = 0; k < 7; ++k) {
    acc[0] = MFMA32(bw[k][0], a[k][0], acc[0]);
    acc[1] = MFMA32(bw[k][0], a[k][1], acc[1]);
    acc[2] = MFMA32(bw[k][1], a[k][0], acc[2]);
    acc[3] = MFMA32(bw[k][1], a[k][1], acc[3]);
  }
  STAMP(1);
  const f32x16 r = ksplit_reduce(acc, lds, w, lane);
  STAMP(2);
  float v[16];
#pragma unroll
  for (int s = 0; s < 2; ++s) {
    u32x4 o;
#pragma unroll
    for (int j = 0; j < 4; ++j) {
      const float ga0 = gv[2 * s + (j >> 1)][(2 * j) & 3], ga1 = gv[2 * s + (j >> 1)][(2 * j + 1) & 3];
      o[j] = pack2(bflo(xv[s][j]) + ga0 * r[8 * s + 2 * j], bfhi(xv[s][j]) + ga1 * r[8 * s + 2 * j + 1]);
      v[8 * s + 2 * j] = bflo(o[j]);
      v[8 * s + 2 * j + 1] = bfhi(o[j]);
    }
    *(u32x4*)(ws + OFF_X1 + ((size_t)(mt * 28 + nt * 2 + s) * 64 + lane) * 16) = o;
  }
  float sm = 0.f;
#pragma unroll
  for (int j = 0; j < 16; ++j) sm += v[j];
  sm += __shfl_xor(sm, 32);
  const float mean = sm * (1.f / 32.f);
  float m2 = 0.f;
#pragma unroll
  for (int j = 0; j < 16; ++j) { const float d = v[j] - mean; m2 += d * d; }
  m2 += __shfl_xor(m2, 32);
  if (hi == 0) *(f32x2*)(ws + OFF_STATS + ((size_t)(mt * 8 + nb) * 32 + l31) * 16 + wn * 8) = f32x2{mean, m2};
  PFLUSH(3);
}

constexpr int k4_posG(int g) { return g < 2 ? g : g < 4 ? g + 2 : 8 + 3 * (g - 4); }
constexpr int k4_posX(int s) { return s == 0 ? 3 : s == 1 ? 7 : s < 27 ? 10 + 3 * (s - 2) : 84; }
constexpr int k4_iss(int k) { return k <= 20 ? 23 + 3 * k : k == 21 ? 85 : k == 22 ? 89 : 93; }
__global__ __launch_bounds__(256) __attribute__((amdgpu_waves_per_eu(1, 1))) void k4_gu(char* __restrict__ ws) {
  extern __shared__ __attribute__((aligned(16))) char lds[];
  const int t = threadIdx.x, lane = t & 63, w = __builtin_amdgcn_readfirstlane(t >> 6);
  PDECL; STAMP(0);
  const int xc = blockIdx.x & 7, bi = blockIdx.x >> 3;
  const int mb = (xc & 1) * 4 + (bi & 3), fb = (xc >> 1) * 7 + (bi >> 2);
  const int mt = mb * 8 + 2 * w;
  const char* A = ws + OFF_X1 + (size_t)mt * 28 * 1024 + lane * 16;
  const char* B = ws + OFF_WGU + (size_t)(fb * 4 + w) * 29 * 1024 + lane * 16;
  const int hi = lane >> 5, l31 = lane & 31;
  u32x4 xr[29][2], st[2][4];
  auto ldG = [&](auto gc) {
    constexpr int g = decltype(gc)::value;
    GLDS16(B + g * 1024, lds + (g * 4 + w) * 1024);
  };
  auto ldXa = [&](auto sc) { constexpr int s = decltype(sc)::value; gload16a(xr[s][0], A + s * 1024); };
  auto ldXb = [&](auto sc) { constexpr int s = decltype(sc)::value; gload16a(xr[s][1], A + (28 + s) * 1024); };
  ldG(IC(0)); ldG(IC(1)); ldXa(IC(0)); ldXb(IC(0)); ldG(IC(2)); ldG(IC(3)); ldXa(IC(1)); ldXb(IC(1));
  static_for<2, 7>([&](auto sc) { ldG(IC(sc + 2)); ldXa(sc); ldXb(sc); });
  f32x16 acc[2][4];
#pragma unroll
  for (int i = 0; i < 8; ++i)
#pragma unroll
    for (int r = 0; r < 16; ++r) acc[i >> 2][i & 3][r] = 0.f;
  const unsigned wa0 = lds_addr(lds) + lane * 16, wa1 = wa0 + 65536;
  const char* S = ws + OFF_STATS + (size_t)mt * 4096 + (hi * 4 * 32 + l31) * 16;
  u32x4 wb[3][4];
  float rstd[2];
  auto rdw = [&](auto kc) {
    constexpr int k = decltype(kc)::value;
    constexpr int o = (k & 15) * 4096;
    const unsigned a = (k >= 16) ? wa1 : wa0;
    lread16<o>(wb[k % 3][0], a); lread16<o + 1024>(wb[k % 3][1], a);
    lread16<o + 2048>(wb[k % 3][2], a); lread16<o + 3072>(wb[k % 3][3], a);
  };
  PDECL2;
  BST(0);
  vbar<21>();
  BST(8);
  STAMP(1);
  rdw(IC(0)); rdw(IC(1));
  vlwait<19, 4>();
  rdya(xr[0][0]); rdya(xr[0][1]);
  rdyv(wb[0][0]); rdyv(wb[0][1]); rdyv(wb[0][2]); rdyv(wb[0][3]);
  SB();
  static_for<0, 29>([&](auto kc) {
    constexpr int k = decltype(kc)::value;
    static_for<0, 8>([&](auto gc) {
      constexpr int g = decltype(gc)::value;
      acc[g & 1][g >> 1] = MFMA32(BF8(wb[k % 3][g >> 1]), BF8(xr[k][g & 1]), acc[g & 1][g >> 1]);
      if constexpr (g == 0) {
        if constexpr (k % 2 == 0 && k + 2 < 29) {
          if constexpr (k % 4 == 0) BST(1 + k / 4);
          vbar<k4_iss(k) - 1 - k4_posG(k + 3 < 29 ? k + 3 : 28)>();
          if constexpr (k % 4 == 0) BST(9 + k / 4);
        }
        if constexpr (k + 2 < 29) rdw(IC(k + 2));
      }
      if constexpr (g == 1 && k + 9 < 29) ldG(IC(k + 9));
      if constexpr (g == 3 && k + 7 < 28) ldXa(IC(k + 7));
      if constexpr (g == 5 && k + 7 < 28) ldXb(IC(k + 7));
      if constexpr ((k == 21 || k == 22) && (g & 1) == 1) gload16(st[k - 21][g >> 1], S + (k - 21) * 4096 + (g >> 1) * 512);
      if constexpr ((k == 26 || k == 27) && g == 1) {
        constexpr int tt = k - 26;
        if constexpr (tt == 0) {
          vwait<0>();
#pragma unroll
          for (int i = 0; i < 8; ++i) rdyv(st[i >> 2][i & 3]);
        }
        f32x4 p[4];
#pragma unroll
        for (int i = 0; i < 4; ++i) p[i] = F4(st[tt][i]);
        if (hi) p[3] = f32x4{0.f, 0.f, 0.f, 0.f};
        float sm = fadd1(p[0][0], p[0][2]), M2 = fadd1(p[0][1], p[0][3]);
#pragma unroll
        for (int i = 1; i < 4; ++i) { sm = fadd1(sm, fadd1(p[i][0], p[i][2])); M2 = fadd1(M2, fadd1(p[i][1], p[i][3])); }
        const float mu = xhalf_sum(sm) * (1.f / 14.f);
        if (hi) { p[3][0] = mu; p[3][2] = mu; }
        float dd = 0.f;
#pragma unroll
        for (int i = 0; i < 4; ++i) {
          const float d0 = fsub1(p[i][0], mu), d1 = fsub1(p[i][2], mu);
          fmac1(dd, d0, d0); fmac1(dd, d1, d1);
        }
        const float var = xhalf_sum(fmaf(dd, 32.f, M2)) * (1.f / 448.f) + 1e-5f;
        rstd[tt] = rsqrtf(var);
        const float sg = var * rstd[tt];
        const float mh = rbf(mu), sh = rbf(sg);
        const unsigned pm = pack2(-mh, mh - mu), ps = pack2(sh, sg - sh);
        xr[28][tt] = u32x4{pm, pm, ps, ps};
      }
      if constexpr (g == 7 && k + 1 < 29) {
        vlwait<(k + 1 < 28) ? k4_iss(k + 1) - 1 - k4_posX(k + 1) : 63, (k + 2 < 29) ? 4 : 0>();
        if constexpr (k + 1 < 28) { rdya(xr[k + 1][0]); rdya(xr[k + 1][1]); }
        rdyv(wb[(k + 1) % 3][0]); rdyv(wb[(k + 1) % 3][1]); rdyv(wb[(k + 1) % 3][2]); rdyv(wb[(k + 1) % 3][3]);
      }
      if constexpr (!((k == 26 || k == 27) && g >= 1 && g < 6)) SB();
    });
  });
  STAMP(2);
  const float c0 = -2.f * 0.7978845608028654f * 1.4426950408889634f, c1 = c0 * 0.044715f;
  char* hdst = ws + OFF_HFF + ((size_t)(mt * 112 + fb * 4) * 64 + lane) * 16;
#pragma unroll
  for (int tt = 0; tt < 2; ++tt)
#pragma unroll
    for (int n = 0; n < 4; ++n) {
      float hh[8];
#pragma unroll
      for (int j = 0; j < 8; ++j) {
        const float g = acc[tt][n][j] * rstd[tt], u = acc[tt][n][8 + j] * rstd[tt];
        const float e = __builtin_amdgcn_exp2f(g * fmaf(g * g, c1, c0));
        hh[j] = g * u * __builtin_amdgcn_rcpf(1.f + e);
      }
      u32x4 o = {pack2(hh[0], hh[1]), pack2(hh[2], hh[3]), pack2(hh[4], hh[5]), pack2(hh[6], hh[7])};
      *(u32x4*)(hdst + (tt * 112 + n) * 1024) = o;
    }
  PFLUSH(4);
  PFLUSH2(8);
}

__global__ __launch_bounds__(256) __attribute__((amdgpu_waves_per_eu(1, 1))) void k5_down(char* __restrict__ ws,
                                                                                          uint16_t* __restrict__ out) {
  __shared__ __attribute__((aligned(16))) char lds[48 * 1024];
  const int t = threadIdx.x, lane = t & 63, w = __builtin_amdgcn_readfirstlane(t >> 6);
  PDECL; STAMP(0);
  const int xc = blockIdx.x & 7, bi = blockIdx.x >> 3;
  const int mb = xc * 4 + (bi & 3), nb = bi >> 2;
  const char* A = ws + OFF_HFF + ((size_t)mb * 2 * 112 + 28 * w) * 1024 + lane * 16;
  const char* B = ws + OFF_WD + ((size_t)nb * 2 * 112 + 28 * w) * 1024 + lane * 16;
  const int wm = w & 1, wn = w >> 1;
  const int hi = lane >> 5, l31 = lane & 31;
  const int mt = mb * 2 + wm, nt = nb * 2 + wn;
  constexpr int D = 12;
  bf16x8 a[D][2], bw[D][2];
  auto ld = [&](int k) {
    a[k % D][0] = *(const bf16x8*)(A + k * 1024);
    bw[k % D][0] = *(const bf16x8*)(B + k * 1024);
    a[k % D][1] = *(const bf16x8*)(A + (112 + k) * 1024);
    bw[k % D][1] = *(const bf16x8*)(B + (112 + k) * 1024);
  };
#pragma unroll
  for (int k = 0; k < D; ++k) ld(k);
  u32x4 xv[2];
  f32x4 gv[4];
  f32x16 acc[4];
#pragma unroll
  for (int i = 0; i < 4; ++i)
#pragma unroll
    for (int r = 0; r < 16; ++r) acc[i][r] = 0.f;
  __builtin_amdgcn_sched_barrier(0);
#pragma unroll
  for (int k = 0; k < 28; ++k) {
    acc[0] = MFMA32(bw[k % D][0], a[k % D][0], acc[0]);
    acc[1] = MFMA32(bw[k % D][0], a[k % D][1], acc[1]);
    acc[2] = MFMA32(bw[k % D][1], a[k % D][0], acc[2]);
    acc[3] = MFMA32(bw[k % D][1], a[k % D][1], acc[3]);
    if (k + D < 28) ld(k + D);
    if (k + D == 28) {
      const char* xs = ws + OFF_X1 + ((size_t)(mt * 28 + nt * 2) * 64 + lane) * 16;
      xv[0] = *(const u32x4*)xs;
      xv[1] = *(const u32x4*)(xs + 1024);
      const float* gp = (const float*)(ws + OFF_GAB) + 448 + nt * 32 + hi * 8;
      gv[0] = *(const f32x4*)gp; gv[1] = *(const f32x4*)(gp + 4);
      gv[2] = *(const f32x4*)(gp + 16); gv[3] = *(const f32x4*)(gp + 20);
    }
    __builtin_amdgcn_sched_barrier(0);
  }
  STAMP(1);
  const f32x16 r = ksplit_reduce(acc, lds, w, lane);
  STAMP(2);
#pragma unroll
  for (int s = 0; s < 2; ++s) {
    u32x4 o;
#pragma unroll
    for (int j = 0; j < 4; ++j) {
      const float ga0 = gv[2 * s + (j >> 1)][(2 * j) & 3], ga1 = gv[2 * s + (j >> 1)][(2 * j + 1) & 3];
      o[j] = pack2(bflo(xv[s][j]) + ga0 * r[8 * s + 2 * j], bfhi(xv[s][j]) + ga1 * r[8 * s + 2 * j + 1]);
    }
    *(u32x4*)(out + (size_t)(mt * 32 + l31) * 448 + nt * 32 + s * 16 + hi * 8) = o;
  }
  PFLUSH(5);
}

constexpr int LDS_K1 = 94 * 1024;
constexpr int LDS_K2 = 56 * 1024;
constexpr int LDS_K4 = 116 * 1024;

extern "C" void kernel_launch(void* const* d_in, const int* in_sizes, int n_in, void* d_out, int out_size, void* d_ws,
                              size_t ws_size, hipStream_t stream) {
  const uint16_t* x = (const uint16_t*)d_in[0];
  const uint16_t* wqkv = (const uint16_t*)d_in[1];
  const uint16_t* wo = (const uint16_t*)d_in[2];
  const uint16_t* wg = (const uint16_t*)d_in[4];
  const uint16_t* wu = (const uint16_t*)d_in[5];
  const uint16_t* wd = (const uint16_t*)d_in[6];
  const uint16_t* cv = (const uint16_t*)d_in[7];
  const uint16_t* wmod = (const uint16_t*)d_in[8];
  char* ws = (char*)d_ws;
  (void)hipFuncSetAttribute((const void*)k1_qkv, hipFuncAttributeMaxDynamicSharedMemorySize, LDS_K1);
  (void)hipFuncSetAttribute((const void*)k2_attn, hipFuncAttributeMaxDynamicSharedMemorySize, LDS_K2);
  (void)hipFuncSetAttribute((const void*)k4_gu, hipFuncAttributeMaxDynamicSharedMemorySize, LDS_K4);
  k0_prep<<<162, 256, 0, stream>>>(x, wqkv, cv, wmod, ws);
  k1_qkv<<<256, 256, LDS_K1, stream>>>(ws);
  k2_attn<<<361, 512, LDS_K2, stream>>>(wo, wg, wu, wd, cv, wmod, ws);
  k3_wo<<<224, 256, 0, stream>>>(x, ws);
  k4_gu<<<224, 256, LDS_K4, stream>>>(ws);
  k5_down<<<224, 256, 0, stream>>>(ws, (uint16_t*)d_out);
}
```

```cpp
#include <hip/hip_runtime.h>
#include <stdint.h>

typedef __attribute__((ext_vector_type(8))) short bf16x8;
typedef __attribute__((ext_vector_type(16))) float f32x16;
typedef __attribute__((ext_vector_type(4))) float f32x4;
typedef __attribute__((ext_vector_type(2))) float f32x2;
typedef __attribute__((ext_vector_type(2))) __bf16 bf16x2_t;
typedef __attribute__((ext_vector_type(4))) unsigned u32x4;
typedef __attribute__((ext_vector_type(2))) unsigned u32x2;

#define DEVI __device__ __forceinline__

DEVI unsigned pack2(float a, float b) {
  f32x2 v = {a, b};
  bf16x2_t r = __builtin_convertvector(v, bf16x2_t);
  return __builtin_bit_cast(unsigned, r);
}
DEVI float bflo(unsigned w) { return __builtin_bit_cast(float, w << 16); }
DEVI float bfhi(unsigned w) { return __builtin_bit_cast(float, w & 0xffff0000u); }
DEVI float rbf(float a) { return bflo(pack2(a, a)); }
DEVI int swap23(int d) { return (d & ~12) | ((d & 4) << 1) | ((d & 8) >> 1); }

constexpr size_t OFF_XN = 0;
constexpr size_t OFF_WQKV = OFF_XN + 64 * 28 * 1024;
constexpr size_t OFF_WO = OFF_WQKV + 48 * 28 * 1024;
constexpr size_t OFF_WGU = OFF_WO + 14 * 28 * 1024;
constexpr size_t OFF_WD = OFF_WGU + 112 * 29 * 1024;
constexpr size_t OFF_Q = OFF_WD + 14 * 112 * 1024;
constexpr size_t OFF_K = OFF_Q + 16 * 128 * 1024;
constexpr size_t OFF_V = OFF_K + 16 * 128 * 1024;
constexpr size_t OFF_O = OFF_V + 16 * 128 * 1024;
constexpr size_t OFF_X1 = OFF_O + 64 * 28 * 1024;
constexpr size_t OFF_HFF = OFF_X1 + 64 * 28 * 1024;
constexpr size_t OFF_STATS = OFF_HFF + 64 * 112 * 1024;
constexpr size_t OFF_MODA = OFF_STATS + 64 * 4096;
constexpr size_t OFF_MODB = OFF_MODA + 4096;
constexpr size_t OFF_GAB = OFF_MODB + 4096;
constexpr size_t OFF_PART = OFF_GAB + 4096;
constexpr size_t WS_END = OFF_PART + 28 * 3584 * 8;

#define PDECL
#define STAMP(i)
#define PFLUSH(kid)
#define PDECL2
#define BST(i)
#define PFLUSH2(kid)
#define GLDS16(src, dst) __builtin_amdgcn_global_load_lds((const unsigned*)(src), (unsigned*)(dst), 16, 0, 0)
#define GLDS4(src, dst) __builtin_amdgcn_global_load_lds((const unsigned*)(src), (unsigned*)(dst), 4, 0, 0)

template <int N>
DEVI void wait_bar() {
  asm volatile("s_waitcnt vmcnt(%0) lgkmcnt(0)\n\ts_barrier" ::"n"(N) : "memory");
}
template <int N>
DEVI void wait_bar4(u32x4& a, u32x4& b, u32x4& c, u32x4& d) {
  asm volatile("s_waitcnt vmcnt(%4) lgkmcnt(0)\n\ts_barrier" : "+v"(a), "+v"(b), "+v"(c), "+v"(d) : "n"(N) : "memory");
}
DEVI void gload16(u32x4& r, const char* p) {
  asm volatile("global_load_dwordx4 %0, %1, off" : "=v"(r) : "v"(p) : "memory");
}

typedef __attribute__((address_space(3))) char lds_char;
DEVI unsigned lds_addr(const char* p) { return (unsigned)(size_t)(lds_char*)p; }
template <int OFF>
DEVI void lread16(u32x4& r, unsigned a) {
  asm volatile("ds_read_b128 %0, %1 offset:%2" : "=v"(r) : "v"(a), "n"(OFF) : "memory");
}
template <int OFF>
DEVI void lwrite16(const u32x4& r, unsigned a) {
  asm volatile("ds_write_b128 %1, %0 offset:%2" ::"v"(r), "v"(a), "n"(OFF) : "memory");
}
template <int N>
DEVI void lwait() { asm volatile("s_waitcnt lgkmcnt(%0)" ::"n"(N) : "memory"); }
template <int N>
DEVI void vwait() { asm volatile("s_waitcnt vmcnt(%0)" ::"n"(N) : "memory"); }
DEVI void rdyv(u32x4& a) { asm volatile("" : "+v"(a)); }
DEVI void rdya(u32x4& a) { asm volatile("" : "+a"(a)); }
DEVI void gload16a(u32x4& r, const char* p) {
  asm volatile("global_load_dwordx4 %0, %1, off" : "=a"(r) : "v"(p) : "memory");
}
DEVI float dot2(unsigned a, unsigned b, float c) {
  return __builtin_amdgcn_fdot2_f32_bf16(__builtin_bit_cast(bf16x2_t, a), __builtin_bit_cast(bf16x2_t, b), c, false);
}
DEVI float fmul1(float a, float b) { float r; asm("v_mul_f32 %0, %1, %2" : "=v"(r) : "v"(a), "v"(b)); return r; }
DEVI float fadd1(float a, float b) { float r; asm("v_add_f32 %0, %1, %2" : "=v"(r) : "v"(a), "v"(b)); return r; }
DEVI float fsub1(float a, float b) { float r; asm("v_sub_f32 %0, %1, %2" : "=v"(r) : "v"(a), "v"(b)); return r; }
DEVI void fmac1(float& c, float a, float b) { asm("v_fmac_f32 %0, %1, %2" : "+v"(c) : "v"(a), "v"(b)); }
template <int NV, int NL>
DEVI void vlwait() { asm volatile("s_waitcnt vmcnt(%0) lgkmcnt(%1)" ::"n"(NV), "n"(NL) : "memory"); }
template <int N>
DEVI void vbar() { asm volatile("s_waitcnt vmcnt(%0)\n\ts_barrier" ::"n"(N) : "memory"); }
DEVI float xhalf_sum(float v) {
  const unsigned u = __builtin_bit_cast(unsigned, v);
  const f32x2 r = __builtin_bit_cast(f32x2, __builtin_amdgcn_permlane32_swap(u, u, false, false));
  return fadd1(r[0], r[1]);
}
#define F4(x) __builtin_bit_cast(f32x4, x)
#define SB() __builtin_amdgcn_sched_barrier(0)
#define BF8(x) __builtin_bit_cast(bf16x8, x)
#define IC(v) std::integral_constant<int, (v)>{}

template <int I, int N, class F>
DEVI void static_for(F&& f) {
  if constexpr (I < N) {
    f(std::integral_constant<int, I>{});
    static_for<I + 1, N>(f);
  }
}

#define MFMA32(a, b, c) __builtin_amdgcn_mfma_f32_32x32x16_bf16(a, b, c, 0, 0, 0)
#define MFMA16(a, b, c) __builtin_amdgcn_mfma_f32_16x16x32_bf16(a, b, c, 0, 0, 0)

DEVI void mod_unit(const uint16_t* __restrict__ cvec, const uint16_t* __restrict__ wmod, char* __restrict__ ws, int col0,
                   int t, float* sm) {
  const int lane = t & 63, w = t >> 6, q4 = t & 3, rs = t >> 2;
  const uint16_t* mp = wmod + (size_t)rs * 2688 + col0 + q4 * 8;
  u32x4 mv[7];
  float cs[7];
#pragma unroll
  for (int i = 0; i < 7; ++i) mv[i] = *(const u32x4*)(mp + (size_t)i * 64 * 2688);
#pragma unroll
  for (int i = 0; i < 7; ++i) {
    float cv = bflo((unsigned)cvec[rs + 64 * i]);
    cs[i] = rbf(cv / (1.f + __expf(-cv)));
  }
  float acc[8];
#pragma unroll
  for (int j = 0; j < 8; ++j) acc[j] = 0.f;
#pragma unroll
  for (int i = 0; i < 7; ++i)
#pragma unroll
    for (int j = 0; j < 4; ++j) {
      acc[2 * j] += cs[i] * bflo(mv[i][j]);
      acc[2 * j + 1] += cs[i] * bfhi(mv[i][j]);
    }
#pragma unroll
  for (int j = 0; j < 8; ++j) {
    acc[j] += __shfl_xor(acc[j], 4);
    acc[j] += __shfl_xor(acc[j], 8);
    acc[j] += __shfl_xor(acc[j], 16);
    acc[j] += __shfl_xor(acc[j], 32);
  }
  if (lane < 4) {
#pragma unroll
    for (int j = 0; j < 8; ++j) sm[w * 32 + lane * 8 + j] = acc[j];
  }
  __syncthreads();
  if (t < 32) {
    const float m = rbf(sm[t] + sm[32 + t] + sm[64 + t] + sm[96 + t]);
    const int part = col0 / 448, k = col0 % 448 + t;
    if (part == 2 || part == 5) {
      ((float*)(ws + OFF_GAB))[(part == 5 ? 448 : 0) + k] = m;
    } else {
      float* dst = (float*)(ws + (part >= 3 ? OFF_MODB : OFF_MODA)) + (k >> 3) * 16 + (k & 7);
      if (part == 1 || part == 4) dst[0] = rbf(1.f + m);
      else dst[8] = m;
    }
  }
}

DEVI void retile_unit(const uint16_t* __restrict__ wsrc, char* __restrict__ ws, int role, int kr, int sub, int t) {
  const int k0 = kr * 32;
  const int koct = t / 56, coct = t % 56;
  const int ldw = role == 0 ? 1344 : 448, c0 = role == 0 ? sub * 448 : 0;
  if (t < 224) {
    u32x4 wv[8];
    const uint16_t* p = wsrc + (size_t)(k0 + koct * 8) * ldw + c0 + coct * 8;
#pragma unroll
    for (int i = 0; i < 8; ++i) wv[i] = *(const u32x4*)(p + (size_t)i * ldw);
    const int ksl = koct >> 1, half = koct & 1;
#pragma unroll
    for (int c = 0; c < 8; ++c) {
      u32x4 o;
#pragma unroll
      for (int i = 0; i < 4; ++i)
        o[i] = __builtin_amdgcn_perm(wv[2 * i + 1][c >> 1], wv[2 * i][c >> 1], (c & 1) ? 0x07060302u : 0x05040100u);
      const int n = coct * 8 + c;
      char* dst;
      if (role == 0) {
        const int head = n / 28, d = n % 28;
        const int idx = (sub < 2) ? swap23(d) : d;
        dst = ws + OFF_WQKV + ((size_t)((head * 3 + sub) * 28 + 2 * kr + ksl) * 64 + half * 32 + idx) * 16;
      } else if (role == 2) {
        dst = ws + OFF_WO + ((size_t)((n >> 5) * 28 + 2 * kr + ksl) * 64 + half * 32 + swap23(n & 31)) * 16;
      } else {
        dst = ws + OFF_WD + ((size_t)((n >> 5) * 112 + 2 * kr + ksl) * 64 + half * 32 + swap23(n & 31)) * 16;
      }
      *(u32x4*)dst = o;
    }
  } else if (role == 0) {
    const int z = t - 224;
#pragma unroll
    for (int i = 0; i < 8; ++i) {
      const int pc = z * 8 + i, head = pc >> 4, d = 28 + ((pc >> 2) & 3), ko = pc & 3;
      const int idx = (sub < 2) ? swap23(d) : d;
      char* dst = ws + OFF_WQKV + ((size_t)((head * 3 + sub) * 28 + 2 * kr + (ko >> 1)) * 64 + (ko & 1) * 32 + idx) * 16;
      *(u32x4*)dst = u32x4{0, 0, 0, 0};
    }
  }
}

#define PARG
#define PPASS
DEVI void fold_unit(const uint16_t* __restrict__ wsrc, char* __restrict__ ws, int mat, int kr, int cq, int t, char* red PARG) {
  const int ko = t >= 224, c2 = t - 224 * ko, oct = kr * 2 + ko;
  if (t < 448) {
    unsigned wv[8];
    const uint16_t* p = wsrc + (size_t)(oct * 8) * 1792 + cq * 448 + c2 * 2;
#pragma unroll
    for (int i = 0; i < 8; ++i) wv[i] = *(const unsigned*)(p + (size_t)i * 1792);
    const f32x4* tp = (const f32x4*)(ws + OFF_MODB + oct * 64);
    const f32x4 t0 = tp[0], t1 = tp[1], t2 = tp[2], t3 = tp[3];
    const float sc[8] = {t0[0], t0[1], t0[2], t0[3], t1[0], t1[1], t1[2], t1[3]};
    const float sh[8] = {t2[0], t2[1], t2[2], t2[3], t3[0], t3[1], t3[2], t3[3]};
    f32x2 pr[2];
    asm volatile("s_waitcnt vmcnt(0)\n\ts_setprio 3" ::: "memory");
    STAMP(3);
#pragma unroll
    for (int c = 0; c < 2; ++c) {
      u32x4 o;
      float bs = 0.f, cs = 0.f;
#pragma unroll
      for (int i = 0; i < 4; ++i) {
        const float a = c ? bfhi(wv[2 * i]) : bflo(wv[2 * i]);
        const float b = c ? bfhi(wv[2 * i + 1]) : bflo(wv[2 * i + 1]);
        bs += a * sh[2 * i] + b * sh[2 * i + 1];
        o[i] = pack2(a * sc[2 * i], b * sc[2 * i + 1]);
        cs += bflo(o[i]) + bfhi(o[i]);
      }
      pr[c] = f32x2{cs, bs};
      const int f = cq * 448 + c2 * 2 + c;
      *(u32x4*)(ws + OFF_WGU + ((size_t)((f >> 4) * 29 + kr) * 64 + ko * 32 + mat * 16 + swap23(f & 15)) * 16) = o;
    }
    *(f32x4*)(red + ko * 3584 + c2 * 16) = f32x4{pr[0][0], pr[0][1], pr[1][0], pr[1][1]};
  }
  STAMP(4);
  asm volatile("s_waitcnt lgkmcnt(0)\n\ts_barrier" ::: "memory");
  STAMP(6);
  if (t < 224)
    *(f32x4*)(ws + OFF_PART + ((size_t)kr * 3584 + mat * 1792 + cq * 448) * 8 + t * 16) =
        *(const f32x4*)(red + t * 16) + *(const f32x4*)(red + 3584 + t * 16);
}

__global__ __launch_bounds__(256) void k0_prep(const uint16_t* __restrict__ x, const uint16_t* __restrict__ wqkv,
                                               const uint16_t* __restrict__ cvec, const uint16_t* __restrict__ wmod,
                                               char* __restrict__ ws) {
  __shared__ __attribute__((aligned(16))) float sm[512];
  const int b = blockIdx.x, t = threadIdx.x, lane = t & 63, w = t >> 6;
  PDECL; STAMP(0);
  if (b < 72) {
    const int u = (b & 7) * 9 + (b >> 3);
    if (u < 70) mod_unit(cvec, wmod, ws, u * 32, t, sm);
  } else if (b < 136) {
    const int mt = b - 72, tok = lane & 31, half = lane >> 5;
    const uint16_t* src = x + (size_t)(mt * 32 + tok) * 448 + (7 * w) * 16 + half * 8;
    u32x4 v[7];
#pragma unroll
    for (int i = 0; i < 7; ++i) v[i] = *(const u32x4*)(src + i * 16);
    float s = 0.f;
#pragma unroll
    for (int i = 0; i < 7; ++i)
#pragma unroll
      for (int j = 0; j < 4; ++j) s += bflo(v[i][j]) + bfhi(v[i][j]);
    const float lm = s * (1.f / 56.f);
    float m2 = 0.f;
#pragma unroll
    for (int i = 0; i < 7; ++i)
#pragma unroll
      for (int j = 0; j < 4; ++j) {
        float a = bflo(v[i][j]) - lm, c = bfhi(v[i][j]) - lm;
        m2 += a * a + c * c;
      }
    f32x2* part = (f32x2*)sm;
    part[(w * 2 + half) * 32 + tok] = f32x2{lm, m2};
    __syncthreads();
    STAMP(1);
    float mu = 0.f, M2 = 0.f;
    f32x2 p[8];
#pragma unroll
    for (int i = 0; i < 8; ++i) { p[i] = part[i * 32 + tok]; mu += p[i][0]; M2 += p[i][1]; }
    mu *= 0.125f;
#pragma unroll
    for (int i = 0; i < 8; ++i) { float d = p[i][0] - mu; M2 += 56.f * d * d; }
    const float rstd = rsqrtf(M2 * (1.f / 448.f) + 1e-5f);
    char* dst = ws + OFF_XN + ((size_t)(mt * 28 + 7 * w) * 64 + lane) * 16;
#pragma unroll
    for (int i = 0; i < 7; ++i) {
      u32x4 o;
#pragma unroll
      for (int j = 0; j < 4; ++j) o[j] = pack2((bflo(v[i][j]) - mu) * rstd, (bfhi(v[i][j]) - mu) * rstd);
      *(u32x4*)(dst + i * 1024) = o;
    }
  } else {
    const int rb = b - 136;
    retile_unit(wqkv, ws, 0, rb / 3, rb % 3, t);
  }
  PFLUSH(0);
}

constexpr int k1_posW(int c) { return c == 0 ? 3 : c == 1 ? 10 : c == 2 ? 13 : 20 + 7 * (c - 3); }
constexpr int k1_posX(int k) { return (k < 4 ? 4 : k < 8 ? 10 : k < 24 ? 13 + 3 * (k / 4 - 2) : 22) + k; }
constexpr int k1_iss(int k) { return 18 + (2 * k < 32 ? 2 * k : 32); }
__global__ __launch_bounds__(256) __attribute__((amdgpu_waves_per_eu(1, 1))) void k1_qkv(char* __restrict__ ws) {
  extern __shared__ __attribute__((aligned(16))) char lds[];
  const int t = threadIdx.x, lane = t & 63, w = __builtin_amdgcn_readfirstlane(t >> 6);
  PDECL; STAMP(0);
  const int xc = blockIdx.x & 7, bi = blockIdx.x >> 3;
  const int mb = (xc & 3) * 4 + (bi & 3), head = (xc >> 2) * 8 + (bi >> 2);
  const int mt = mb * 4 + w;
  const int hi = lane >> 5, l31 = lane & 31;
  const char* A = ws + OFF_XN + (size_t)mt * 28 * 1024 + lane * 16;
  const char* B = ws + OFF_WQKV + ((size_t)head * 3 * 28 + w) * 1024 + lane * 16;
  char* modv = lds + 87 * 1024 + w * 1024;
  float* P = (float*)(lds + 91 * 1024);
  GLDS16(ws + OFF_MODA + (4 * (lane >> 3) + w) * 128 + (lane & 7) * 16, modv);
  u32x4 xr[29], sw[7][3];
  auto ldW = [&](auto cc, auto ic) {
    constexpr int c = decltype(cc)::value, i = decltype(ic)::value;
    gload16(sw[c][i], B + (size_t)(i * 28 + 4 * c) * 1024);
  };
  auto ldX = [&](auto kc) {
    constexpr int k = decltype(kc)::value;
    gload16a(xr[k], A + k * 1024);
  };
  auto ldN = [&](auto nc) {
    constexpr int n = decltype(nc)::value, g = n / 7, r = n % 7;
    if constexpr (n >= 35) { if constexpr (n < 39) ldX(IC(24 + n - 35)); }
    else if constexpr (r < 3) ldW(IC(g + 2), IC(r));
    else ldX(IC(4 * (g + 1) + r - 3));
  };
  static_for<0, 3>([&](auto i) { ldW(IC(0), i); });
  static_for<0, 4>(ldX);
  static_for<0, 3>([&](auto i) { ldW(IC(1), i); });
  f32x16 acc[3];
#pragma unroll
  for (int i = 0; i < 3; ++i)
#pragma unroll
    for (int r = 0; r < 16; ++r) acc[i][r] = 0.f;
  const unsigned wl0 = lds_addr(lds) + lane * 16, wl1 = wl0 + 49152;
  const unsigned sa0 = wl0 + w * 3072, sa1 = sa0 + 49152;
  const unsigned ml = lds_addr(modv) + hi * 64;
  u32x4 wb[3][3], mv[7][4];
  float bp[3] = {0.f, 0.f, 0.f};
  auto rdw = [&](auto kc) {
    constexpr int k = decltype(kc)::value;
    constexpr int o = (k & 15) * 3072;
    const unsigned a = (k >= 16) ? wl1 : wl0;
    lread16<o>(wb[k % 3][0], a); lread16<o + 1024>(wb[k % 3][1], a); lread16<o + 2048>(wb[k % 3][2], a);
  };
  auto rdm = [&](auto cc) {
    constexpr int c = decltype(cc)::value;
    lread16<c * 128>(mv[c][0], ml); lread16<c * 128 + 16>(mv[c][1], ml);
    lread16<c * 128 + 32>(mv[c][2], ml); lread16<c * 128 + 48>(mv[c][3], ml);
  };
  auto rdym = [&](auto cc) {
    constexpr int c = decltype(cc)::value;
    rdyv(mv[c][0]); rdyv(mv[c][1]); rdyv(mv[c][2]); rdyv(mv[c][3]);
  };
  auto word = [&](auto cc, auto qc) {
    constexpr int c = decltype(cc)::value, q = decltype(qc)::value, i = q >> 2, j = q & 3;
    const unsigned wv = sw[c][i][j];
    const float lo = bflo(wv), hh = bfhi(wv);
    const f32x4 sc = F4(mv[c][j >> 1]), sh = F4(mv[c][2 + (j >> 1)]);
    fmac1(bp[i], lo, sh[(2 * j) & 3]);
    fmac1(bp[i], hh, sh[(2 * j + 1) & 3]);
    sw[c][i][j] = pack2(fmul1(lo, sc[(2 * j) & 3]), fmul1(hh, sc[(2 * j + 1) & 3]));
    if constexpr (j == 3) lwrite16<(c & 3) * 12288 + i * 1024>(sw[c][i], c < 4 ? sa0 : sa1);
  };
  PDECL2;
  BST(0);
  vwait<7>();
  BST(8);
  rdyv(sw[0][0]); rdyv(sw[0][1]); rdyv(sw[0][2]);
  rdm(IC(0)); rdm(IC(1)); rdm(IC(2));
  ldN(IC(0));
  lwait<0>();
  static_for<0, 3>(rdym);
  static_for<0, 12>([&](auto q) {
    word(IC(0), q);
    if constexpr (q % 2 == 0) ldN(IC(1 + q / 2));
    SB();
  });
  wait_bar<7>();
  STAMP(1);
  rdya(xr[0]);
  rdyv(sw[1][0]); rdyv(sw[1][1]); rdyv(sw[1][2]);
  rdw(IC(0)); rdw(IC(1));
  lwait<3>();
  rdyv(wb[0][0]); rdyv(wb[0][1]); rdyv(wb[0][2]);
  xr[28] = u32x4{0x3f803f80u, 0, 0, 0};
  SB();
  static_for<0, 29>([&](auto kc) {
    constexpr int k = decltype(kc)::value;
    constexpr int cn = (k < 2) ? 1 : (k + 6) / 4, r = (k + 6) % 4;
    constexpr bool PR = k < 22;
    auto gap = [&](auto gc) {
      constexpr int g = decltype(gc)::value;
      if constexpr (k < 2) { word(IC(1), IC(6 * k + 2 * g)); word(IC(1), IC(6 * k + 2 * g + 1)); }
      else if constexpr (PR) word(IC(cn), IC(3 * r + g));
    };
    acc[0] = MFMA32(BF8(wb[k % 3][0]), BF8(xr[k]), acc[0]);
    if constexpr ((k + 2) % 4 == 0) {
      constexpr int j = (k + 2) / 4;
      BST(j);
      wait_bar<(j < 6) ? k1_iss(k) - 1 - k1_posW(j + 1) : 63>();
      BST(8 + j);
      if constexpr (j < 6) { rdyv(sw[j + 1][0]); rdyv(sw[j + 1][1]); rdyv(sw[j + 1][2]); }
    }
    if constexpr (k >= 2 && r == 2 && cn + 1 < 7) rdm(IC(cn + 1));
    if constexpr (k + 2 < 29) rdw(IC(k + 2));
    gap(IC(0));
    SB();
    acc[1] = MFMA32(BF8(wb[k % 3][1]), BF8(xr[k]), acc[1]);
    ldN(IC(7 + 2 * k));
    gap(IC(1));
    if constexpr (k == 23) {
      if (w < 3) {
        float bs = P[(w * 4 + 0) * 64 + lane] + P[(w * 4 + 1) * 64 + lane] + P[(w * 4 + 2) * 64 + lane] + P[(w * 4 + 3) * 64 + lane];
        bs = xhalf_sum(bs);
        const float bh = rbf(bs);
        *(u32x4*)(lds + (84 + w) * 1024 + lane * 16) = u32x4{hi ? 0u : pack2(bh, bs - bh), 0, 0, 0};
      }
    }
    SB();
    acc[2] = MFMA32(BF8(xr[k]), BF8(wb[k % 3][2]), acc[2]);
    ldN(IC(8 + 2 * k));
    gap(IC(2));
    if constexpr (k == 21) {
#pragma unroll
      for (int i = 0; i < 3; ++i) P[(i * 4 + w) * 64 + lane] = bp[i];
    }
    if constexpr (k + 1 < 29) {
      vlwait<(k + 1 < 28) ? k1_iss(k + 1) - 1 - k1_posX(k + 1) : 63, (k + 2 < 29) ? 3 : 0>();
      if constexpr (k + 1 < 28) rdya(xr[k + 1]);
      rdyv(wb[(k + 1) % 3][0]); rdyv(wb[(k + 1) % 3][1]); rdyv(wb[(k + 1) % 3][2]);
      if constexpr (k >= 2 && r == 2 && cn + 1 < 7) rdym(IC(cn + 1));
    }
    SB();
  });
  STAMP(2);
  const float qs = 0.18898223650461362f * 1.4426950408889634f;
#pragma unroll
  for (int p = 0; p < 2; ++p) {
    char* base = ws + (p ? OFF_K : OFF_Q) + ((size_t)(head * 128 + mt * 2 + (l31 >> 4)) * 64 + (l31 & 15)) * 16;
    const float sc = p ? 1.f : qs;
#pragma unroll
    for (int s = 0; s < 2; ++s) {
      u32x4 o;
#pragma unroll
      for (int j = 0; j < 4; ++j) o[j] = pack2(acc[p][8 * s + 2 * j] * sc, acc[p][8 * s + 2 * j + 1] * sc);
      *(u32x4*)(base + (2 * s + hi) * 256) = o;
    }
  }
  {
    char* base = ws + OFF_V + ((size_t)((head * 64 + mt) * 2 + (l31 >> 4)) * 64 + (l31 & 15)) * 16;
#pragma unroll
    for (int s = 0; s < 2; ++s) {
      u32x4 o = {pack2(acc[2][4 * s], acc[2][4 * s + 1]), pack2(acc[2][4 * s + 2], acc[2][4 * s + 3]),
                 pack2(acc[2][8 + 4 * s], acc[2][8 + 4 * s + 1]), pack2(acc[2][8 + 4 * s + 2], acc[2][8 + 4 * s + 3])};
      if (l31 == 28) o = u32x4{0x3f803f80u, 0x3f803f80u, 0x3f803f80u, 0x3f803f80u};
      *(u32x4*)(base + (2 * s + hi) * 256) = o;
    }
  }
  PFLUSH(1);
  PFLUSH2(6);
}

template <bool ODD>
DEVI void attn_pv(const char* vp, const u32x2 (&p)[17], f32x4& o0, f32x4& o1) {
  bf16x8 vf[9][2];
#pragma unroll
  for (int v = 0; v < 3; ++v) { vf[v][0] = *(const bf16x8*)(vp + v * 2048); vf[v][1] = *(const bf16x8*)(vp + v * 2048 + 1024); }
#pragma unroll
  for (int v = 0; v < 9; ++v) {
    if (v + 3 < 9) {
      vf[v + 3][0] = *(const bf16x8*)(vp + (v + 3) * 2048);
      vf[v + 3][1] = *(const bf16x8*)(vp + (v + 3) * 2048 + 1024);
    }
    const int i0 = 2 * v - (ODD ? 1 : 0), i1 = i0 + 1;
    const u32x2 z = {0, 0};
    const u32x2 pa = i0 >= 0 ? p[i0 >= 0 ? i0 : 0] : z, pc = i1 < 17 ? p[i1 < 17 ? i1 : 0] : z;
    const u32x4 pw = {pa[0], pa[1], pc[0], pc[1]};
    const bf16x8 pf = __builtin_bit_cast(bf16x8, pw);
    o0 = MFMA16(vf[v][0], pf, o0);
    o1 = MFMA16(vf[v][1], pf, o1);
    SB();
  }
}

DEVI void attn_wave(const char* ldsK, const char* ldsV, const char* ldsQ, int w, int lane, int nneg, char* odst, int kbase) {
  const int g = lane >> 4, qq = lane & 15;
  const bf16x8 q = *(const bf16x8*)(ldsQ + w * 1024 + lane * 16);
  const float NEG = -1e30f;
  f32x4 s[17];
  const char* kp = ldsK + w * 1024 + lane * 16;
  bf16x8 kf[17];
#pragma unroll
  for (int tt = 0; tt < 4; ++tt) kf[tt] = *(const bf16x8*)(kp + tt * 1024);
#pragma unroll
  for (int tt = 0; tt < 17; ++tt) {
    if (tt + 4 < 17) kf[tt + 4] = *(const bf16x8*)(kp + (tt + 4) * 1024);
    s[tt] = MFMA16(kf[tt], q, (f32x4{0.f, 0.f, 0.f, 0.f}));
    SB();
  }
#pragma unroll
  for (int r = 0; r < 4; ++r) {
    s[0][r] = (4 * g + r > qq) ? s[0][r] : NEG;
    s[16][r] = (4 * g + r <= qq) ? s[16][r] : NEG;
  }
  if (nneg > 0) {
#pragma unroll
    for (int tt = 0; tt < 16; ++tt)
      if (w + tt < nneg) s[tt] = f32x4{NEG, NEG, NEG, NEG};
  }
  float m = NEG;
#pragma unroll
  for (int tt = 0; tt < 17; ++tt) m = fmaxf(fmaxf(m, fmaxf(s[tt][0], s[tt][1])), fmaxf(s[tt][2], s[tt][3]));
  m = fmaxf(m, __shfl_xor(m, 16));
  m = fmaxf(m, __shfl_xor(m, 32));
  u32x2 pb[17];
#pragma unroll
  for (int tt = 0; tt < 17; ++tt) {
    const float e0 = __builtin_amdgcn_exp2f(s[tt][0] - m), e1 = __builtin_amdgcn_exp2f(s[tt][1] - m);
    const float e2 = __builtin_amdgcn_exp2f(s[tt][2] - m), e3 = __builtin_amdgcn_exp2f(s[tt][3] - m);
    pb[tt] = u32x2{pack2(e0, e1), pack2(e2, e3)};
  }
  wait_bar<0>();
  f32x4 o0 = {0.f, 0.f, 0.f, 0.f}, o1 = {0.f, 0.f, 0.f, 0.f};
  const char* vp = ldsV + (w >> 1) * 2048 + lane * 16;
  if (w & 1) attn_pv<true>(vp, pb, o0, o1);
  else attn_pv<false>(vp, pb, o0, o1);
  const float sum = __shfl(o1[0], 48 + qq);
  const float inv = 1.f / sum;
  {
    const int k = kbase + 4 * g;
    u32x2 o = {pack2(o0[0] * inv, o0[1] * inv), pack2(o0[2] * inv, o0[3] * inv)};
    *(u32x2*)(odst + (size_t)(k >> 4) * 1024 + ((k >> 3) & 1) * 512 + (k & 4) * 2) = o;
  }
  if (g < 3) {
    const int k = kbase + 16 + 4 * g;
    u32x2 o = {pack2(o1[0] * inv, o1[1] * inv), pack2(o1[2] * inv, o1[3] * inv)};
    *(u32x2*)(odst + (size_t)(k >> 4) * 1024 + ((k >> 3) & 1) * 512 + (k & 4) * 2) = o;
  }
}

__global__ __launch_bounds__(512) __attribute__((amdgpu_waves_per_eu(4, 4))) void k2_attn(
    const uint16_t* __restrict__ wo, const uint16_t* __restrict__ wg, const uint16_t* __restrict__ wu,
    char* __restrict__ ws) {
  extern __shared__ __attribute__((aligned(16))) char lds[];
  const int t = threadIdx.x, lane = t & 63, w = __builtin_amdgcn_readfirstlane(t >> 6);
  PDECL; STAMP(0);
  if (__builtin_expect(blockIdx.x >= 256, 0)) {
    const int u = blockIdx.x - 256;
    if (u < 224) fold_unit((u & 4) ? wu : wg, ws, (u >> 2) & 1, u >> 3, u & 3, t, lds PPASS);
    else if (w < 4) retile_unit(wo, ws, 2, u - 224, 0, t);
    PFLUSH(2);
    return;
  }
  const int xc = blockIdx.x & 7, bi = blockIdx.x >> 3;
  const int head = xc * 2 + (bi & 1), qb = bi >> 1;
  char* ldsK = lds;
  char* ldsV = lds + 24 * 1024;
  char* ldsQ = lds + 48 * 1024;
  const int sub0 = qb * 8 - 16;
  GLDS16(ws + OFF_Q + (size_t)(head * 128 + qb * 8 + w) * 1024 + lane * 16, ldsQ + w * 1024);
#pragma unroll
  for (int i = 0; i < 3; ++i) {
    const int p = w * 3 + i;
    const int sidx = max(sub0 + p, 0);
    GLDS16(ws + OFF_K + (size_t)(head * 128 + sidx) * 1024 + lane * 16, ldsK + p * 1024);
  }
#pragma unroll
  for (int i = 0; i < 3; ++i) {
    const int p = w * 3 + i;
    const int sidx = max(sub0 + p, 0);
    GLDS16(ws + OFF_V + (size_t)(head * 128 + sidx) * 1024 + lane * 16, ldsV + p * 1024);
  }
  wait_bar<3>();
  STAMP(1);
  const int tok = qb * 128 + w * 16 + (lane & 15);
  char* odst = ws + OFF_O + (size_t)(tok >> 5) * 28 * 1024 + (tok & 31) * 16;
  const int kbase = head * 28;
  const int nneg = -sub0;
  attn_wave(ldsK, ldsV, ldsQ, w, lane, nneg, odst, kbase);
  PFLUSH(2);
}

DEVI f32x16 ksplit_reduce(f32x16 (&acc)[4], char* lds, int w, int lane) {
#pragma unroll
  for (int tl = 0; tl < 4; ++tl) {
    if (tl != w) {
      const int slot = (w > tl) ? w - 1 : w;
      char* p = lds + (tl * 3 + slot) * 4096 + lane * 16;
#pragma unroll
      for (int q = 0; q < 4; ++q)
        *(f32x4*)(p + q * 1024) = f32x4{acc[tl][4 * q], acc[tl][4 * q + 1], acc[tl][4 * q + 2], acc[tl][4 * q + 3]};
    }
  }
  f32x16 r = (w == 0) ? acc[0] : (w == 1) ? acc[1] : (w == 2) ? acc[2] : acc[3];
  __syncthreads();
#pragma unroll
  for (int s = 0; s < 3; ++s) {
    const char* p = lds + (w * 3 + s) * 4096 + lane * 16;
#pragma unroll
    for (int q = 0; q < 4; ++q) {
      const f32x4 v = *(const f32x4*)(p + q * 1024);
#pragma unroll
      for (int j = 0; j < 4; ++j) r[4 * q + j] += v[j];
    }
  }
  return r;
}

__global__ __launch_bounds__(256) __attribute__((amdgpu_waves_per_eu(1, 1))) void k3_wo(const uint16_t* __restrict__ x,
                                                                                        char* __restrict__ ws) {
  __shared__ __attribute__((aligned(16))) char lds[48 * 1024];
  const int t = threadIdx.x, lane = t & 63, w = __builtin_amdgcn_readfirstlane(t >> 6);
  PDECL; STAMP(0);
  if (__builtin_expect(blockIdx.x >= 224, 0)) {
    if (t < 224) {
      const int z = t >= 112, col = (blockIdx.x - 224) * 112 + t - 112 * z;
      f32x2 v = {0.f, 0.f};
#pragma unroll
      for (int i = 0; i < 28; ++i) v += *(const f32x2*)(ws + OFF_PART + ((size_t)i * 3584 + col) * 8);
      const float ch = rbf(v[0]), cl = v[0] - ch, bh = rbf(v[1]), bl = v[1] - bh;
      u32x4 o = {pack2(ch, ch), pack2(cl, cl), pack2(bh, bh), pack2(bl, bl)};
      if (z) o = u32x4{0, 0, 0, 0};
      const int mat = col >= 1792, f = col - 1792 * mat;
      *(u32x4*)(ws + OFF_WGU + ((size_t)((f >> 4) * 29 + 28) * 64 + z * 32 + mat * 16 + swap23(f & 15)) * 16) = o;
    }
    PFLUSH(3);
    return;
  }
  const int xc = blockIdx.x & 7, bi = blockIdx.x >> 3;
  const int mb = xc * 4 + (bi & 3), nb = bi >> 2;
  const char* A = ws + OFF_O + ((size_t)mb * 2 * 28 + 7 * w) * 1024 + lane * 16;
  const char* B = ws + OFF_WO + ((size_t)nb * 2 * 28 + 7 * w) * 1024 + lane * 16;
  const int wm = w & 1, wn = w >> 1;
  const int hi = lane >> 5, l31 = lane & 31;
  const int mt = mb * 2 + wm, nt = nb * 2 + wn;
  bf16x8 a[7][2], bw[7][2];
#pragma unroll
  for (int k = 0; k < 7; ++k) {
    a[k][0] = *(const bf16x8*)(A + k * 1024);
    bw[k][0] = *(const bf16x8*)(B + k * 1024);
    a[k][1] = *(const bf16x8*)(A + (28 + k) * 1024);
    bw[k][1] = *(const bf16x8*)(B + (28 + k) * 1024);
  }
  u32x4 xv[2];
  f32x4 gv[4];
  {
    const uint16_t* xs = x + (size_t)(mt * 32 + l31) * 448 + nt * 32 + hi * 8;
    xv[0] = *(const u32x4*)xs;
    xv[1] = *(const u32x4*)(xs + 16);
    const float* gp = (const float*)(ws + OFF_GAB) + nt * 32 + hi * 8;
    gv[0] = *(const f32x4*)gp; gv[1] = *(const f32x4*)(gp + 4);
    gv[2] = *(const f32x4*)(gp + 16); gv[3] = *(const f32x4*)(gp + 20);
  }
  __builtin_amdgcn_sched_barrier(0);
  f32x16 acc[4];
#pragma unroll
  for (int i = 0; i < 4; ++i)
#pragma unroll
    for (int r = 0; r < 16; ++r) acc[i][r] = 0.f;
#pragma unroll
  for (int k = 0; k < 7; ++k) {
    acc[0] = MFMA32(bw[k][0], a[k][0], acc[0]);
    acc[1] = MFMA32(bw[k][0], a[k][1], acc[1]);
    acc[2] = MFMA32(bw[k][1], a[k][0], acc[2]);
    acc[3] = MFMA32(bw[k][1], a[k][1], acc[3]);
  }
  STAMP(1);
  const f32x16 r = ksplit_reduce(acc, lds, w, lane);
  STAMP(2);
  float v[16];
#pragma unroll
  for (int s = 0; s < 2; ++s) {
    u32x4 o;
#pragma unroll
    for (int j = 0; j < 4; ++j) {
      const float ga0 = gv[2 * s + (j >> 1)][(2 * j) & 3], ga1 = gv[2 * s + (j >> 1)][(2 * j + 1) & 3];
      o[j] = pack2(bflo(xv[s][j]) + ga0 * r[8 * s + 2 * j], bfhi(xv[s][j]) + ga1 * r[8 * s + 2 * j + 1]);
      v[8 * s + 2 * j] = bflo(o[j]);
      v[8 * s + 2 * j + 1] = bfhi(o[j]);
    }
    *(u32x4*)(ws + OFF_X1 + ((size_t)(mt * 28 + nt * 2 + s) * 64 + lane) * 16) = o;
  }
  float sm = 0.f;
#pragma unroll
  for (int j = 0; j < 16; ++j) sm += v[j];
  sm += __shfl_xor(sm, 32);
  const float mean = sm * (1.f / 32.f);
  float m2 = 0.f;
#pragma unroll
  for (int j = 0; j < 16; ++j) { const float d = v[j] - mean; m2 += d * d; }
  m2 += __shfl_xor(m2, 32);
  if (hi == 0) *(f32x2*)(ws + OFF_STATS + ((size_t)(mt * 8 + nb) * 32 + l31) * 16 + wn * 8) = f32x2{mean, m2};
  PFLUSH(3);
}

constexpr int k4_posG(int g) { return g < 2 ? g : g < 4 ? g + 2 : g <= 9 ? 8 + 3 * (g - 4) : g == 10 ? 30 : 4 + 3 * g; }
constexpr int k4_posX(int s) { return s == 0 ? 3 : s == 1 ? 7 : s <= 6 ? 10 + 3 * (s - 2) : s == 7 ? 27 : s == 8 ? 34 : s < 27 ? 12 + 3 * s : 92; }
constexpr int k4_iss(int k) { return k == 0 ? 23 : k == 1 ? 30 : k <= 20 ? 31 + 3 * k : 93; }
__global__ __launch_bounds__(256) __attribute__((amdgpu_waves_per_eu(1, 1))) void k4_gu(
    const uint16_t* __restrict__ wd, const uint16_t* __restrict__ cvec, const uint16_t* __restrict__ wmod,
    char* __restrict__ ws) {
  extern __shared__ __attribute__((aligned(16))) char lds[];
  const int t = threadIdx.x, lane = t & 63, w = __builtin_amdgcn_readfirstlane(t >> 6);
  PDECL; STAMP(0);
  if (__builtin_expect(blockIdx.x >= 224, 0)) {
    const int j = blockIdx.x - 224;
    u32x4 wv[2][8];
#pragma unroll
    for (int h = 0; h < 2; ++h) {
      const int id = min(t + 256 * h, 391);
      const uint16_t* p = wd + (size_t)((j * 7 + id / 56) * 8) * 448 + (id % 56) * 8;
#pragma unroll
      for (int i = 0; i < 8; ++i) wv[h][i] = *(const u32x4*)(p + (size_t)i * 448);
    }
    const int v = (j & 7) * 2 + (j >> 3);
    if (j < 16 && v < 14) mod_unit(cvec, wmod, ws, 5 * 448 + v * 32, t, (float*)lds);
#pragma unroll
    for (int h = 0; h < 2; ++h) {
      const int id = t + 256 * h, ko = j * 7 + id / 56, coct = id % 56;
      if (id < 392) {
#pragma unroll
        for (int c = 0; c < 8; ++c) {
          u32x4 o;
#pragma unroll
          for (int i = 0; i < 4; ++i)
            o[i] = __builtin_amdgcn_perm(wv[h][2 * i + 1][c >> 1], wv[h][2 * i][c >> 1], (c & 1) ? 0x07060302u : 0x05040100u);
          const int n = coct * 8 + c;
          *(u32x4*)(ws + OFF_WD + ((size_t)((n >> 5) * 112 + (ko >> 1)) * 64 + (ko & 1) * 32 + swap23(n & 31)) * 16) = o;
        }
      }
    }
    PFLUSH(4);
    return;
  }
  const int xc = blockIdx.x & 7, bi = blockIdx.x >> 3;
  const int mb = (xc & 1) * 4 + (bi & 3), fb = (xc >> 1) * 7 + (bi >> 2);
  const int mt = mb * 8 + 2 * w;
  const char* A = ws + OFF_X1 + (size_t)mt * 28 * 1024 + lane * 16;
  const char* B = ws + OFF_WGU + (size_t)(fb * 4 + w) * 29 * 1024 + lane * 16;
  const int hi = lane >> 5, l31 = lane & 31;
  u32x4 xr[29][2], st[2][4];
  auto ldG = [&](auto gc) {
    constexpr int g = decltype(gc)::value;
    GLDS16(B + g * 1024, lds + (g * 4 + w) * 1024);
  };
  auto ldXa = [&](auto sc) { constexpr int s = decltype(sc)::value; gload16a(xr[s][0], A + s * 1024); };
  auto ldXb = [&](auto sc) { constexpr int s = decltype(sc)::value; gload16a(xr[s][1], A + (28 + s) * 1024); };
  ldG(IC(0)); ldG(IC(1)); ldXa(IC(0)); ldXb(IC(0)); ldG(IC(2)); ldG(IC(3)); ldXa(IC(1)); ldXb(IC(1));
  const char* S = ws + OFF_STATS + (size_t)mt * 4096 + (hi * 4 * 32 + l31) * 16;
  static_for<2, 7>([&](auto sc) { ldG(IC(sc + 2)); ldXa(sc); ldXb(sc); });
  f32x16 acc[2][4];
#pragma unroll
  for (int i = 0; i < 8; ++i)
#pragma unroll
    for (int r = 0; r < 16; ++r) acc[i >> 2][i & 3][r] = 0.f;
  const unsigned wa0 = lds_addr(lds) + lane * 16, wa1 = wa0 + 65536;
  u32x4 wb[3][4];
  float rstd[2], var[2], ta[4], tb[4], d[8], mu;
  f32x4 p[4];
  auto stat = [&](auto tc, auto sc) {
    constexpr int tt = decltype(tc)::value, q = decltype(sc)::value;
    if constexpr (q == 0) {
#pragma unroll
      for (int i = 0; i < 4; ++i) p[i] = F4(st[tt][i]);
      if (hi) p[3] = f32x4{0.f, 0.f, 0.f, 0.f};
    } else if constexpr (q == 1 || q == 2) {
#pragma unroll
      for (int i = 2 * q - 2; i < 2 * q; ++i) { ta[i] = fadd1(p[i][0], p[i][2]); tb[i] = fadd1(p[i][1], p[i][3]); }
    } else if constexpr (q == 3) {
      ta[0] = fadd1(ta[0], ta[1]); ta[2] = fadd1(ta[2], ta[3]); tb[0] = fadd1(tb[0], tb[1]); tb[2] = fadd1(tb[2], tb[3]);
    } else if constexpr (q == 4) {
      ta[0] = fadd1(ta[0], ta[2]); tb[0] = fadd1(tb[0], tb[2]);
    } else if constexpr (q == 5) {
      ta[0] = xhalf_sum(ta[0]);
    } else if constexpr (q == 6) {
      mu = fmul1(ta[0], 1.f / 14.f);
      if (hi) { p[3][0] = mu; p[3][2] = mu; }
    } else if constexpr (q == 7 || q == 8) {
#pragma unroll
      for (int i = 2 * q - 14; i < 2 * q - 12; ++i) { d[2 * i] = fsub1(p[i][0], mu); d[2 * i + 1] = fsub1(p[i][2], mu); }
    } else if constexpr (q == 9) {
#pragma unroll
      for (int i = 0; i < 4; ++i) ta[i] = fmul1(d[i], d[i]);
    } else if constexpr (q == 10) {
#pragma unroll
      for (int i = 0; i < 4; ++i) fmac1(ta[i], d[4 + i], d[4 + i]);
    } else if constexpr (q == 11) {
      ta[0] = fadd1(ta[0], ta[1]); ta[2] = fadd1(ta[2], ta[3]);
      ta[1] = rbf(mu);
    } else if constexpr (q == 12) {
      fmac1(tb[0], fadd1(ta[0], ta[2]), 32.f);
    } else if constexpr (q == 13) {
      tb[0] = xhalf_sum(tb[0]);
    } else if constexpr (q == 14) {
      var[tt] = fmaf(tb[0], 1.f / 448.f, 1e-5f);
      rstd[tt] = __builtin_amdgcn_rsqf(var[tt]);
    } else if constexpr (q == 15) {
      ta[0] = fmul1(var[tt], rstd[tt]);
      ta[3] = fsub1(ta[1], mu);
    } else if constexpr (q == 16) {
      ta[2] = rbf(ta[0]);
    } else if constexpr (q == 17) {
      const unsigned pm = pack2(-ta[1], ta[3]), ps = pack2(ta[2], fsub1(ta[0], ta[2]));
      xr[28][tt] = u32x4{pm, pm, ps, ps};
    }
  };
  auto rdw = [&](auto kc) {
    constexpr int k = decltype(kc)::value;
    constexpr int o = (k & 15) * 4096;
    const unsigned a = (k >= 16) ? wa1 : wa0;
    lread16<o>(wb[k % 3][0], a); lread16<o + 1024>(wb[k % 3][1], a);
    lread16<o + 2048>(wb[k % 3][2], a); lread16<o + 3072>(wb[k % 3][3], a);
  };
  PDECL2;
  BST(0);
  vbar<21>();
  BST(8);
  STAMP(1);
  rdw(IC(0)); rdw(IC(1));
  vlwait<19, 4>();
  rdya(xr[0][0]); rdya(xr[0][1]);
  rdyv(wb[0][0]); rdyv(wb[0][1]); rdyv(wb[0][2]); rdyv(wb[0][3]);
  SB();
  static_for<0, 29>([&](auto kc) {
    constexpr int k = decltype(kc)::value;
    static_for<0, 8>([&](auto gc) {
      constexpr int g = decltype(gc)::value;
      acc[g & 1][g >> 1] = MFMA32(BF8(wb[k % 3][g >> 1]), BF8(xr[k][g & 1]), acc[g & 1][g >> 1]);
      if constexpr (g == 0) {
        if constexpr (k % 2 == 0 && k + 2 < 29) {
          if constexpr (k % 4 == 0) BST(1 + k / 4);
          vbar<k4_iss(k) - 1 - k4_posG(k + 3 < 29 ? k + 3 : 28)>();
          if constexpr (k % 4 == 0) BST(9 + k / 4);
        }
        if constexpr (k + 2 < 29) rdw(IC(k + 2));
      }
      if constexpr (g == 1 && k + 9 < 29) ldG(IC(k + 9));
      if constexpr (g == 3 && k + 7 < 28) ldXa(IC(k + 7));
      if constexpr (g == 5 && k + 7 < 28) ldXb(IC(k + 7));
      if constexpr (k < 2 && (g == 2 || g == 4 || g >= 6)) gload16(st[k & 1][g == 7 ? 3 : g / 2 - 1], S + k * 4096 + (g == 7 ? 3 : g / 2 - 1) * 512);
      if constexpr (k >= 9 && k < 14) stat(IC((((k - 9) * 8 + g) / 20) & 1), IC(((k - 9) * 8 + g) % 20));
      if constexpr (g == 7 && k + 1 < 29) {
        vlwait<(k + 1 < 28) ? k4_iss(k + 1) - 1 - k4_posX(k + 1) : 63, (k + 2 < 29) ? 4 : 0>();
        if constexpr (k + 1 < 28) { rdya(xr[k + 1][0]); rdya(xr[k + 1][1]); }
        rdyv(wb[(k + 1) % 3][0]); rdyv(wb[(k + 1) % 3][1]); rdyv(wb[(k + 1) % 3][2]); rdyv(wb[(k + 1) % 3][3]);
        if constexpr (k == 8) {
#pragma unroll
          for (int i = 0; i < 8; ++i) rdyv(st[i >> 2][i & 3]);
        }
      }
      SB();
    });
  });
  STAMP(2);
  const float c0 = -2.f * 0.7978845608028654f * 1.4426950408889634f, c1 = c0 * 0.044715f;
  char* hdst = ws + OFF_HFF + ((size_t)(mt * 112 + fb * 4) * 64 + lane) * 16;
#pragma unroll
  for (int tt = 0; tt < 2; ++tt)
#pragma unroll
    for (int n = 0; n < 4; ++n) {
      const float e0 = c0 * rstd[tt], e1 = c1 * rstd[tt] * rstd[tt] * rstd[tt];
      float hh[8];
#pragma unroll
      for (int j = 0; j < 8; ++j) {
        const float g = acc[tt][n][j], u = acc[tt][n][8 + j];
        const float e = __builtin_amdgcn_exp2f(g * fmaf(g * g, e1, e0));
        hh[j] = g * u * __builtin_amdgcn_rcpf(fmaf(e, var[tt], var[tt]));
      }
      u32x4 o = {pack2(hh[0], hh[1]), pack2(hh[2], hh[3]), pack2(hh[4], hh[5]), pack2(hh[6], hh[7])};
      *(u32x4*)(hdst + (tt * 112 + n) * 1024) = o;
    }
  PFLUSH(4);
  PFLUSH2(8);
}

__global__ __launch_bounds__(256) __attribute__((amdgpu_waves_per_eu(1, 1))) void k5_down(char* __restrict__ ws,
                                                                                          uint16_t* __restrict__ out) {
  __shared__ __attribute__((aligned(16))) char lds[48 * 1024];
  const int t = threadIdx.x, lane = t & 63, w = __builtin_amdgcn_readfirstlane(t >> 6);
  PDECL; STAMP(0);
  const int xc = blockIdx.x & 7, bi = blockIdx.x >> 3;
  const int mb = xc * 4 + (bi & 3), nb = bi >> 2;
  const char* A = ws + OFF_HFF + ((size_t)mb * 2 * 112 + 28 * w) * 1024 + lane * 16;
  const char* B = ws + OFF_WD + ((size_t)nb * 2 * 112 + 28 * w) * 1024 + lane * 16;
  const int wm = w & 1, wn = w >> 1;
  const int hi = lane >> 5, l31 = lane & 31;
  const int mt = mb * 2 + wm, nt = nb * 2 + wn;
  constexpr int D = 12;
  bf16x8 a[D][2], bw[D][2];
  auto ld = [&](int k) {
    a[k % D][0] = *(const bf16x8*)(A + k * 1024);
    bw[k % D][0] = *(const bf16x8*)(B + k * 1024);
    a[k % D][1] = *(const bf16x8*)(A + (112 + k) * 1024);
    bw[k % D][1] = *(const bf16x8*)(B + (112 + k) * 1024);
  };
#pragma unroll
  for (int k = 0; k < D; ++k) ld(k);
  u32x4 xv[2];
  f32x4 gv[4];
  f32x16 acc[4];
#pragma unroll
  for (int i = 0; i < 4; ++i)
#pragma unroll
    for (int r = 0; r < 16; ++r) acc[i][r] = 0.f;
  __builtin_amdgcn_sched_barrier(0);
#pragma unroll
  for (int k = 0; k < 28; ++k) {
    acc[0] = MFMA32(bw[k % D][0], a[k % D][0], acc[0]);
    acc[1] = MFMA32(bw[k % D][0], a[k % D][1], acc[1]);
    acc[2] = MFMA32(bw[k % D][1], a[k % D][0], acc[2]);
    acc[3] = MFMA32(bw[k % D][1], a[k % D][1], acc[3]);
    if (k + D < 28) ld(k + D);
    if (k + D == 28) {
      const char* xs = ws + OFF_X1 + ((size_t)(mt * 28 + nt * 2) * 64 + lane) * 16;
      xv[0] = *(const u32x4*)xs;
      xv[1] = *(const u32x4*)(xs + 1024);
      const float* gp = (const float*)(ws + OFF_GAB) + 448 + nt * 32 + hi * 8;
      gv[0] = *(const f32x4*)gp; gv[1] = *(const f32x4*)(gp + 4);
      gv[2] = *(const f32x4*)(gp + 16); gv[3] = *(const f32x4*)(gp + 20);
    }
    __builtin_amdgcn_sched_barrier(0);
  }
  STAMP(1);
  const f32x16 r = ksplit_reduce(acc, lds, w, lane);
  STAMP(2);
#pragma unroll
  for (int s = 0; s < 2; ++s) {
    u32x4 o;
#pragma unroll
    for (int j = 0; j < 4; ++j) {
      const float ga0 = gv[2 * s + (j >> 1)][(2 * j) & 3], ga1 = gv[2 * s + (j >> 1)][(2 * j + 1) & 3];
      o[j] = pack2(bflo(xv[s][j]) + ga0 * r[8 * s + 2 * j], bfhi(xv[s][j]) + ga1 * r[8 * s + 2 * j + 1]);
    }
    *(u32x4*)(out + (size_t)(mt * 32 + l31) * 448 + nt * 32 + s * 16 + hi * 8) = o;
  }
  PFLUSH(5);
}

constexpr int LDS_K1 = 94 * 1024;
constexpr int LDS_K2 = 56 * 1024;
constexpr int LDS_K4 = 116 * 1024;

extern "C" void kernel_launch(void* const* d_in, const int* in_sizes, int n_in, void* d_out, int out_size, void* d_ws,
                              size_t ws_size, hipStream_t stream) {
  const uint16_t* x = (const uint16_t*)d_in[0];
  const uint16_t* wqkv = (const uint16_t*)d_in[1];
  const uint16_t* wo = (const uint16_t*)d_in[2];
  const uint16_t* wg = (const uint16_t*)d_in[4];
  const uint16_t* wu = (const uint16_t*)d_in[5];
  const uint16_t* wd = (const uint16_t*)d_in[6];
  const uint16_t* cv = (const uint16_t*)d_in[7];
  const uint16_t* wmod = (const uint16_t*)d_in[8];
  char* ws = (char*)d_ws;
  (void)hipFuncSetAttribute((const void*)k1_qkv, hipFuncAttributeMaxDynamicSharedMemorySize, LDS_K1);
  (void)hipFuncSetAttribute((const void*)k2_attn, hipFuncAttributeMaxDynamicSharedMemorySize, LDS_K2);
  (void)hipFuncSetAttribute((const void*)k4_gu, hipFuncAttributeMaxDynamicSharedMemorySize, LDS_K4);
  k0_prep<<<178, 256, 0, stream>>>(x, wqkv, cv, wmod, ws);
  k1_qkv<<<256, 256, LDS_K1, stream>>>(ws);
  k2_attn<<<494, 512, LDS_K2, stream>>>(wo, wg, wu, ws);
  k3_wo<<<256, 256, 0, stream>>>(x, ws);
  k4_gu<<<256, 256, LDS_K4, stream>>>(wd, cv, wmod, ws);
  k5_down<<<224, 256, 0, stream>>>(ws, (uint16_t*)d_out);
}
```

```cpp
#include <hip/hip_runtime.h>
#include <stdint.h>

typedef __attribute__((ext_vector_type(8))) short bf16x8;
typedef __attribute__((ext_vector_type(16))) float f32x16;
typedef __attribute__((ext_vector_type(4))) float f32x4;
typedef __attribute__((ext_vector_type(2))) float f32x2;
typedef __attribute__((ext_vector_type(2))) __bf16 bf16x2_t;
typedef __attribute__((ext_vector_type(4))) unsigned u32x4;
typedef __attribute__((ext_vector_type(2))) unsigned u32x2;

#define DEVI __device__ __forceinline__

DEVI unsigned pack2(float a, float b) {
  f32x2 v = {a, b};
  bf16x2_t r = __builtin_convertvector(v, bf16x2_t);
  return __builtin_bit_cast(unsigned, r);
}
DEVI float bflo(unsigned w) { return __builtin_bit_cast(float, w << 16); }
DEVI float bfhi(unsigned w) { return __builtin_bit_cast(float, w & 0xffff0000u); }
DEVI float rbf(float a) { return bflo(pack2(a, a)); }
DEVI int swap23(int d) { return (d & ~12) | ((d & 4) << 1) | ((d & 8) >> 1); }

constexpr size_t OFF_XN = 0;
constexpr size_t OFF_WQKV = OFF_XN + 64 * 28 * 1024;
constexpr size_t OFF_WO = OFF_WQKV + 48 * 28 * 1024;
constexpr size_t OFF_WGU = OFF_WO + 14 * 28 * 1024;
constexpr size_t OFF_WD = OFF_WGU + 112 * 29 * 1024;
constexpr size_t OFF_Q = OFF_WD + 14 * 112 * 1024;
constexpr size_t OFF_K = OFF_Q + 16 * 128 * 1024;
constexpr size_t OFF_V = OFF_K + 16 * 128 * 1024;
constexpr size_t OFF_O = OFF_V + 16 * 128 * 1024;
constexpr size_t OFF_X1 = OFF_O + 64 * 28 * 1024;
constexpr size_t OFF_HFF = OFF_X1 + 64 * 28 * 1024;
constexpr size_t OFF_STATS = OFF_HFF + 64 * 112 * 1024;
constexpr size_t OFF_MODA = OFF_STATS + 64 * 4096;
constexpr size_t OFF_MODB = OFF_MODA + 4096;
constexpr size_t OFF_GAB = OFF_MODB + 4096;
constexpr size_t OFF_PART = OFF_GAB + 4096;
constexpr size_t WS_END = OFF_PART + 28 * 3584 * 8;

#define PDECL
#define STAMP(i)
#define PFLUSH(kid)
#define PDECL2
#define BST(i)
#define PFLUSH2(kid)
#define GLDS16(src, dst) __builtin_amdgcn_global_load_lds((const unsigned*)(src), (unsigned*)(dst), 16, 0, 0)
#define GLDS4(src, dst) __builtin_amdgcn_global_load_lds((const unsigned*)(src), (unsigned*)(dst), 4, 0, 0)

template <int N>
DEVI void wait_bar() {
  asm volatile("s_waitcnt vmcnt(%0) lgkmcnt(0)\n\ts_barrier" ::"n"(N) : "memory");
}
template <int N>
DEVI void wait_bar4(u32x4& a, u32x4& b, u32x4& c, u32x4& d) {
  asm volatile("s_waitcnt vmcnt(%4) lgkmcnt(0)\n\ts_barrier" : "+v"(a), "+v"(b), "+v"(c), "+v"(d) : "n"(N) : "memory");
}
DEVI void gload16(u32x4& r, const char* p) {
  asm volatile("global_load_dwordx4 %0, %1, off" : "=v"(r) : "v"(p) : "memory");
}

typedef __attribute__((address_space(3))) char lds_char;
DEVI unsigned lds_addr(const char* p) { return (unsigned)(size_t)(lds_char*)p; }
template <int OFF>
DEVI void lread16(u32x4& r, unsigned a) {
  asm volatile("ds_read_b128 %0, %1 offset:%2" : "=v"(r) : "v"(a), "n"(OFF) : "memory");
}
template <int OFF>
DEVI void lwrite16(const u32x4& r, unsigned a) {
  asm volatile("ds_write_b128 %1, %0 offset:%2" ::"v"(r), "v"(a), "n"(OFF) : "memory");
}
template <int N>
DEVI void lwait() { asm volatile("s_waitcnt lgkmcnt(%0)" ::"n"(N) : "memory"); }
template <int N>
DEVI void vwait() { asm volatile("s_waitcnt vmcnt(%0)" ::"n"(N) : "memory"); }
DEVI void rdyv(u32x4& a) { asm volatile("" : "+v"(a)); }
DEVI void rdya(u32x4& a) { asm volatile("" : "+a"(a)); }
DEVI void gload16a(u32x4& r, const char* p) {
  asm volatile("global_load_dwordx4 %0, %1, off" : "=a"(r) : "v"(p) : "memory");
}
template <int OFF>
DEVI void gload16o(u32x4& r, const char* p) {
  asm volatile("global_load_dwordx4 %0, %1, off offset:%2" : "=v"(r) : "v"(p), "n"(OFF) : "memory");
}
template <int OFF>
DEVI void gload16ao(u32x4& r, const char* p) {
  asm volatile("global_load_dwordx4 %0, %1, off offset:%2" : "=a"(r) : "v"(p), "n"(OFF) : "memory");
}
DEVI unsigned scale2(unsigned w, float s0, float s1) {
  unsigned r; float t0, t1;
  asm("v_lshlrev_b32 %1, 16, %3\n\tv_and_b32 %2, 0xffff0000, %3\n\tv_mul_f32 %1, %1, %4\n\tv_mul_f32 %2, %2, %5\n\t"
      "v_cvt_pk_bf16_f32 %0, %1, %2" : "=v"(r), "=&v"(t0), "=&v"(t1) : "v"(w), "v"(s0), "v"(s1));
  return r;
}
DEVI float dot2(unsigned a, unsigned b, float c) {
  return __builtin_amdgcn_fdot2_f32_bf16(__builtin_bit_cast(bf16x2_t, a), __builtin_bit_cast(bf16x2_t, b), c, false);
}
DEVI float fmul1(float a, float b) { float r; asm("v_mul_f32 %0, %1, %2" : "=v"(r) : "v"(a), "v"(b)); return r; }
DEVI float fadd1(float a, float b) { float r; asm("v_add_f32 %0, %1, %2" : "=v"(r) : "v"(a), "v"(b)); return r; }
DEVI float fsub1(float a, float b) { float r; asm("v_sub_f32 %0, %1, %2" : "=v"(r) : "v"(a), "v"(b)); return r; }
DEVI void fmac1(float& c, float a, float b) { asm("v_fmac_f32 %0, %1, %2" : "+v"(c) : "v"(a), "v"(b)); }
template <int NV, int NL>
DEVI void vlwait() { asm volatile("s_waitcnt vmcnt(%0) lgkmcnt(%1)" ::"n"(NV), "n"(NL) : "memory"); }
template <int N>
DEVI void vbar() { asm volatile("s_waitcnt vmcnt(%0)\n\ts_barrier" ::"n"(N) : "memory"); }
DEVI float xhalf_sum(float v) {
  const unsigned u = __builtin_bit_cast(unsigned, v);
  const f32x2 r = __builtin_bit_cast(f32x2, __builtin_amdgcn_permlane32_swap(u, u, false, false));
  return fadd1(r[0], r[1]);
}
#define F4(x) __builtin_bit_cast(f32x4, x)
#define SB() __builtin_amdgcn_sched_barrier(0)
#define BF8(x) __builtin_bit_cast(bf16x8, x)
#define IC(v) std::integral_constant<int, (v)>{}

template <int I, int N, class F>
DEVI void static_for(F&& f) {
  if constexpr (I < N) {
    f(std::integral_constant<int, I>{});
    static_for<I + 1, N>(f);
  }
}

#define MFMA32(a, b, c) __builtin_amdgcn_mfma_f32_32x32x16_bf16(a, b, c, 0, 0, 0)
#define MFMA16(a, b, c) __builtin_amdgcn_mfma_f32_16x16x32_bf16(a, b, c, 0, 0, 0)

DEVI void mod_unit(const uint16_t* __restrict__ cvec, const uint16_t* __restrict__ wmod, char* __restrict__ ws, int col0,
                   int t, float* sm) {
  const int lane = t & 63, w = t >> 6, q4 = t & 3, rs = t >> 2;
  const uint16_t* mp = wmod + (size_t)rs * 2688 + col0 + q4 * 8;
  u32x4 mv[7];
  float cs[7];
#pragma unroll
  for (int i = 0; i < 7; ++i) mv[i] = *(const u32x4*)(mp + (size_t)i * 64 * 2688);
#pragma unroll
  for (int i = 0; i < 7; ++i) {
    float cv = bflo((unsigned)cvec[rs + 64 * i]);
    cs[i] = rbf(cv / (1.f + __expf(-cv)));
  }
  float acc[8];
#pragma unroll
  for (int j = 0; j < 8; ++j) acc[j] = 0.f;
#pragma unroll
  for (int i = 0; i < 7; ++i)
#pragma unroll
    for (int j = 0; j < 4; ++j) {
      acc[2 * j] += cs[i] * bflo(mv[i][j]);
      acc[2 * j + 1] += cs[i] * bfhi(mv[i][j]);
    }
#pragma unroll
  for (int j = 0; j < 8; ++j) {
    acc[j] += __shfl_xor(acc[j], 4);
    acc[j] += __shfl_xor(acc[j], 8);
    acc[j] += __shfl_xor(acc[j], 16);
    acc[j] += __shfl_xor(acc[j], 32);
  }
  if (lane < 4) {
#pragma unroll
    for (int j = 0; j < 8; ++j) sm[w * 32 + lane * 8 + j] = acc[j];
  }
  __syncthreads();
  if (t < 32) {
    const float m = rbf(sm[t] + sm[32 + t] + sm[64 + t] + sm[96 + t]);
    const int part = col0 / 448, k = col0 % 448 + t;
    if (part == 2 || part == 5) {
      ((float*)(ws + OFF_GAB))[(part == 5 ? 448 : 0) + k] = m;
    } else {
      float* dst = (float*)(ws + (part >= 3 ? OFF_MODB : OFF_MODA)) + (k >> 3) * 16 + (k & 7);
      if (part == 1 || part == 4) dst[0] = rbf(1.f + m);
      else if (part == 3) dst[8] = m;
      else ((uint16_t*)(ws + OFF_MODA + (k >> 3) * 64 + 32))[k & 7] = (uint16_t)pack2(m, m);
    }
  }
}

DEVI void retile_unit(const uint16_t* __restrict__ wsrc, char* __restrict__ ws, int role, int kr, int sub, int t) {
  const int k0 = kr * 32;
  const int koct = t / 56, coct = t % 56;
  const int ldw = role == 0 ? 1344 : 448, c0 = role == 0 ? sub * 448 : 0;
  if (t < 224) {
    u32x4 wv[8];
    const uint16_t* p = wsrc + (size_t)(k0 + koct * 8) * ldw + c0 + coct * 8;
#pragma unroll
    for (int i = 0; i < 8; ++i) wv[i] = *(const u32x4*)(p + (size_t)i * ldw);
    const int ksl = koct >> 1, half = koct & 1;
#pragma unroll
    for (int c = 0; c < 8; ++c) {
      u32x4 o;
#pragma unroll
      for (int i = 0; i < 4; ++i)
        o[i] = __builtin_amdgcn_perm(wv[2 * i + 1][c >> 1], wv[2 * i][c >> 1], (c & 1) ? 0x07060302u : 0x05040100u);
      const int n = coct * 8 + c;
      char* dst;
      if (role == 0) {
        const int head = n / 28, d = n % 28;
        const int idx = (sub < 2) ? swap23(d) : d;
        dst = ws + OFF_WQKV + ((size_t)(head * 84 + ((2 * kr + ksl) & 3) * 21 + (kr >> 1) * 3 + sub) * 64 + half * 32 + idx) * 16;
      } else if (role == 2) {
        dst = ws + OFF_WO + ((size_t)((n >> 5) * 28 + 2 * kr + ksl) * 64 + half * 32 + swap23(n & 31)) * 16;
      } else {
        dst = ws + OFF_WD + ((size_t)((n >> 5) * 112 + 2 * kr + ksl) * 64 + half * 32 + swap23(n & 31)) * 16;
      }
      *(u32x4*)dst = o;
    }
  } else if (role == 0) {
    const int z = t - 224;
#pragma unroll
    for (int i = 0; i < 8; ++i) {
      const int pc = z * 8 + i, head = pc >> 4, d = 28 + ((pc >> 2) & 3), ko = pc & 3;
      const int idx = (sub < 2) ? swap23(d) : d;
      char* dst = ws + OFF_WQKV + ((size_t)(head * 84 + ((2 * kr + (ko >> 1)) & 3) * 21 + (kr >> 1) * 3 + sub) * 64 + (ko & 1) * 32 + idx) * 16;
      *(u32x4*)dst = u32x4{0, 0, 0, 0};
    }
  }
}

#define PARG
#define PPASS
DEVI void fold_unit(const uint16_t* __restrict__ wsrc, char* __restrict__ ws, int mat, int kr, int cq, int t, char* red PARG) {
  const int ko = t >= 224, c2 = t - 224 * ko, oct = kr * 2 + ko;
  if (t < 448) {
    unsigned wv[8];
    const uint16_t* p = wsrc + (size_t)(oct * 8) * 1792 + cq * 448 + c2 * 2;
#pragma unroll
    for (int i = 0; i < 8; ++i) wv[i] = *(const unsigned*)(p + (size_t)i * 1792);
    const f32x4* tp = (const f32x4*)(ws + OFF_MODB + oct * 64);
    const f32x4 t0 = tp[0], t1 = tp[1], t2 = tp[2], t3 = tp[3];
    const float sc[8] = {t0[0], t0[1], t0[2], t0[3], t1[0], t1[1], t1[2], t1[3]};
    const float sh[8] = {t2[0], t2[1], t2[2], t2[3], t3[0], t3[1], t3[2], t3[3]};
    f32x2 pr[2];
    asm volatile("s_waitcnt vmcnt(0)\n\ts_setprio 3" ::: "memory");
    STAMP(3);
#pragma unroll
    for (int c = 0; c < 2; ++c) {
      u32x4 o;
      float bs = 0.f, cs = 0.f;
#pragma unroll
      for (int i = 0; i < 4; ++i) {
        const float a = c ? bfhi(wv[2 * i]) : bflo(wv[2 * i]);
        const float b = c ? bfhi(wv[2 * i + 1]) : bflo(wv[2 * i + 1]);
        bs += a * sh[2 * i] + b * sh[2 * i + 1];
        o[i] = pack2(a * sc[2 * i], b * sc[2 * i + 1]);
        cs += bflo(o[i]) + bfhi(o[i]);
      }
      pr[c] = f32x2{cs, bs};
      const int f = cq * 448 + c2 * 2 + c;
      *(u32x4*)(ws + OFF_WGU + ((size_t)((f >> 4) * 29 + kr) * 64 + ko * 32 + mat * 16 + swap23(f & 15)) * 16) = o;
    }
    *(f32x4*)(red + ko * 3584 + c2 * 16) = f32x4{pr[0][0], pr[0][1], pr[1][0], pr[1][1]};
  }
  STAMP(4);
  asm volatile("s_waitcnt lgkmcnt(0)\n\ts_barrier" ::: "memory");
  STAMP(6);
  if (t < 224)
    *(f32x4*)(ws + OFF_PART + ((size_t)kr * 3584 + mat * 1792 + cq * 448) * 8 + t * 16) =
        *(const f32x4*)(red + t * 16) + *(const f32x4*)(red + 3584 + t * 16);
}

__global__ __launch_bounds__(256) void k0_prep(const uint16_t* __restrict__ x, const uint16_t* __restrict__ wqkv,
                                               const uint16_t* __restrict__ cvec, const uint16_t* __restrict__ wmod,
                                               char* __restrict__ ws) {
  __shared__ __attribute__((aligned(16))) float sm[512];
  const int b = blockIdx.x, t = threadIdx.x, lane = t & 63, w = t >> 6;
  PDECL; STAMP(0);
  if (b < 72) {
    const int u = (b & 7) * 9 + (b >> 3);
    if (u < 70) mod_unit(cvec, wmod, ws, u * 32, t, sm);
  } else if (b < 136) {
    const int mt = b - 72, tok = lane & 31, half = lane >> 5;
    const uint16_t* src = x + (size_t)(mt * 32 + tok) * 448 + (7 * w) * 16 + half * 8;
    u32x4 v[7];
#pragma unroll
    for (int i = 0; i < 7; ++i) v[i] = *(const u32x4*)(src + i * 16);
    float s = 0.f;
#pragma unroll
    for (int i = 0; i < 7; ++i)
#pragma unroll
      for (int j = 0; j < 4; ++j) s += bflo(v[i][j]) + bfhi(v[i][j]);
    const float lm = s * (1.f / 56.f);
    float m2 = 0.f;
#pragma unroll
    for (int i = 0; i < 7; ++i)
#pragma unroll
      for (int j = 0; j < 4; ++j) {
        float a = bflo(v[i][j]) - lm, c = bfhi(v[i][j]) - lm;
        m2 += a * a + c * c;
      }
    f32x2* part = (f32x2*)sm;
    part[(w * 2 + half) * 32 + tok] = f32x2{lm, m2};
    __syncthreads();
    STAMP(1);
    float mu = 0.f, M2 = 0.f;
    f32x2 p[8];
#pragma unroll
    for (int i = 0; i < 8; ++i) { p[i] = part[i * 32 + tok]; mu += p[i][0]; M2 += p[i][1]; }
    mu *= 0.125f;
#pragma unroll
    for (int i = 0; i < 8; ++i) { float d = p[i][0] - mu; M2 += 56.f * d * d; }
    const float rstd = rsqrtf(M2 * (1.f / 448.f) + 1e-5f);
    char* dst = ws + OFF_XN + ((size_t)(mt * 28 + 7 * w) * 64 + lane) * 16;
#pragma unroll
    for (int i = 0; i < 7; ++i) {
      u32x4 o;
#pragma unroll
      for (int j = 0; j < 4; ++j) o[j] = pack2((bflo(v[i][j]) - mu) * rstd, (bfhi(v[i][j]) - mu) * rstd);
      *(u32x4*)(dst + i * 1024) = o;
    }
  } else {
    const int rb = b - 136;
    retile_unit(wqkv, ws, 0, rb / 3, rb % 3, t);
  }
  PFLUSH(0);
}

constexpr int k1_posW(int c) { return c == 0 ? 3 : c == 1 ? 10 : c == 2 ? 13 : 20 + 7 * (c - 3); }
constexpr int k1_posX(int k) { return (k < 4 ? 4 : k < 8 ? 10 : k < 24 ? 13 + 3 * (k / 4 - 2) : 22) + k; }
constexpr int k1_iss(int k) { return 18 + (2 * k < 32 ? 2 * k : 32); }
__global__ __launch_bounds__(256) __attribute__((amdgpu_waves_per_eu(1, 1))) void k1_qkv(char* __restrict__ ws) {
  extern __shared__ __attribute__((aligned(16))) char lds[];
  const int t = threadIdx.x, lane = t & 63, w = __builtin_amdgcn_readfirstlane(t >> 6);
  PDECL; STAMP(0);
  const int xc = blockIdx.x & 7, bi = blockIdx.x >> 3;
  const int mb = (xc & 3) * 4 + (bi & 3), head = (xc >> 2) * 8 + (bi >> 2);
  const int mt = mb * 4 + w;
  const int hi = lane >> 5, l31 = lane & 31;
  const char* A0 = ws + OFF_XN + (size_t)mt * 28 * 1024 + lane * 16 + 4096;
  const char* B0 = ws + OFF_WQKV + ((size_t)head * 84 + w * 21) * 1024 + lane * 16 + 4096;
  const char* A[4] = {A0, A0 + 8192, A0 + 16384, A0 + 24576};
  const char* B[3] = {B0, B0 + 8192, B0 + 16384};
  char* modv = lds + 87 * 1024 + w * 1024;
  float* P = (float*)(lds + 91 * 1024);
  GLDS16(ws + OFF_MODA + (4 * (lane >> 3) + w) * 128 + (lane & 7) * 16, modv);
  u32x4 xr[29], sw[7][3];
  auto ldW = [&](auto cc, auto ic) {
    constexpr int c = decltype(cc)::value, i = decltype(ic)::value;
    gload16o<((c * 3 + i) & 7) * 1024 - 4096>(sw[c][i], B[(c * 3 + i) >> 3]);
  };
  auto ldX = [&](auto kc) {
    constexpr int k = decltype(kc)::value;
    gload16ao<(k & 7) * 1024 - 4096>(xr[k], A[k >> 3]);
  };
  auto ldN = [&](auto nc) {
    constexpr int n = decltype(nc)::value, g = n / 7, r = n % 7;
    if constexpr (n >= 35) { if constexpr (n < 39) ldX(IC(24 + n - 35)); }
    else if constexpr (r < 3) ldW(IC(g + 2), IC(r));
    else ldX(IC(4 * (g + 1) + r - 3));
  };
  static_for<0, 3>([&](auto i) { ldW(IC(0), i); });
  static_for<0, 4>(ldX);
  static_for<0, 3>([&](auto i) { ldW(IC(1), i); });
  f32x16 acc[3], accb[3];
#pragma unroll
  for (int i = 0; i < 3; ++i)
#pragma unroll
    for (int r = 0; r < 16; ++r) { acc[i][r] = 0.f; accb[i][r] = 0.f; }
  const unsigned wl0 = lds_addr(lds) + lane * 16, wl1 = wl0 + 49152;
  const unsigned sa0 = wl0 + w * 3072, sa1 = sa0 + 49152;
  const unsigned ml = lds_addr(modv) + hi * 64;
  u32x4 wb[3][3], mv[7][3], so[3];
  auto rdw = [&](auto kc) {
    constexpr int k = decltype(kc)::value;
    constexpr int o = (k & 15) * 3072;
    const unsigned a = (k >= 16) ? wl1 : wl0;
    lread16<o>(wb[k % 3][0], a); lread16<o + 1024>(wb[k % 3][1], a); lread16<o + 2048>(wb[k % 3][2], a);
  };
  auto rdm = [&](auto cc) {
    constexpr int c = decltype(cc)::value;
    lread16<c * 128>(mv[c][0], ml); lread16<c * 128 + 16>(mv[c][1], ml);
    lread16<c * 128 + 32>(mv[c][2], ml);
  };
  auto rdym = [&](auto cc) {
    constexpr int c = decltype(cc)::value;
    rdyv(mv[c][0]); rdyv(mv[c][1]); rdyv(mv[c][2]);
  };
  auto word = [&](auto cc, auto qc) {
    constexpr int c = decltype(cc)::value, q = decltype(qc)::value, i = q >> 2, j = q & 3;
    if constexpr (j == 0) accb[i] = MFMA32(BF8(mv[c][2]), BF8(sw[c][i]), accb[i]);
    const f32x4 sc = F4(mv[c][j >> 1]);
    so[i][j] = scale2(sw[c][i][j], sc[(2 * j) & 3], sc[(2 * j + 1) & 3]);
    if constexpr (j == 3) lwrite16<(c & 3) * 12288 + i * 1024>(so[i], c < 4 ? sa0 : sa1);
  };
  PDECL2;
  BST(0);
  vwait<7>();
  BST(8);
  rdyv(sw[0][0]); rdyv(sw[0][1]); rdyv(sw[0][2]);
  rdm(IC(0)); rdm(IC(1)); rdm(IC(2));
  ldN(IC(0));
  lwait<0>();
  static_for<0, 3>(rdym);
  static_for<0, 12>([&](auto q) {
    word(IC(0), q);
    if constexpr (q % 2 == 0) ldN(IC(1 + q / 2));
    SB();
  });
  wait_bar<7>();
  STAMP(1);
  rdya(xr[0]);
  rdyv(sw[1][0]); rdyv(sw[1][1]); rdyv(sw[1][2]);
  rdw(IC(0)); rdw(IC(1));
  lwait<3>();
  rdyv(wb[0][0]); rdyv(wb[0][1]); rdyv(wb[0][2]);
  xr[28] = u32x4{0x3f803f80u, 0, 0, 0};
  SB();
  static_for<0, 29>([&](auto kc) {
    constexpr int k = decltype(kc)::value;
    constexpr int cn = (k < 2) ? 1 : (k + 6) / 4, r = (k + 6) % 4;
    constexpr bool PR = k < 22;
    auto gap = [&](auto gc) {
      constexpr int g = decltype(gc)::value;
      if constexpr (k < 2) { word(IC(1), IC(6 * k + 2 * g)); word(IC(1), IC(6 * k + 2 * g + 1)); }
      else if constexpr (PR) word(IC(cn), IC(3 * r + g));
    };
    acc[0] = MFMA32(BF8(wb[k % 3][0]), BF8(xr[k]), acc[0]);
    constexpr bool BS = (k + 2) % 4 == 0;
    if constexpr (BS && k < 22) {
      constexpr int j = (k + 2) / 4;
      vwait<k1_iss(k) - 1 - k1_posW(j + 1)>();
      rdyv(sw[j + 1][0]); rdyv(sw[j + 1][1]); rdyv(sw[j + 1][2]);
    }
    if constexpr (k >= 2 && r == 2 && cn + 1 < 7) rdm(IC(cn + 1));
    if constexpr (!BS && k + 2 < 29) rdw(IC(k + 2));
    gap(IC(0));
    SB();
    acc[1] = MFMA32(BF8(wb[k % 3][1]), BF8(xr[k]), acc[1]);
    if constexpr (BS) {
      BST((k + 2) / 4);
      wait_bar<63>();
      BST(8 + (k + 2) / 4);
      rdw(IC(k + 2));
    }
    ldN(IC(7 + 2 * k));
    gap(IC(1));
    if constexpr (k == 23) {
      if (w < 3) {
        float bs = P[(w * 4 + 0) * 64 + lane] + P[(w * 4 + 1) * 64 + lane] + P[(w * 4 + 2) * 64 + lane] + P[(w * 4 + 3) * 64 + lane];
        const float bh = rbf(bs);
        *(u32x4*)(lds + (84 + w) * 1024 + lane * 16) = u32x4{hi ? 0u : pack2(bh, bs - bh), 0, 0, 0};
      }
    }
    SB();
    acc[2] = MFMA32(BF8(xr[k]), BF8(wb[k % 3][2]), acc[2]);
    ldN(IC(8 + 2 * k));
    gap(IC(2));
    if constexpr (k == 21) {
#pragma unroll
      for (int i = 0; i < 3; ++i) P[(i * 4 + w) * 64 + lane] = accb[i][0];
    }
    if constexpr (k + 1 < 29) {
      constexpr int nw = k == 0 ? 1 : k == 1 ? 2 : k == 21 ? 4 : (PR && r != 0) ? 1 : 0;
      vlwait<(k + 1 < 28) ? k1_iss(k + 1) - 1 - k1_posX(k + 1) : 63, (k + 2 < 29) ? 3 + nw : 0>();
      if constexpr (k + 1 < 28) rdya(xr[k + 1]);
      rdyv(wb[(k + 1) % 3][0]); rdyv(wb[(k + 1) % 3][1]); rdyv(wb[(k + 1) % 3][2]);
      if constexpr (k >= 2 && r == 2 && cn + 1 < 7) rdym(IC(cn + 1));
    }
    SB();
  });
  STAMP(2);
  const float qs = 0.18898223650461362f * 1.4426950408889634f;
#pragma unroll
  for (int p = 0; p < 2; ++p) {
    char* base = ws + (p ? OFF_K : OFF_Q) + ((size_t)(head * 128 + mt * 2 + (l31 >> 4)) * 64 + (l31 & 15)) * 16;
    const float sc = p ? 1.f : qs;
#pragma unroll
    for (int s = 0; s < 2; ++s) {
      u32x4 o;
#pragma unroll
      for (int j = 0; j < 4; ++j) o[j] = pack2(acc[p][8 * s + 2 * j] * sc, acc[p][8 * s + 2 * j + 1] * sc);
      *(u32x4*)(base + (2 * s + hi) * 256) = o;
    }
  }
  {
    char* base = ws + OFF_V + ((size_t)((head * 64 + mt) * 2 + (l31 >> 4)) * 64 + (l31 & 15)) * 16;
#pragma unroll
    for (int s = 0; s < 2; ++s) {
      u32x4 o = {pack2(acc[2][4 * s], acc[2][4 * s + 1]), pack2(acc[2][4 * s + 2], acc[2][4 * s + 3]),
                 pack2(acc[2][8 + 4 * s], acc[2][8 + 4 * s + 1]), pack2(acc[2][8 + 4 * s + 2], acc[2][8 + 4 * s + 3])};
      if (l31 == 28) o = u32x4{0x3f803f80u, 0x3f803f80u, 0x3f803f80u, 0x3f803f80u};
      *(u32x4*)(base + (2 * s + hi) * 256) = o;
    }
  }
  PFLUSH(1);
  PFLUSH2(6);
}

template <bool ODD>
DEVI void attn_pv(const char* vp, const u32x2 (&p)[17], f32x4& o0, f32x4& o1) {
  bf16x8 vf[9][2];
#pragma unroll
  for (int v = 0; v < 3; ++v) { vf[v][0] = *(const bf16x8*)(vp + v * 2048); vf[v][1] = *(const bf16x8*)(vp + v * 2048 + 1024); }
#pragma unroll
  for (int v = 0; v < 9; ++v) {
    if (v + 3 < 9) {
      vf[v + 3][0] = *(const bf16x8*)(vp + (v + 3) * 2048);
      vf[v + 3][1] = *(const bf16x8*)(vp + (v + 3) * 2048 + 1024);
    }
    const int i0 = 2 * v - (ODD ? 1 : 0), i1 = i0 + 1;
    const u32x2 z = {0, 0};
    const u32x2 pa = i0 >= 0 ? p[i0 >= 0 ? i0 : 0] : z, pc = i1 < 17 ? p[i1 < 17 ? i1 : 0] : z;
    const u32x4 pw = {pa[0], pa[1], pc[0], pc[1]};
    const bf16x8 pf = __builtin_bit_cast(bf16x8, pw);
    o0 = MFMA16(vf[v][0], pf, o0);
    o1 = MFMA16(vf[v][1], pf, o1);
    SB();
  }
}

DEVI void attn_wave(const char* ldsK, const char* ldsV, const char* ldsQ, int w, int lane, int nneg, char* odst, int kbase) {
  const int g = lane >> 4, qq = lane & 15;
  const bf16x8 q = *(const bf16x8*)(ldsQ + w * 1024 + lane * 16);
  const float NEG = -1e30f;
  f32x4 s[17];
  const char* kp = ldsK + w * 1024 + lane * 16;
  bf16x8 kf[17];
#pragma unroll
  for (int tt = 0; tt < 4; ++tt) kf[tt] = *(const bf16x8*)(kp + tt * 1024);
#pragma unroll
  for (int tt = 0; tt < 17; ++tt) {
    if (tt + 4 < 17) kf[tt + 4] = *(const bf16x8*)(kp + (tt + 4) * 1024);
    s[tt] = MFMA16(kf[tt], q, (f32x4{0.f, 0.f, 0.f, 0.f}));
    SB();
  }
#pragma unroll
  for (int r = 0; r < 4; ++r) {
    s[0][r] = (4 * g + r > qq) ? s[0][r] : NEG;
    s[16][r] = (4 * g + r <= qq) ? s[16][r] : NEG;
  }
  if (nneg > 0) {
#pragma unroll
    for (int tt = 0; tt < 16; ++tt)
      if (w + tt < nneg) s[tt] = f32x4{NEG, NEG, NEG, NEG};
  }
  float m = NEG;
#pragma unroll
  for (int tt = 0; tt < 17; ++tt) m = fmaxf(fmaxf(m, fmaxf(s[tt][0], s[tt][1])), fmaxf(s[tt][2], s[tt][3]));
  m = fmaxf(m, __shfl_xor(m, 16));
  m = fmaxf(m, __shfl_xor(m, 32));
  u32x2 pb[17];
#pragma unroll
  for (int tt = 0; tt < 17; ++tt) {
    const float e0 = __builtin_amdgcn_exp2f(s[tt][0] - m), e1 = __builtin_amdgcn_exp2f(s[tt][1] - m);
    const float e2 = __builtin_amdgcn_exp2f(s[tt][2] - m), e3 = __builtin_amdgcn_exp2f(s[tt][3] - m);
    pb[tt] = u32x2{pack2(e0, e1), pack2(e2, e3)};
  }
  wait_bar<0>();
  f32x4 o0 = {0.f, 0.f, 0.f, 0.f}, o1 = {0.f, 0.f, 0.f, 0.f};
  const char* vp = ldsV + (w >> 1) * 2048 + lane * 16;
  if (w & 1) attn_pv<true>(vp, pb, o0, o1);
  else attn_pv<false>(vp, pb, o0, o1);
  const float sum = __shfl(o1[0], 48 + qq);
  const float inv = 1.f / sum;
  {
    const int k = kbase + 4 * g;
    u32x2 o = {pack2(o0[0] * inv, o0[1] * inv), pack2(o0[2] * inv, o0[3] * inv)};
    *(u32x2*)(odst + (size_t)(k >> 4) * 1024 + ((k >> 3) & 1) * 512 + (k & 4) * 2) = o;
  }
  if (g < 3) {
    const int k = kbase + 16 + 4 * g;
    u32x2 o = {pack2(o1[0] * inv, o1[1] * inv), pack2(o1[2] * inv, o1[3] * inv)};
    *(u32x2*)(odst + (size_t)(k >> 4) * 1024 + ((k >> 3) & 1) * 512 + (k & 4) * 2) = o;
  }
}

__global__ __launch_bounds__(512) __attribute__((amdgpu_waves_per_eu(4, 4))) void k2_attn(
    const uint16_t* __restrict__ wo, const uint16_t* __restrict__ wg, const uint16_t* __restrict__ wu,
    char* __restrict__ ws) {
  extern __shared__ __attribute__((aligned(16))) char lds[];
  const int t = threadIdx.x, lane = t & 63, w = __builtin_amdgcn_readfirstlane(t >> 6);
  PDECL; STAMP(0);
  if (__builtin_expect(blockIdx.x >= 256, 0)) {
    const int u = blockIdx.x - 256;
    if (u < 224) fold_unit((u & 4) ? wu : wg, ws, (u >> 2) & 1, u >> 3, u & 3, t, lds PPASS);
    else if (w < 4) retile_unit(wo, ws, 2, u - 224, 0, t);
    PFLUSH(2);
    return;
  }
  const int xc = blockIdx.x & 7, bi = blockIdx.x >> 3;
  const int head = xc * 2 + (bi & 1), qb = bi >> 1;
  char* ldsK = lds;
  char* ldsV = lds + 24 * 1024;
  char* ldsQ = lds + 48 * 1024;
  const int sub0 = qb * 8 - 16;
  GLDS16(ws + OFF_Q + (size_t)(head * 128 + qb * 8 + w) * 1024 + lane * 16, ldsQ + w * 1024);
#pragma unroll
  for (int i = 0; i < 3; ++i) {
    const int p = w * 3 + i;
    const int sidx = max(sub0 + p, 0);
    GLDS16(ws + OFF_K + (size_t)(head * 128 + sidx) * 1024 + lane * 16, ldsK + p * 1024);
  }
#pragma unroll
  for (int i = 0; i < 3; ++i) {
    const int p = w * 3 + i;
    const int sidx = max(sub0 + p, 0);
    GLDS16(ws + OFF_V + (size_t)(head * 128 + sidx) * 1024 + lane * 16, ldsV + p * 1024);
  }
  wait_bar<3>();
  STAMP(1);
  const int tok = qb * 128 + w * 16 + (lane & 15);
  char* odst = ws + OFF_O + (size_t)(tok >> 5) * 28 * 1024 + (tok & 31) * 16;
  const int kbase = head * 28;
  const int nneg = -sub0;
  attn_wave(ldsK, ldsV, ldsQ, w, lane, nneg, odst, kbase);
  PFLUSH(2);
}

DEVI f32x16 ksplit_reduce(f32x16 (&acc)[4], char* lds, int w, int lane) {
#pragma unroll
  for (int tl = 0; tl < 4; ++tl) {
    if (tl != w) {
      const int slot = (w > tl) ? w - 1 : w;
      char* p = lds + (tl * 3 + slot) * 4096 + lane * 16;
#pragma unroll
      for (int q = 0; q < 4; ++q)
        *(f32x4*)(p + q * 1024) = f32x4{acc[tl][4 * q], acc[tl][4 * q + 1], acc[tl][4 * q + 2], acc[tl][4 * q + 3]};
    }
  }
  f32x16 r = (w == 0) ? acc[0] : (w == 1) ? acc[1] : (w == 2) ? acc[2] : acc[3];
  __syncthreads();
#pragma unroll
  for (int s = 0; s < 3; ++s) {
    const char* p = lds + (w * 3 + s) * 4096 + lane * 16;
#pragma unroll
    for (int q = 0; q < 4; ++q) {
      const f32x4 v = *(const f32x4*)(p + q * 1024);
#pragma unroll
      for (int j = 0; j < 4; ++j) r[4 * q + j] += v[j];
    }
  }
  return r;
}

__global__ __launch_bounds__(256) __attribute__((amdgpu_waves_per_eu(1, 1))) void k3_wo(const uint16_t* __restrict__ x,
                                                                                        char* __restrict__ ws) {
  __shared__ __attribute__((aligned(16))) char lds[48 * 1024];
  const int t = threadIdx.x, lane = t & 63, w = __builtin_amdgcn_readfirstlane(t >> 6);
  PDECL; STAMP(0);
  if (__builtin_expect(blockIdx.x >= 224, 0)) {
    if (t < 224) {
      const int z = t >= 112, col = (blockIdx.x - 224) * 112 + t - 112 * z;
      f32x2 v = {0.f, 0.f};
#pragma unroll
      for (int i = 0; i < 28; ++i) v += *(const f32x2*)(ws + OFF_PART + ((size_t)i * 3584 + col) * 8);
      const float ch = rbf(v[0]), cl = v[0] - ch, bh = rbf(v[1]), bl = v[1] - bh;
      u32x4 o = {pack2(ch, ch), pack2(cl, cl), pack2(bh, bh), pack2(bl, bl)};
      if (z) o = u32x4{0, 0, 0, 0};
      const int mat = col >= 1792, f = col - 1792 * mat;
      *(u32x4*)(ws + OFF_WGU + ((size_t)((f >> 4) * 29 + 28) * 64 + z * 32 + mat * 16 + swap23(f & 15)) * 16) = o;
    }
    PFLUSH(3);
    return;
  }
  const int xc = blockIdx.x & 7, bi = blockIdx.x >> 3;
  const int mb = xc * 4 + (bi & 3), nb = bi >> 2;
  const char* A = ws + OFF_O + ((size_t)mb * 2 * 28 + 7 * w) * 1024 + lane * 16;
  const char* B = ws + OFF_WO + ((size_t)nb * 2 * 28 + 7 * w) * 1024 + lane * 16;
  const int wm = w & 1, wn = w >> 1;
  const int hi = lane >> 5, l31 = lane & 31;
  const int mt = mb * 2 + wm, nt = nb * 2 + wn;
  bf16x8 a[7][2], bw[7][2];
#pragma unroll
  for (int k = 0; k < 7; ++k) {
    a[k][0] = *(const bf16x8*)(A + k * 1024);
    bw[k][0] = *(const bf16x8*)(B + k * 1024);
    a[k][1] = *(const bf16x8*)(A + (28 + k) * 1024);
    bw[k][1] = *(const bf16x8*)(B + (28 + k) * 1024);
  }
  u32x4 xv[2];
  f32x4 gv[4];
  {
    const uint16_t* xs = x + (size_t)(mt * 32 + l31) * 448 + nt * 32 + hi * 8;
    xv[0] = *(const u32x4*)xs;
    xv[1] = *(const u32x4*)(xs + 16);
    const float* gp = (const float*)(ws + OFF_GAB) + nt * 32 + hi * 8;
    gv[0] = *(const f32x4*)gp; gv[1] = *(const f32x4*)(gp + 4);
    gv[2] = *(const f32x4*)(gp + 16); gv[3] = *(const f32x4*)(gp + 20);
  }
  __builtin_amdgcn_sched_barrier(0);
  f32x16 acc[4];
#pragma unroll
  for (int i = 0; i < 4; ++i)
#pragma unroll
    for (int r = 0; r < 16; ++r) acc[i][r] = 0.f;
#pragma unroll
  for (int k = 0; k < 7; ++k) {
    acc[0] = MFMA32(bw[k][0], a[k][0], acc[0]);
    acc[1] = MFMA32(bw[k][0], a[k][1], acc[1]);
    acc[2] = MFMA32(bw[k][1], a[k][0], acc[2]);
    acc[3] = MFMA32(bw[k][1], a[k][1], acc[3]);
  }
  STAMP(1);
  const f32x16 r = ksplit_reduce(acc, lds, w, lane);
  STAMP(2);
  float v[16];
#pragma unroll
  for (int s = 0; s < 2; ++s) {
    u32x4 o;
#pragma unroll
    for (int j = 0; j < 4; ++j) {
      const float ga0 = gv[2 * s + (j >> 1)][(2 * j) & 3], ga1 = gv[2 * s + (j >> 1)][(2 * j + 1) & 3];
      o[j] = pack2(bflo(xv[s][j]) + ga0 * r[8 * s + 2 * j], bfhi(xv[s][j]) + ga1 * r[8 * s + 2 * j + 1]);
      v[8 * s + 2 * j] = bflo(o[j]);
      v[8 * s + 2 * j + 1] = bfhi(o[j]);
    }
    *(u32x4*)(ws + OFF_X1 + ((size_t)(mt * 28 + nt * 2 + s) * 64 + lane) * 16) = o;
  }
  float sm = 0.f;
#pragma unroll
  for (int j = 0; j < 16; ++j) sm += v[j];
  sm += __shfl_xor(sm, 32);
  const float mean = sm * (1.f / 32.f);
  float m2 = 0.f;
#pragma unroll
  for (int j = 0; j < 16; ++j) { const float d = v[j] - mean; m2 += d * d; }
  m2 += __shfl_xor(m2, 32);
  if (hi == 0) *(f32x2*)(ws + OFF_STATS + ((size_t)(mt * 8 + nb) * 32 + l31) * 16 + wn * 8) = f32x2{mean, m2};
  PFLUSH(3);
}

constexpr int k4_posG(int g) { return g < 2 ? g : g < 4 ? g + 2 : g <= 9 ? 8 + 3 * (g - 4) : g == 10 ? 30 : 4 + 3 * g; }
constexpr int k4_posX(int s) { return s == 0 ? 3 : s == 1 ? 7 : s <= 6 ? 10 + 3 * (s - 2) : s == 7 ? 27 : s == 8 ? 34 : s < 27 ? 12 + 3 * s : 92; }
constexpr int k4_iss(int k) { return k == 0 ? 23 : k == 1 ? 30 : k <= 20 ? 31 + 3 * k : 93; }
__global__ __launch_bounds__(256) __attribute__((amdgpu_waves_per_eu(1, 1))) void k4_gu(
    const uint16_t* __restrict__ wd, const uint16_t* __restrict__ cvec, const uint16_t* __restrict__ wmod,
    char* __restrict__ ws) {
  extern __shared__ __attribute__((aligned(16))) char lds[];
  const int t = threadIdx.x, lane = t & 63, w = __builtin_amdgcn_readfirstlane(t >> 6);
  PDECL; STAMP(0);
  if (__builtin_expect(blockIdx.x >= 224, 0)) {
    const int j = blockIdx.x - 224;
    u32x4 wv[2][8];
#pragma unroll
    for (int h = 0; h < 2; ++h) {
      const int id = min(t + 256 * h, 391);
      const uint16_t* p = wd + (size_t)((j * 7 + id / 56) * 8) * 448 + (id % 56) * 8;
#pragma unroll
      for (int i = 0; i < 8; ++i) wv[h][i] = *(const u32x4*)(p + (size_t)i * 448);
    }
    const int v = (j & 7) * 2 + (j >> 3);
    if (j < 16 && v < 14) mod_unit(cvec, wmod, ws, 5 * 448 + v * 32, t, (float*)lds);
#pragma unroll
    for (int h = 0; h < 2; ++h) {
      const int id = t + 256 * h, ko = j * 7 + id / 56, coct = id % 56;
      if (id < 392) {
#pragma unroll
        for (int c = 0; c < 8; ++c) {
          u32x4 o;
#pragma unroll
          for (int i = 0; i < 4; ++i)
            o[i] = __builtin_amdgcn_perm(wv[h][2 * i + 1][c >> 1], wv[h][2 * i][c >> 1], (c & 1) ? 0x07060302u : 0x05040100u);
          const int n = coct * 8 + c;
          *(u32x4*)(ws + OFF_WD + ((size_t)((n >> 5) * 112 + (ko >> 1)) * 64 + (ko & 1) * 32 + swap23(n & 31)) * 16) = o;
        }
      }
    }
    PFLUSH(4);
    return;
  }
  const int xc = blockIdx.x & 7, bi = blockIdx.x >> 3;
  const int mb = (xc & 1) * 4 + (bi & 3), fb = (xc >> 1) * 7 + (bi >> 2);
  const int mt = mb * 8 + 2 * w;
  const char* A = ws + OFF_X1 + (size_t)mt * 28 * 1024 + lane * 16;
  const char* B = ws + OFF_WGU + (size_t)(fb * 4 + w) * 29 * 1024 + lane * 16;
  const int hi = lane >> 5, l31 = lane & 31;
  u32x4 xr[29][2], st[2][4];
  auto ldG = [&](auto gc) {
    constexpr int g = decltype(gc)::value;
    GLDS16(B + g * 1024, lds + (g * 4 + w) * 1024);
  };
  auto ldXa = [&](auto sc) { constexpr int s = decltype(sc)::value; gload16a(xr[s][0], A + s * 1024); };
  auto ldXb = [&](auto sc) { constexpr int s = decltype(sc)::value; gload16a(xr[s][1], A + (28 + s) * 1024); };
  ldG(IC(0)); ldG(IC(1)); ldXa(IC(0)); ldXb(IC(0)); ldG(IC(2)); ldG(IC(3)); ldXa(IC(1)); ldXb(IC(1));
  const char* S = ws + OFF_STATS + (size_t)mt * 4096 + (hi * 4 * 32 + l31) * 16;
  static_for<2, 7>([&](auto sc) { ldG(IC(sc + 2)); ldXa(sc); ldXb(sc); });
  f32x16 acc[2][4];
#pragma unroll
  for (int i = 0; i < 8; ++i)
#pragma unroll
    for (int r = 0; r < 16; ++r) acc[i >> 2][i & 3][r] = 0.f;
  const unsigned wa0 = lds_addr(lds) + lane * 16, wa1 = wa0 + 65536;
  u32x4 wb[3][4];
  float rstd[2], var[2], ta[4], tb[4], d[8], mu;
  f32x4 p[4];
  auto stat = [&](auto tc, auto sc) {
    constexpr int tt = decltype(tc)::value, q = decltype(sc)::value;
    if constexpr (q == 0) {
#pragma unroll
      for (int i = 0; i < 4; ++i) p[i] = F4(st[tt][i]);
      if (hi) p[3] = f32x4{0.f, 0.f, 0.f, 0.f};
    } else if constexpr (q == 1 || q == 2) {
#pragma unroll
      for (int i = 2 * q - 2; i < 2 * q; ++i) { ta[i] = fadd1(p[i][0], p[i][2]); tb[i] = fadd1(p[i][1], p[i][3]); }
    } else if constexpr (q == 3) {
      ta[0] = fadd1(ta[0], ta[1]); ta[2] = fadd1(ta[2], ta[3]); tb[0] = fadd1(tb[0], tb[1]); tb[2] = fadd1(tb[2], tb[3]);
    } else if constexpr (q == 4) {
      ta[0] = fadd1(ta[0], ta[2]); tb[0] = fadd1(tb[0], tb[2]);
    } else if constexpr (q == 5) {
      ta[0] = xhalf_sum(ta[0]);
    } else if constexpr (q == 6) {
      mu = fmul1(ta[0], 1.f / 14.f);
      if (hi) { p[3][0] = mu; p[3][2] = mu; }
    } else if constexpr (q == 7 || q == 8) {
#pragma unroll
      for (int i = 2 * q - 14; i < 2 * q - 12; ++i) { d[2 * i] = fsub1(p[i][0], mu); d[2 * i + 1] = fsub1(p[i][2], mu); }
    } else if constexpr (q == 9) {
#pragma unroll
      for (int i = 0; i < 4; ++i) ta[i] = fmul1(d[i], d[i]);
    } else if constexpr (q == 10) {
#pragma unroll
      for (int i = 0; i < 4; ++i) fmac1(ta[i], d[4 + i], d[4 + i]);
    } else if constexpr (q == 11) {
      ta[0] = fadd1(ta[0], ta[1]); ta[2] = fadd1(ta[2], ta[3]);
      ta[1] = rbf(mu);
    } else if constexpr (q == 12) {
      fmac1(tb[0], fadd1(ta[0], ta[2]), 32.f);
    } else if constexpr (q == 13) {
      tb[0] = xhalf_sum(tb[0]);
    } else if constexpr (q == 14) {
      var[tt] = fmaf(tb[0], 1.f / 448.f, 1e-5f);
      rstd[tt] = __builtin_amdgcn_rsqf(var[tt]);
    } else if constexpr (q == 15) {
      ta[0] = fmul1(var[tt], rstd[tt]);
      ta[3] = fsub1(ta[1], mu);
    } else if constexpr (q == 16) {
      ta[2] = rbf(ta[0]);
    } else if constexpr (q == 17) {
      const unsigned pm = pack2(-ta[1], ta[3]), ps = pack2(ta[2], fsub1(ta[0], ta[2]));
      xr[28][tt] = u32x4{pm, pm, ps, ps};
    }
  };
  auto rdw = [&](auto kc) {
    constexpr int k = decltype(kc)::value;
    constexpr int o = (k & 15) * 4096;
    const unsigned a = (k >= 16) ? wa1 : wa0;
    lread16<o>(wb[k % 3][0], a); lread16<o + 1024>(wb[k % 3][1], a);
    lread16<o + 2048>(wb[k % 3][2], a); lread16<o + 3072>(wb[k % 3][3], a);
  };
  PDECL2;
  BST(0);
  vbar<21>();
  BST(8);
  STAMP(1);
  rdw(IC(0)); rdw(IC(1));
  vlwait<19, 4>();
  rdya(xr[0][0]); rdya(xr[0][1]);
  rdyv(wb[0][0]); rdyv(wb[0][1]); rdyv(wb[0][2]); rdyv(wb[0][3]);
  SB();
  static_for<0, 29>([&](auto kc) {
    constexpr int k = decltype(kc)::value;
    static_for<0, 8>([&](auto gc) {
      constexpr int g = decltype(gc)::value;
      acc[g & 1][g >> 1] = MFMA32(BF8(wb[k % 3][g >> 1]), BF8(xr[k][g & 1]), acc[g & 1][g >> 1]);
      if constexpr (g == 0) {
        if constexpr (k % 2 == 0 && k + 2 < 29) {
          if constexpr (k % 4 == 0) BST(1 + k / 4);
          vbar<k4_iss(k) - 1 - k4_posG(k + 3 < 29 ? k + 3 : 28)>();
          if constexpr (k % 4 == 0) BST(9 + k / 4);
        }
        if constexpr (k + 2 < 29) rdw(IC(k + 2));
      }
      if constexpr (g == 1 && k + 9 < 29) ldG(IC(k + 9));
      if constexpr (g == 3 && k + 7 < 28) ldXa(IC(k + 7));
      if constexpr (g == 5 && k + 7 < 28) ldXb(IC(k + 7));
      if constexpr (k < 2 && (g == 2 || g == 4 || g >= 6)) gload16(st[k & 1][g == 7 ? 3 : g / 2 - 1], S + k * 4096 + (g == 7 ? 3 : g / 2 - 1) * 512);
      if constexpr (k >= 9 && k < 14) stat(IC((((k - 9) * 8 + g) / 20) & 1), IC(((k - 9) * 8 + g) % 20));
      if constexpr (g == 7 && k + 1 < 29) {
        vlwait<(k + 1 < 28) ? k4_iss(k + 1) - 1 - k4_posX(k + 1) : 63, (k + 2 < 29) ? 4 : 0>();
        if constexpr (k + 1 < 28) { rdya(xr[k + 1][0]); rdya(xr[k + 1][1]); }
        rdyv(wb[(k + 1) % 3][0]); rdyv(wb[(k + 1) % 3][1]); rdyv(wb[(k + 1) % 3][2]); rdyv(wb[(k + 1) % 3][3]);
        if constexpr (k == 8) {
#pragma unroll
          for (int i = 0; i < 8; ++i) rdyv(st[i >> 2][i & 3]);
        }
      }
      SB();
    });
  });
  STAMP(2);
  const float c0 = -2.f * 0.7978845608028654f * 1.4426950408889634f, c1 = c0 * 0.044715f;
  char* hdst = ws + OFF_HFF + ((size_t)(mt * 112 + fb * 4) * 64 + lane) * 16;
#pragma unroll
  for (int tt = 0; tt < 2; ++tt)
#pragma unroll
    for (int n = 0; n < 4; ++n) {
      const float e0 = c0 * rstd[tt], e1 = c1 * rstd[tt] * rstd[tt] * rstd[tt];
      float hh[8];
#pragma unroll
      for (int j = 0; j < 8; ++j) {
        const float g = acc[tt][n][j], u = acc[tt][n][8 + j];
        const float e = __builtin_amdgcn_exp2f(g * fmaf(g * g, e1, e0));
        hh[j] = g * u * __builtin_amdgcn_rcpf(fmaf(e, var[tt], var[tt]));
      }
      u32x4 o = {pack2(hh[0], hh[1]), pack2(hh[2], hh[3]), pack2(hh[4], hh[5]), pack2(hh[6], hh[7])};
      *(u32x4*)(hdst + (tt * 112 + n) * 1024) = o;
    }
  PFLUSH(4);
  PFLUSH2(8);
}

__global__ __launch_bounds__(256) __attribute__((amdgpu_waves_per_eu(1, 1))) void k5_down(char* __restrict__ ws,
                                                                                          uint16_t* __restrict__ out) {
  __shared__ __attribute__((aligned(16))) char lds[48 * 1024];
  const int t = threadIdx.x, lane = t & 63, w = __builtin_amdgcn_readfirstlane(t >> 6);
  PDECL; STAMP(0);
  const int xc = blockIdx.x & 7, bi = blockIdx.x >> 3;
  const int mb = xc * 4 + (bi & 3), nb = bi >> 2;
  const char* A = ws + OFF_HFF + ((size_t)mb * 2 * 112 + 28 * w) * 1024 + lane * 16;
  const char* B = ws + OFF_WD + ((size_t)nb * 2 * 112 + 28 * w) * 1024 + lane * 16;
  const int wm = w & 1, wn = w >> 1;
  const int hi = lane >> 5, l31 = lane & 31;
  const int mt = mb * 2 + wm, nt = nb * 2 + wn;
  constexpr int D = 12;
  bf16x8 a[D][2], bw[D][2];
  auto ld = [&](int k) {
    a[k % D][0] = *(const bf16x8*)(A + k * 1024);
    bw[k % D][0] = *(const bf16x8*)(B + k * 1024);
    a[k % D][1] = *(const bf16x8*)(A + (112 + k) * 1024);
    bw[k % D][1] = *(const bf16x8*)(B + (112 + k) * 1024);
  };
#pragma unroll
  for (int k = 0; k < D; ++k) ld(k);
  u32x4 xv[2];
  f32x4 gv[4];
  f32x16 acc[4];
#pragma unroll
  for (int i = 0; i < 4; ++i)
#pragma unroll
    for (int r = 0; r < 16; ++r) acc[i][r] = 0.f;
  __builtin_amdgcn_sched_barrier(0);
#pragma unroll
  for (int k = 0; k < 28; ++k) {
    acc[0] = MFMA32(bw[k % D][0], a[k % D][0], acc[0]);
    acc[1] = MFMA32(bw[k % D][0], a[k % D][1], acc[1]);
    acc[2] = MFMA32(bw[k % D][1], a[k % D][0], acc[2]);
    acc[3] = MFMA32(bw[k % D][1], a[k % D][1], acc[3]);
    if (k + D < 28) ld(k + D);
    if (k + D == 28) {
      const char* xs = ws + OFF_X1 + ((size_t)(mt * 28 + nt * 2) * 64 + lane) * 16;
      xv[0] = *(const u32x4*)xs;
      xv[1] = *(const u32x4*)(xs + 1024);
      const float* gp = (const float*)(ws + OFF_GAB) + 448 + nt * 32 + hi * 8;
      gv[0] = *(const f32x4*)gp; gv[1] = *(const f32x4*)(gp + 4);
      gv[2] = *(const f32x4*)(gp + 16); gv[3] = *(const f32x4*)(gp + 20);
    }
    __builtin_amdgcn_sched_barrier(0);
  }
  STAMP(1);
  const f32x16 r = ksplit_reduce(acc, lds, w, lane);
  STAMP(2);
#pragma unroll
  for (int s = 0; s < 2; ++s) {
    u32x4 o;
#pragma unroll
    for (int j = 0; j < 4; ++j) {
      const float ga0 = gv[2 * s + (j >> 1)][(2 * j) & 3], ga1 = gv[2 * s + (j >> 1)][(2 * j + 1) & 3];
      o[j] = pack2(bflo(xv[s][j]) + ga0 * r[8 * s + 2 * j], bfhi(xv[s][j]) + ga1 * r[8 * s + 2 * j + 1]);
    }
    *(u32x4*)(out + (size_t)(mt * 32 + l31) * 448 + nt * 32 + s * 16 + hi * 8) = o;
  }
  PFLUSH(5);
}

constexpr int LDS_K1 = 94 * 1024;
constexpr int LDS_K2 = 56 * 1024;
constexpr int LDS_K4 = 116 * 1024;

extern "C" void kernel_launch(void* const* d_in, const int* in_sizes, int n_in, void* d_out, int out_size, void* d_ws,
                              size_t ws_size, hipStream_t stream) {
  const uint16_t* x = (const uint16_t*)d_in[0];
  const uint16_t* wqkv = (const uint16_t*)d_in[1];
  const uint16_t* wo = (const uint16_t*)d_in[2];
  const uint16_t* wg = (const uint16_t*)d_in[4];
  const uint16_t* wu = (const uint16_t*)d_in[5];
  const uint16_t* wd = (const uint16_t*)d_in[6];
  const uint16_t* cv = (const uint16_t*)d_in[7];
  const uint16_t* wmod = (const uint16_t*)d_in[8];
  char* ws = (char*)d_ws;
  (void)hipFuncSetAttribute((const void*)k1_qkv, hipFuncAttributeMaxDynamicSharedMemorySize, LDS_K1);
  (void)hipFuncSetAttribute((const void*)k2_attn, hipFuncAttributeMaxDynamicSharedMemorySize, LDS_K2);
  (void)hipFuncSetAttribute((const void*)k4_gu, hipFuncAttributeMaxDynamicSharedMemorySize, LDS_K4);
  k0_prep<<<178, 256, 0, stream>>>(x, wqkv, cv, wmod, ws);
  k1_qkv<<<256, 256, LDS_K1, stream>>>(ws);
  k2_attn<<<494, 512, LDS_K2, stream>>>(wo, wg, wu, ws);
  k3_wo<<<256, 256, 0, stream>>>(x, ws);
  k4_gu<<<256, 256, LDS_K4, stream>>>(wd, cv, wmod, ws);
  k5_down<<<224, 256, 0, stream>>>(ws, (uint16_t*)d_out);
}
```

```cpp
#include <hip/hip_runtime.h>
#include <stdint.h>

typedef __attribute__((ext_vector_type(8))) short bf16x8;
typedef __attribute__((ext_vector_type(16))) float f32x16;
typedef __attribute__((ext_vector_type(4))) float f32x4;
typedef __attribute__((ext_vector_type(2))) float f32x2;
typedef __attribute__((ext_vector_type(2))) __bf16 bf16x2_t;
typedef __attribute__((ext_vector_type(4))) unsigned u32x4;
typedef __attribute__((ext_vector_type(2))) unsigned u32x2;

#define STMASK 151
#define ST16(p, v) (*(u32x4*)(p) = (v))
#define ST8(p, v) (*(u32x2*)(p) = (v))
template <int BIT>
__device__ __forceinline__ void st16w(void* p, u32x4 v) {
  if constexpr ((STMASK >> BIT) & 1)
    asm volatile("global_store_dwordx4 %0, %1, off sc1\n s_nop 1" ::"v"(p), "v"(v) : "memory");
  else
    *(u32x4*)p = v;
}

#define DEVI __device__ __forceinline__

DEVI unsigned pack2(float a, float b) {
  f32x2 v = {a, b};
  bf16x2_t r = __builtin_convertvector(v, bf16x2_t);
  return __builtin_bit_cast(unsigned, r);
}
DEVI float bflo(unsigned w) { return __builtin_bit_cast(float, w << 16); }
DEVI float bfhi(unsigned w) { return __builtin_bit_cast(float, w & 0xffff0000u); }
DEVI float rbf(float a) { return bflo(pack2(a, a)); }
DEVI int swap23(int d) { return (d & ~12) | ((d & 4) << 1) | ((d & 8) >> 1); }

constexpr size_t OFF_XN = 0;
constexpr size_t OFF_WQKV = OFF_XN + 64 * 28 * 1024;
constexpr size_t OFF_WO = OFF_WQKV + 48 * 28 * 1024;
constexpr size_t OFF_WGU = OFF_WO + 14 * 28 * 1024;
constexpr size_t OFF_WD = OFF_WGU + 112 * 29 * 1024;
constexpr size_t OFF_Q = OFF_WD + 14 * 112 * 1024;
constexpr size_t OFF_K = OFF_Q + 16 * 128 * 1024;
constexpr size_t OFF_V = OFF_K + 16 * 128 * 1024;
constexpr size_t OFF_O = OFF_V + 16 * 128 * 1024;
constexpr size_t OFF_X1 = OFF_O + 64 * 28 * 1024;
constexpr size_t OFF_HFF = OFF_X1 + 64 * 28 * 1024;
constexpr size_t OFF_STATS = OFF_HFF + 64 * 112 * 1024;
constexpr size_t OFF_MODA = OFF_STATS + 64 * 4096;
constexpr size_t OFF_MODB = OFF_MODA + 4096;
constexpr size_t OFF_GAB = OFF_MODB + 4096;
constexpr size_t OFF_PART = OFF_GAB + 4096;
constexpr size_t WS_END = OFF_PART + 28 * 3584 * 8;

#define PDECL
#define STAMP(i)
#define PFLUSH(kid)
#define PDECL2
#define BST(i)
#define PFLUSH2(kid)
#define GLDS16(src, dst) __builtin_amdgcn_global_load_lds((const unsigned*)(src), (unsigned*)(dst), 16, 0, 0)
#define GLDS4(src, dst) __builtin_amdgcn_global_load_lds((const unsigned*)(src), (unsigned*)(dst), 4, 0, 0)

template <int N>
DEVI void wait_bar() {
  asm volatile("s_waitcnt vmcnt(%0) lgkmcnt(0)\n\ts_barrier" ::"n"(N) : "memory");
}
template <int N>
DEVI void wait_bar4(u32x4& a, u32x4& b, u32x4& c, u32x4& d) {
  asm volatile("s_waitcnt vmcnt(%4) lgkmcnt(0)\n\ts_barrier" : "+v"(a), "+v"(b), "+v"(c), "+v"(d) : "n"(N) : "memory");
}
DEVI void gload16(u32x4& r, const char* p) {
  asm volatile("global_load_dwordx4 %0, %1, off" : "=v"(r) : "v"(p) : "memory");
}

typedef __attribute__((address_space(3))) char lds_char;
DEVI unsigned lds_addr(const char* p) { return (unsigned)(size_t)(lds_char*)p; }
template <int OFF>
DEVI void lread16(u32x4& r, unsigned a) {
  asm volatile("ds_read_b128 %0, %1 offset:%2" : "=v"(r) : "v"(a), "n"(OFF) : "memory");
}
template <int OFF>
DEVI void lwrite16(const u32x4& r, unsigned a) {
  asm volatile("ds_write_b128 %1, %0 offset:%2" ::"v"(r), "v"(a), "n"(OFF) : "memory");
}
template <int N>
DEVI void lwait() { asm volatile("s_waitcnt lgkmcnt(%0)" ::"n"(N) : "memory"); }
template <int N>
DEVI void vwait() { asm volatile("s_waitcnt vmcnt(%0)" ::"n"(N) : "memory"); }
DEVI void rdyv(u32x4& a) { asm volatile("" : "+v"(a)); }
DEVI void rdya(u32x4& a) { asm volatile("" : "+a"(a)); }
DEVI void gload16a(u32x4& r, const char* p) {
  asm volatile("global_load_dwordx4 %0, %1, off" : "=a"(r) : "v"(p) : "memory");
}
template <int OFF>
DEVI void gload16o(u32x4& r, const char* p) {
  asm volatile("global_load_dwordx4 %0, %1, off offset:%2" : "=v"(r) : "v"(p), "n"(OFF) : "memory");
}
template <int OFF>
DEVI void gload16ao(u32x4& r, const char* p) {
  asm volatile("global_load_dwordx4 %0, %1, off offset:%2" : "=a"(r) : "v"(p), "n"(OFF) : "memory");
}
DEVI unsigned scale2(unsigned w, float s0, float s1) {
  unsigned r; float t0, t1;
  asm("v_lshlrev_b32 %1, 16, %3\n\tv_and_b32 %2, 0xffff0000, %3\n\tv_mul_f32 %1, %1, %4\n\tv_mul_f32 %2, %2, %5\n\t"
      "v_cvt_pk_bf16_f32 %0, %1, %2" : "=v"(r), "=&v"(t0), "=&v"(t1) : "v"(w), "v"(s0), "v"(s1));
  return r;
}
DEVI float dot2(unsigned a, unsigned b, float c) {
  return __builtin_amdgcn_fdot2_f32_bf16(__builtin_bit_cast(bf16x2_t, a), __builtin_bit_cast(bf16x2_t, b), c, false);
}
DEVI float fmul1(float a, float b) { float r; asm("v_mul_f32 %0, %1, %2" : "=v"(r) : "v"(a), "v"(b)); return r; }
DEVI float fadd1(float a, float b) { float r; asm("v_add_f32 %0, %1, %2" : "=v"(r) : "v"(a), "v"(b)); return r; }
DEVI float fsub1(float a, float b) { float r; asm("v_sub_f32 %0, %1, %2" : "=v"(r) : "v"(a), "v"(b)); return r; }
DEVI void fmac1(float& c, float a, float b) { asm("v_fmac_f32 %0, %1, %2" : "+v"(c) : "v"(a), "v"(b)); }
template <int NV, int NL>
DEVI void vlwait() { asm volatile("s_waitcnt vmcnt(%0) lgkmcnt(%1)" ::"n"(NV), "n"(NL) : "memory"); }
template <int N>
DEVI void vbar() { asm volatile("s_waitcnt vmcnt(%0)\n\ts_barrier" ::"n"(N) : "memory"); }
DEVI float xhalf_sum(float v) {
  const unsigned u = __builtin_bit_cast(unsigned, v);
  const f32x2 r = __builtin_bit_cast(f32x2, __builtin_amdgcn_permlane32_swap(u, u, false, false));
  return fadd1(r[0], r[1]);
}
#define F4(x) __builtin_bit_cast(f32x4, x)
#define SB() __builtin_amdgcn_sched_barrier(0)
#define BF8(x) __builtin_bit_cast(bf16x8, x)
#define IC(v) std::integral_constant<int, (v)>{}

template <int I, int N, class F>
DEVI void static_for(F&& f) {
  if constexpr (I < N) {
    f(std::integral_constant<int, I>{});
    static_for<I + 1, N>(f);
  }
}

#define MFMA32(a, b, c) __builtin_amdgcn_mfma_f32_32x32x16_bf16(a, b, c, 0, 0, 0)
#define MFMA16(a, b, c) __builtin_amdgcn_mfma_f32_16x16x32_bf16(a, b, c, 0, 0, 0)

DEVI void mod_unit(const uint16_t* __restrict__ cvec, const uint16_t* __restrict__ wmod, char* __restrict__ ws, int col0,
                   int t, float* sm) {
  const int lane = t & 63, w = t >> 6, q4 = t & 3, rs = t >> 2;
  const uint16_t* mp = wmod + (size_t)rs * 2688 + col0 + q4 * 8;
  u32x4 mv[7];
  float cs[7];
#pragma unroll
  for (int i = 0; i < 7; ++i) mv[i] = *(const u32x4*)(mp + (size_t)i * 64 * 2688);
#pragma unroll
  for (int i = 0; i < 7; ++i) {
    float cv = bflo((unsigned)cvec[rs + 64 * i]);
    cs[i] = rbf(cv / (1.f + __expf(-cv)));
  }
  float acc[8];
#pragma unroll
  for (int j = 0; j < 8; ++j) acc[j] = 0.f;
#pragma unroll
  for (int i = 0; i < 7; ++i)
#pragma unroll
    for (int j = 0; j < 4; ++j) {
      acc[2 * j] += cs[i] * bflo(mv[i][j]);
      acc[2 * j + 1] += cs[i] * bfhi(mv[i][j]);
    }
#pragma unroll
  for (int j = 0; j < 8; ++j) {
    acc[j] += __shfl_xor(acc[j], 4);
    acc[j] += __shfl_xor(acc[j], 8);
    acc[j] += __shfl_xor(acc[j], 16);
    acc[j] += __shfl_xor(acc[j], 32);
  }
  if (lane < 4) {
#pragma unroll
    for (int j = 0; j < 8; ++j) sm[w * 32 + lane * 8 + j] = acc[j];
  }
  __syncthreads();
  if (t < 32) {
    const float m = rbf(sm[t] + sm[32 + t] + sm[64 + t] + sm[96 + t]);
    const int part = col0 / 448, k = col0 % 448 + t;
    if (part == 2 || part == 5) {
      ((float*)(ws + OFF_GAB))[(part == 5 ? 448 : 0) + k] = m;
    } else {
      float* dst = (float*)(ws + (part >= 3 ? OFF_MODB : OFF_MODA)) + (k >> 3) * 16 + (k & 7);
      if (part == 1 || part == 4) dst[0] = rbf(1.f + m);
      else if (part == 3) dst[8] = m;
      else ((uint16_t*)(ws + OFF_MODA + (k >> 3) * 64 + 32))[k & 7] = (uint16_t)pack2(m, m);
    }
  }
}

DEVI void retile_unit(const uint16_t* __restrict__ wsrc, char* __restrict__ ws, int role, int kr, int sub, int t) {
  const int k0 = kr * 32;
  const int koct = t / 56, coct = t % 56;
  const int ldw = role == 0 ? 1344 : 448, c0 = role == 0 ? sub * 448 : 0;
  if (t < 224) {
    u32x4 wv[8];
    const uint16_t* p = wsrc + (size_t)(k0 + koct * 8) * ldw + c0 + coct * 8;
#pragma unroll
    for (int i = 0; i < 8; ++i) wv[i] = *(const u32x4*)(p + (size_t)i * ldw);
    const int ksl = koct >> 1, half = koct & 1;
#pragma unroll
    for (int c = 0; c < 8; ++c) {
      u32x4 o;
#pragma unroll
      for (int i = 0; i < 4; ++i)
        o[i] = __builtin_amdgcn_perm(wv[2 * i + 1][c >> 1], wv[2 * i][c >> 1], (c & 1) ? 0x07060302u : 0x05040100u);
      const int n = coct * 8 + c;
      char* dst;
      if (role == 0) {
        const int head = n / 28, d = n % 28;
        const int idx = (sub < 2) ? swap23(d) : d;
        dst = ws + OFF_WQKV + ((size_t)(head * 84 + ((2 * kr + ksl) & 3) * 21 + (kr >> 1) * 3 + sub) * 64 + half * 32 + idx) * 16;
      } else if (role == 2) {
        dst = ws + OFF_WO + ((size_t)((n >> 5) * 28 + 2 * kr + ksl) * 64 + half * 32 + swap23(n & 31)) * 16;
      } else {
        dst = ws + OFF_WD + ((size_t)((n >> 5) * 112 + 2 * kr + ksl) * 64 + half * 32 + swap23(n & 31)) * 16;
      }
      ST16(dst, o);
    }
  } else if (role == 0) {
    const int z = t - 224;
#pragma unroll
    for (int i = 0; i < 8; ++i) {
      const int pc = z * 8 + i, head = pc >> 4, d = 28 + ((pc >> 2) & 3), ko = pc & 3;
      const int idx = (sub < 2) ? swap23(d) : d;
      char* dst = ws + OFF_WQKV + ((size_t)(head * 84 + ((2 * kr + (ko >> 1)) & 3) * 21 + (kr >> 1) * 3 + sub) * 64 + (ko & 1) * 32 + idx) * 16;
      *(u32x4*)dst = u32x4{0, 0, 0, 0};
    }
  }
}

#define PARG
#define PPASS
DEVI void fold_unit(const uint16_t* __restrict__ wsrc, char* __restrict__ ws, int mat, int kr, int cq, int t, char* red PARG) {
  const int ko = t >= 224, c2 = t - 224 * ko, oct = kr * 2 + ko;
  if (t < 448) {
    unsigned wv[8];
    const uint16_t* p = wsrc + (size_t)(oct * 8) * 1792 + cq * 448 + c2 * 2;
#pragma unroll
    for (int i = 0; i < 8; ++i) wv[i] = *(const unsigned*)(p + (size_t)i * 1792);
    const f32x4* tp = (const f32x4*)(ws + OFF_MODB + oct * 64);
    const f32x4 t0 = tp[0], t1 = tp[1], t2 = tp[2], t3 = tp[3];
    const float sc[8] = {t0[0], t0[1], t0[2], t0[3], t1[0], t1[1], t1[2], t1[3]};
    const float sh[8] = {t2[0], t2[1], t2[2], t2[3], t3[0], t3[1], t3[2], t3[3]};
    f32x2 pr[2];
    asm volatile("s_waitcnt vmcnt(0)\n\ts_setprio 3" ::: "memory");
    STAMP(3);
    u32x4 oc[2];
#pragma unroll
    for (int c = 0; c < 2; ++c) {
      u32x4 o;
      float bs = 0.f, cs = 0.f;
#pragma unroll
      for (int i = 0; i < 4; ++i) {
        const float a = c ? bfhi(wv[2 * i]) : bflo(wv[2 * i]);
        const float b = c ? bfhi(wv[2 * i + 1]) : bflo(wv[2 * i + 1]);
        bs += a * sh[2 * i] + b * sh[2 * i + 1];
        o[i] = pack2(a * sc[2 * i], b * sc[2 * i + 1]);
        cs += bflo(o[i]) + bfhi(o[i]);
      }
      pr[c] = f32x2{cs, bs};
      oc[c] = o;
    }
    {
      const int b1 = (c2 >> 1) & 1;
      u32x4 r;
#pragma unroll
      for (int i = 0; i < 4; ++i)
        r[i] = (unsigned)__builtin_amdgcn_mov_dpp((int)(b1 ? oc[0][i] : oc[1][i]), 0x4e, 0xf, 0xf, true);
      const int fA = cq * 448 + (c2 & ~2) * 2 + b1, fB = fA + 4;
      char* base = ws + OFF_WGU + ((size_t)((fA >> 4) * 29 + kr) * 64 + ko * 32 + mat * 16) * 16;
      st16w<2>(base + swap23(fA & 15) * 16, b1 ? r : oc[0]);
      st16w<2>(base + swap23(fB & 15) * 16, b1 ? oc[1] : r);
    }
    *(f32x4*)(red + ko * 3584 + c2 * 16) = f32x4{pr[0][0], pr[0][1], pr[1][0], pr[1][1]};
  }
  STAMP(4);
  asm volatile("s_waitcnt lgkmcnt(0)\n\ts_barrier" ::: "memory");
  STAMP(6);
  if (t < 224)
    st16w<7>(ws + OFF_PART + ((size_t)kr * 3584 + mat * 1792 + cq * 448) * 8 + t * 16,
             __builtin_bit_cast(u32x4, *(const f32x4*)(red + t * 16) + *(const f32x4*)(red + 3584 + t * 16)));
}

__global__ __launch_bounds__(256) void k0_prep(const uint16_t* __restrict__ x, const uint16_t* __restrict__ wqkv,
                                               const uint16_t* __restrict__ cvec, const uint16_t* __restrict__ wmod,
                                               char* __restrict__ ws) {
  __shared__ __attribute__((aligned(16))) float sm[512];
  const int b = blockIdx.x, t = threadIdx.x, lane = t & 63, w = t >> 6;
  PDECL; STAMP(0);
  if (b < 72) {
    const int u = (b & 7) * 9 + (b >> 3);
    if (u < 70) mod_unit(cvec, wmod, ws, u * 32, t, sm);
  } else if (b < 136) {
    const int mt = b - 72, tok = lane & 31, half = lane >> 5;
    const uint16_t* src = x + (size_t)(mt * 32 + tok) * 448 + (7 * w) * 16 + half * 8;
    u32x4 v[7];
#pragma unroll
    for (int i = 0; i < 7; ++i) v[i] = *(const u32x4*)(src + i * 16);
    float s = 0.f;
#pragma unroll
    for (int i = 0; i < 7; ++i)
#pragma unroll
      for (int j = 0; j < 4; ++j) s += bflo(v[i][j]) + bfhi(v[i][j]);
    const float lm = s * (1.f / 56.f);
    float m2 = 0.f;
#pragma unroll
    for (int i = 0; i < 7; ++i)
#pragma unroll
      for (int j = 0; j < 4; ++j) {
        float a = bflo(v[i][j]) - lm, c = bfhi(v[i][j]) - lm;
        m2 += a * a + c * c;
      }
    f32x2* part = (f32x2*)sm;
    part[(w * 2 + half) * 32 + tok] = f32x2{lm, m2};
    __syncthreads();
    STAMP(1);
    float mu = 0.f, M2 = 0.f;
    f32x2 p[8];
#pragma unroll
    for (int i = 0; i < 8; ++i) { p[i] = part[i * 32 + tok]; mu += p[i][0]; M2 += p[i][1]; }
    mu *= 0.125f;
#pragma unroll
    for (int i = 0; i < 8; ++i) { float d = p[i][0] - mu; M2 += 56.f * d * d; }
    const float rstd = rsqrtf(M2 * (1.f / 448.f) + 1e-5f);
    char* dst = ws + OFF_XN + ((size_t)(mt * 28 + 7 * w) * 64 + lane) * 16;
#pragma unroll
    for (int i = 0; i < 7; ++i) {
      u32x4 o;
#pragma unroll
      for (int j = 0; j < 4; ++j) o[j] = pack2((bflo(v[i][j]) - mu) * rstd, (bfhi(v[i][j]) - mu) * rstd);
      st16w<0>(dst + i * 1024, o);
    }
  } else {
    const int rb = b - 136;
    retile_unit(wqkv, ws, 0, rb / 3, rb % 3, t);
  }
  PFLUSH(0);
}

constexpr int k1_posW(int c) { return c == 0 ? 3 : c == 1 ? 10 : c == 2 ? 13 : 20 + 7 * (c - 3); }
constexpr int k1_posX(int k) { return (k < 4 ? 4 : k < 8 ? 10 : k < 24 ? 13 + 3 * (k / 4 - 2) : 22) + k; }
constexpr int k1_iss(int k) { return 18 + (2 * k < 32 ? 2 * k : 32); }
__global__ __launch_bounds__(256) __attribute__((amdgpu_waves_per_eu(1, 1))) void k1_qkv(char* __restrict__ ws) {
  extern __shared__ __attribute__((aligned(16))) char lds[];
  const int t = threadIdx.x, lane = t & 63, w = __builtin_amdgcn_readfirstlane(t >> 6);
  PDECL; STAMP(0);
  const int xc = blockIdx.x & 7, bi = blockIdx.x >> 3;
  const int mb = (xc & 3) * 4 + (bi & 3), head = (xc >> 2) * 8 + (bi >> 2);
  const int mt = mb * 4 + w;
  const int hi = lane >> 5, l31 = lane & 31;
  const char* A0 = ws + OFF_XN + (size_t)mt * 28 * 1024 + lane * 16 + 4096;
  const char* B0 = ws + OFF_WQKV + ((size_t)head * 84 + w * 21) * 1024 + lane * 16 + 4096;
  const char* A[4] = {A0, A0 + 8192, A0 + 16384, A0 + 24576};
  const char* B[3] = {B0, B0 + 8192, B0 + 16384};
  char* modv = lds + 87 * 1024 + w * 1024;
  float* P = (float*)(lds + 91 * 1024);
  GLDS16(ws + OFF_MODA + (4 * (lane >> 3) + w) * 128 + (lane & 7) * 16, modv);
  u32x4 xr[29], sw[7][3];
  auto ldW = [&](auto cc, auto ic) {
    constexpr int c = decltype(cc)::value, i = decltype(ic)::value;
    gload16o<((c * 3 + i) & 7) * 1024 - 4096>(sw[c][i], B[(c * 3 + i) >> 3]);
  };
  auto ldX = [&](auto kc) {
    constexpr int k = decltype(kc)::value;
    gload16ao<(k & 7) * 1024 - 4096>(xr[k], A[k >> 3]);
  };
  auto ldN = [&](auto nc) {
    constexpr int n = decltype(nc)::value, g = n / 7, r = n % 7;
    if constexpr (n >= 35) { if constexpr (n < 39) ldX(IC(24 + n - 35)); }
    else if constexpr (r < 3) ldW(IC(g + 2), IC(r));
    else ldX(IC(4 * (g + 1) + r - 3));
  };
  static_for<0, 3>([&](auto i) { ldW(IC(0), i); });
  static_for<0, 4>(ldX);
  static_for<0, 3>([&](auto i) { ldW(IC(1), i); });
  f32x16 acc[3], accb[3];
#pragma unroll
  for (int i = 0; i < 3; ++i)
#pragma unroll
    for (int r = 0; r < 16; ++r) { acc[i][r] = 0.f; accb[i][r] = 0.f; }
  const unsigned wl0 = lds_addr(lds) + lane * 16, wl1 = wl0 + 49152;
  const unsigned sa0 = wl0 + w * 3072, sa1 = sa0 + 49152;
  const unsigned ml = lds_addr(modv) + hi * 64;
  u32x4 wb[3][3], mv[7][3], so[3];
  auto rdw = [&](auto kc) {
    constexpr int k = decltype(kc)::value;
    constexpr int o = (k & 15) * 3072;
    const unsigned a = (k >= 16) ? wl1 : wl0;
    lread16<o>(wb[k % 3][0], a); lread16<o + 1024>(wb[k % 3][1], a); lread16<o + 2048>(wb[k % 3][2], a);
  };
  auto rdm = [&](auto cc) {
    constexpr int c = decltype(cc)::value;
    lread16<c * 128>(mv[c][0], ml); lread16<c * 128 + 16>(mv[c][1], ml);
    lread16<c * 128 + 32>(mv[c][2], ml);
  };
  auto rdym = [&](auto cc) {
    constexpr int c = decltype(cc)::value;
    rdyv(mv[c][0]); rdyv(mv[c][1]); rdyv(mv[c][2]);
  };
  auto word = [&](auto cc, auto qc) {
    constexpr int c = decltype(cc)::value, q = decltype(qc)::value, i = q >> 2, j = q & 3;
    if constexpr (j == 0) accb[i] = MFMA32(BF8(mv[c][2]), BF8(sw[c][i]), accb[i]);
    const f32x4 sc = F4(mv[c][j >> 1]);
    so[i][j] = scale2(sw[c][i][j], sc[(2 * j) & 3], sc[(2 * j + 1) & 3]);
    if constexpr (j == 3) lwrite16<(c & 3) * 12288 + i * 1024>(so[i], c < 4 ? sa0 : sa1);
  };
  PDECL2;
  BST(0);
  vwait<7>();
  BST(8);
  rdyv(sw[0][0]); rdyv(sw[0][1]); rdyv(sw[0][2]);
  rdm(IC(0)); rdm(IC(1)); rdm(IC(2));
  ldN(IC(0));
  lwait<0>();
  static_for<0, 3>(rdym);
  static_for<0, 12>([&](auto q) {
    word(IC(0), q);
    if constexpr (q % 2 == 0) ldN(IC(1 + q / 2));
    SB();
  });
  wait_bar<7>();
  STAMP(1);
  rdya(xr[0]);
  rdyv(sw[1][0]); rdyv(sw[1][1]); rdyv(sw[1][2]);
  rdw(IC(0)); rdw(IC(1));
  lwait<3>();
  rdyv(wb[0][0]); rdyv(wb[0][1]); rdyv(wb[0][2]);
  xr[28] = u32x4{0x3f803f80u, 0, 0, 0};
  SB();
  static_for<0, 29>([&](auto kc) {
    constexpr int k = decltype(kc)::value;
    constexpr int cn = (k < 2) ? 1 : (k + 6) / 4, r = (k + 6) % 4;
    constexpr bool PR = k < 22;
    auto gap = [&](auto gc) {
      constexpr int g = decltype(gc)::value;
      if constexpr (k < 2) { word(IC(1), IC(6 * k + 2 * g)); word(IC(1), IC(6 * k + 2 * g + 1)); }
      else if constexpr (PR) word(IC(cn), IC(3 * r + g));
    };
    acc[0] = MFMA32(BF8(wb[k % 3][0]), BF8(xr[k]), acc[0]);
    constexpr bool BS = (k + 2) % 4 == 0;
    if constexpr (BS && k < 22) {
      constexpr int j = (k + 2) / 4;
      vwait<k1_iss(k) - 1 - k1_posW(j + 1)>();
      rdyv(sw[j + 1][0]); rdyv(sw[j + 1][1]); rdyv(sw[j + 1][2]);
    }
    if constexpr (k >= 2 && r == 2 && cn + 1 < 7) rdm(IC(cn + 1));
    if constexpr (!BS && k + 2 < 29) rdw(IC(k + 2));
    gap(IC(0));
    SB();
    acc[1] = MFMA32(BF8(wb[k % 3][1]), BF8(xr[k]), acc[1]);
    if constexpr (BS) {
      BST((k + 2) / 4);
      wait_bar<63>();
      BST(8 + (k + 2) / 4);
      rdw(IC(k + 2));
    }
    ldN(IC(7 + 2 * k));
    gap(IC(1));
    if constexpr (k == 23) {
      if (w < 3) {
        float bs = P[(w * 4 + 0) * 64 + lane] + P[(w * 4 + 1) * 64 + lane] + P[(w * 4 + 2) * 64 + lane] + P[(w * 4 + 3) * 64 + lane];
        const float bh = rbf(bs);
        *(u32x4*)(lds + (84 + w) * 1024 + lane * 16) = u32x4{hi ? 0u : pack2(bh, bs - bh), 0, 0, 0};
      }
    }
    SB();
    acc[2] = MFMA32(BF8(xr[k]), BF8(wb[k % 3][2]), acc[2]);
    ldN(IC(8 + 2 * k));
    gap(IC(2));
    if constexpr (k == 21) {
#pragma unroll
      for (int i = 0; i < 3; ++i) P[(i * 4 + w) * 64 + lane] = accb[i][0];
    }
    if constexpr (k + 1 < 29) {
      constexpr int nw = k == 0 ? 1 : k == 1 ? 2 : k == 21 ? 4 : (PR && r != 0) ? 1 : 0;
      vlwait<(k + 1 < 28) ? k1_iss(k + 1) - 1 - k1_posX(k + 1) : 63, (k + 2 < 29) ? 3 + nw : 0>();
      if constexpr (k + 1 < 28) rdya(xr[k + 1]);
      rdyv(wb[(k + 1) % 3][0]); rdyv(wb[(k + 1) % 3][1]); rdyv(wb[(k + 1) % 3][2]);
      if constexpr (k >= 2 && r == 2 && cn + 1 < 7) rdym(IC(cn + 1));
    }
    SB();
  });
  STAMP(2);
  const float qs = 0.18898223650461362f * 1.4426950408889634f;
#pragma unroll
  for (int p = 0; p < 2; ++p) {
    char* base = ws + (p ? OFF_K : OFF_Q) + ((size_t)(head * 128 + mt * 2 + (l31 >> 4)) * 64 + (l31 & 15)) * 16;
    const float sc = p ? 1.f : qs;
#pragma unroll
    for (int s = 0; s < 2; ++s) {
      u32x4 o;
#pragma unroll
      for (int j = 0; j < 4; ++j) o[j] = pack2(acc[p][8 * s + 2 * j] * sc, acc[p][8 * s + 2 * j + 1] * sc);
      st16w<1>(base + (2 * s + hi) * 256, o);
    }
  }
  {
    char* base = ws + OFF_V + ((size_t)((head * 64 + mt) * 2 + (l31 >> 4)) * 64 + (l31 & 15)) * 16;
#pragma unroll
    for (int s = 0; s < 2; ++s) {
      u32x4 o = {pack2(acc[2][4 * s], acc[2][4 * s + 1]), pack2(acc[2][4 * s + 2], acc[2][4 * s + 3]),
                 pack2(acc[2][8 + 4 * s], acc[2][8 + 4 * s + 1]), pack2(acc[2][8 + 4 * s + 2], acc[2][8 + 4 * s + 3])};
      if (l31 == 28) o = u32x4{0x3f803f80u, 0x3f803f80u, 0x3f803f80u, 0x3f803f80u};
      st16w<1>(base + (2 * s + hi) * 256, o);
    }
  }
  PFLUSH(1);
  PFLUSH2(6);
}

template <bool ODD>
DEVI void attn_pv(const char* vp, const u32x2 (&p)[17], f32x4& o0, f32x4& o1) {
  bf16x8 vf[9][2];
#pragma unroll
  for (int v = 0; v < 3; ++v) { vf[v][0] = *(const bf16x8*)(vp + v * 2048); vf[v][1] = *(const bf16x8*)(vp + v * 2048 + 1024); }
#pragma unroll
  for (int v = 0; v < 9; ++v) {
    if (v + 3 < 9) {
      vf[v + 3][0] = *(const bf16x8*)(vp + (v + 3) * 2048);
      vf[v + 3][1] = *(const bf16x8*)(vp + (v + 3) * 2048 + 1024);
    }
    const int i0 = 2 * v - (ODD ? 1 : 0), i1 = i0 + 1;
    const u32x2 z = {0, 0};
    const u32x2 pa = i0 >= 0 ? p[i0 >= 0 ? i0 : 0] : z, pc = i1 < 17 ? p[i1 < 17 ? i1 : 0] : z;
    const u32x4 pw = {pa[0], pa[1], pc[0], pc[1]};
    const bf16x8 pf = __builtin_bit_cast(bf16x8, pw);
    o0 = MFMA16(vf[v][0], pf, o0);
    o1 = MFMA16(vf[v][1], pf, o1);
    SB();
  }
}

DEVI void attn_wave(const char* ldsK, const char* ldsV, const char* ldsQ, int w, int lane, int nneg, char* odst, int kbase) {
  const int g = lane >> 4, qq = lane & 15;
  const bf16x8 q = *(const bf16x8*)(ldsQ + w * 1024 + lane * 16);
  const float NEG = -1e30f;
  f32x4 s[17];
  const char* kp = ldsK + w * 1024 + lane * 16;
  bf16x8 kf[17];
#pragma unroll
  for (int tt = 0; tt < 4; ++tt) kf[tt] = *(const bf16x8*)(kp + tt * 1024);
#pragma unroll
  for (int tt = 0; tt < 17; ++tt) {
    if (tt + 4 < 17) kf[tt + 4] = *(const bf16x8*)(kp + (tt + 4) * 1024);
    s[tt] = MFMA16(kf[tt], q, (f32x4{0.f, 0.f, 0.f, 0.f}));
    SB();
  }
#pragma unroll
  for (int r = 0; r < 4; ++r) {
    s[0][r] = (4 * g + r > qq) ? s[0][r] : NEG;
    s[16][r] = (4 * g + r <= qq) ? s[16][r] : NEG;
  }
  if (nneg > 0) {
#pragma unroll
    for (int tt = 0; tt < 16; ++tt)
      if (w + tt < nneg) s[tt] = f32x4{NEG, NEG, NEG, NEG};
  }
  float m = NEG;
#pragma unroll
  for (int tt = 0; tt < 17; ++tt) m = fmaxf(fmaxf(m, fmaxf(s[tt][0], s[tt][1])), fmaxf(s[tt][2], s[tt][3]));
  m = fmaxf(m, __shfl_xor(m, 16));
  m = fmaxf(m, __shfl_xor(m, 32));
  u32x2 pb[17];
#pragma unroll
  for (int tt = 0; tt < 17; ++tt) {
    const float e0 = __builtin_amdgcn_exp2f(s[tt][0] - m), e1 = __builtin_amdgcn_exp2f(s[tt][1] - m);
    const float e2 = __builtin_amdgcn_exp2f(s[tt][2] - m), e3 = __builtin_amdgcn_exp2f(s[tt][3] - m);
    pb[tt] = u32x2{pack2(e0, e1), pack2(e2, e3)};
  }
  wait_bar<0>();
  f32x4 o0 = {0.f, 0.f, 0.f, 0.f}, o1 = {0.f, 0.f, 0.f, 0.f};
  const char* vp = ldsV + (w >> 1) * 2048 + lane * 16;
  if (w & 1) attn_pv<true>(vp, pb, o0, o1);
  else attn_pv<false>(vp, pb, o0, o1);
  const float sum = __shfl(o1[0], 48 + qq);
  const float inv = 1.f / sum;
  {
    const int k = kbase + 4 * g;
    u32x2 o = {pack2(o0[0] * inv, o0[1] * inv), pack2(o0[2] * inv, o0[3] * inv)};
    ST8(odst + (size_t)(k >> 4) * 1024 + ((k >> 3) & 1) * 512 + (k & 4) * 2, o);
  }
  if (g < 3) {
    const int k = kbase + 16 + 4 * g;
    u32x2 o = {pack2(o1[0] * inv, o1[1] * inv), pack2(o1[2] * inv, o1[3] * inv)};
    ST8(odst + (size_t)(k >> 4) * 1024 + ((k >> 3) & 1) * 512 + (k & 4) * 2, o);
  }
}

__global__ __launch_bounds__(512) __attribute__((amdgpu_waves_per_eu(4, 4))) void k2_attn(
    const uint16_t* __restrict__ wo, const uint16_t* __restrict__ wg, const uint16_t* __restrict__ wu,
    char* __restrict__ ws) {
  extern __shared__ __attribute__((aligned(16))) char lds[];
  const int t = threadIdx.x, lane = t & 63, w = __builtin_amdgcn_readfirstlane(t >> 6);
  PDECL; STAMP(0);
  if (__builtin_expect(blockIdx.x >= 256, 0)) {
    const int u = blockIdx.x - 256;
    if (u < 224) fold_unit((u & 4) ? wu : wg, ws, (u >> 2) & 1, u >> 3, u & 3, t, lds PPASS);
    else if (w < 4) retile_unit(wo, ws, 2, u - 224, 0, t);
    PFLUSH(2);
    return;
  }
  const int xc = blockIdx.x & 7, bi = blockIdx.x >> 3;
  const int head = xc * 2 + (bi & 1), qb = bi >> 1;
  char* ldsK = lds;
  char* ldsV = lds + 24 * 1024;
  char* ldsQ = lds + 48 * 1024;
  const int sub0 = qb * 8 - 16;
  GLDS16(ws + OFF_Q + (size_t)(head * 128 + qb * 8 + w) * 1024 + lane * 16, ldsQ + w * 1024);
#pragma unroll
  for (int i = 0; i < 3; ++i) {
    const int p = w * 3 + i;
    const int sidx = max(sub0 + p, 0);
    GLDS16(ws + OFF_K + (size_t)(head * 128 + sidx) * 1024 + lane * 16, ldsK + p * 1024);
  }
#pragma unroll
  for (int i = 0; i < 3; ++i) {
    const int p = w * 3 + i;
    const int sidx = max(sub0 + p, 0);
    GLDS16(ws + OFF_V + (size_t)(head * 128 + sidx) * 1024 + lane * 16, ldsV + p * 1024);
  }
  wait_bar<3>();
  STAMP(1);
  const int tok = qb * 128 + w * 16 + (lane & 15);
  char* odst = ws + OFF_O + (size_t)(tok >> 5) * 28 * 1024 + (tok & 31) * 16;
  const int kbase = head * 28;
  const int nneg = -sub0;
  attn_wave(ldsK, ldsV, ldsQ, w, lane, nneg, odst, kbase);
  PFLUSH(2);
}

DEVI f32x16 ksplit_reduce(f32x16 (&acc)[4], char* lds, int w, int lane) {
#pragma unroll
  for (int tl = 0; tl < 4; ++tl) {
    if (tl != w) {
      const int slot = (w > tl) ? w - 1 : w;
      char* p = lds + (tl * 3 + slot) * 4096 + lane * 16;
#pragma unroll
      for (int q = 0; q < 4; ++q)
        *(f32x4*)(p + q * 1024) = f32x4{acc[tl][4 * q], acc[tl][4 * q + 1], acc[tl][4 * q + 2], acc[tl][4 * q + 3]};
    }
  }
  f32x16 r = (w == 0) ? acc[0] : (w == 1) ? acc[1] : (w == 2) ? acc[2] : acc[3];
  __syncthreads();
#pragma unroll
  for (int s = 0; s < 3; ++s) {
    const char* p = lds + (w * 3 + s) * 4096 + lane * 16;
#pragma unroll
    for (int q = 0; q < 4; ++q) {
      const f32x4 v = *(const f32x4*)(p + q * 1024);
#pragma unroll
      for (int j = 0; j < 4; ++j) r[4 * q + j] += v[j];
    }
  }
  return r;
}

__global__ __launch_bounds__(256) __attribute__((amdgpu_waves_per_eu(1, 1))) void k3_wo(const uint16_t* __restrict__ x,
                                                                                        char* __restrict__ ws) {
  __shared__ __attribute__((aligned(16))) char lds[48 * 1024];
  const int t = threadIdx.x, lane = t & 63, w = __builtin_amdgcn_readfirstlane(t >> 6);
  PDECL; STAMP(0);
  if (__builtin_expect(blockIdx.x >= 224, 0)) {
    if (t < 224) {
      const int z = t >= 112, col = (blockIdx.x - 224) * 112 + t - 112 * z;
      f32x2 v = {0.f, 0.f};
#pragma unroll
      for (int i = 0; i < 28; ++i) v += *(const f32x2*)(ws + OFF_PART + ((size_t)i * 3584 + col) * 8);
      const float ch = rbf(v[0]), cl = v[0] - ch, bh = rbf(v[1]), bl = v[1] - bh;
      u32x4 o = {pack2(ch, ch), pack2(cl, cl), pack2(bh, bh), pack2(bl, bl)};
      if (z) o = u32x4{0, 0, 0, 0};
      const int mat = col >= 1792, f = col - 1792 * mat;
      ST16(ws + OFF_WGU + ((size_t)((f >> 4) * 29 + 28) * 64 + z * 32 + mat * 16 + swap23(f & 15)) * 16, o);
    }
    PFLUSH(3);
    return;
  }
  const int xc = blockIdx.x & 7, bi = blockIdx.x >> 3;
  const int mb = xc * 4 + (bi & 3), nb = bi >> 2;
  const char* A = ws + OFF_O + ((size_t)mb * 2 * 28 + 7 * w) * 1024 + lane * 16;
  const char* B = ws + OFF_WO + ((size_t)nb * 2 * 28 + 7 * w) * 1024 + lane * 16;
  const int wm = w & 1, wn = w >> 1;
  const int hi = lane >> 5, l31 = lane & 31;
  const int mt = mb * 2 + wm, nt = nb * 2 + wn;
  bf16x8 a[7][2], bw[7][2];
#pragma unroll
  for (int k = 0; k < 7; ++k) {
    a[k][0] = *(const bf16x8*)(A + k * 1024);
    bw[k][0] = *(const bf16x8*)(B + k * 1024);
    a[k][1] = *(const bf16x8*)(A + (28 + k) * 1024);
    bw[k][1] = *(const bf16x8*)(B + (28 + k) * 1024);
  }
  u32x4 xv[2];
  f32x4 gv[4];
  {
    const uint16_t* xs = x + (size_t)(mt * 32 + l31) * 448 + nt * 32 + hi * 8;
    xv[0] = *(const u32x4*)xs;
    xv[1] = *(const u32x4*)(xs + 16);
    const float* gp = (const float*)(ws + OFF_GAB) + nt * 32 + hi * 8;
    gv[0] = *(const f32x4*)gp; gv[1] = *(const f32x4*)(gp + 4);
    gv[2] = *(const f32x4*)(gp + 16); gv[3] = *(const f32x4*)(gp + 20);
  }
  __builtin_amdgcn_sched_barrier(0);
  f32x16 acc[4];
#pragma unroll
  for (int i = 0; i < 4; ++i)
#pragma unroll
    for (int r = 0; r < 16; ++r) acc[i][r] = 0.f;
#pragma unroll
  for (int k = 0; k < 7; ++k) {
    acc[0] = MFMA32(bw[k][0], a[k][0], acc[0]);
    acc[1] = MFMA32(bw[k][0], a[k][1], acc[1]);
    acc[2] = MFMA32(bw[k][1], a[k][0], acc[2]);
    acc[3] = MFMA32(bw[k][1], a[k][1], acc[3]);
  }
  STAMP(1);
  const f32x16 r = ksplit_reduce(acc, lds, w, lane);
  STAMP(2);
  float v[16];
#pragma unroll
  for (int s = 0; s < 2; ++s) {
    u32x4 o;
#pragma unroll
    for (int j = 0; j < 4; ++j) {
      const float ga0 = gv[2 * s + (j >> 1)][(2 * j) & 3], ga1 = gv[2 * s + (j >> 1)][(2 * j + 1) & 3];
      o[j] = pack2(bflo(xv[s][j]) + ga0 * r[8 * s + 2 * j], bfhi(xv[s][j]) + ga1 * r[8 * s + 2 * j + 1]);
      v[8 * s + 2 * j] = bflo(o[j]);
      v[8 * s + 2 * j + 1] = bfhi(o[j]);
    }
    st16w<3>(ws + OFF_X1 + ((size_t)(mt * 28 + nt * 2 + s) * 64 + lane) * 16, o);
  }
  float sm = 0.f;
#pragma unroll
  for (int j = 0; j < 16; ++j) sm += v[j];
  sm += __shfl_xor(sm, 32);
  const float mean = sm * (1.f / 32.f);
  float m2 = 0.f;
#pragma unroll
  for (int j = 0; j < 16; ++j) { const float d = v[j] - mean; m2 += d * d; }
  m2 += __shfl_xor(m2, 32);
  if (hi == 0) *(f32x2*)(ws + OFF_STATS + ((size_t)(mt * 8 + nb) * 32 + l31) * 16 + wn * 8) = f32x2{mean, m2};
  PFLUSH(3);
}

constexpr int k4_posG(int g) { return g < 2 ? g : g < 4 ? g + 2 : g <= 9 ? 8 + 3 * (g - 4) : g == 10 ? 30 : 4 + 3 * g; }
constexpr int k4_posX(int s) { return s == 0 ? 3 : s == 1 ? 7 : s <= 6 ? 10 + 3 * (s - 2) : s == 7 ? 27 : s == 8 ? 34 : s < 27 ? 12 + 3 * s : 92; }
constexpr int k4_iss(int k) { return k == 0 ? 23 : k == 1 ? 30 : k <= 20 ? 31 + 3 * k : 93; }
__global__ __launch_bounds__(256) __attribute__((amdgpu_waves_per_eu(1, 1))) void k4_gu(
    const uint16_t* __restrict__ wd, const uint16_t* __restrict__ cvec, const uint16_t* __restrict__ wmod,
    char* __restrict__ ws) {
  extern __shared__ __attribute__((aligned(16))) char lds[];
  const int t = threadIdx.x, lane = t & 63, w = __builtin_amdgcn_readfirstlane(t >> 6);
  PDECL; STAMP(0);
  if (__builtin_expect(blockIdx.x >= 224, 0)) {
    const int j = blockIdx.x - 224;
    u32x4 wv[2][8];
#pragma unroll
    for (int h = 0; h < 2; ++h) {
      const int id = min(t + 256 * h, 391);
      const uint16_t* p = wd + (size_t)((j * 7 + id / 56) * 8) * 448 + (id % 56) * 8;
#pragma unroll
      for (int i = 0; i < 8; ++i) wv[h][i] = *(const u32x4*)(p + (size_t)i * 448);
    }
    const int v = (j & 7) * 2 + (j >> 3);
    if (j < 16 && v < 14) mod_unit(cvec, wmod, ws, 5 * 448 + v * 32, t, (float*)lds);
#pragma unroll
    for (int h = 0; h < 2; ++h) {
      const int id = t + 256 * h, ko = j * 7 + id / 56, coct = id % 56;
      if (id < 392) {
#pragma unroll
        for (int c = 0; c < 8; ++c) {
          u32x4 o;
#pragma unroll
          for (int i = 0; i < 4; ++i)
            o[i] = __builtin_amdgcn_perm(wv[h][2 * i + 1][c >> 1], wv[h][2 * i][c >> 1], (c & 1) ? 0x07060302u : 0x05040100u);
          const int n = coct * 8 + c;
          ST16(ws + OFF_WD + ((size_t)((n >> 5) * 112 + (ko >> 1)) * 64 + (ko & 1) * 32 + swap23(n & 31)) * 16, o);
        }
      }
    }
    PFLUSH(4);
    return;
  }
  const int xc = blockIdx.x & 7, bi = blockIdx.x >> 3;
  const int mb = (xc & 1) * 4 + (bi & 3), fb = (xc >> 1) * 7 + (bi >> 2);
  const int mt = mb * 8 + 2 * w;
  const char* A = ws + OFF_X1 + (size_t)mt * 28 * 1024 + lane * 16;
  const char* B = ws + OFF_WGU + (size_t)(fb * 4 + w) * 29 * 1024 + lane * 16;
  const int hi = lane >> 5, l31 = lane & 31;
  u32x4 xr[29][2], st[2][4];
  auto ldG = [&](auto gc) {
    constexpr int g = decltype(gc)::value;
    GLDS16(B + g * 1024, lds + (g * 4 + w) * 1024);
  };
  auto ldXa = [&](auto sc) { constexpr int s = decltype(sc)::value; gload16a(xr[s][0], A + s * 1024); };
  auto ldXb = [&](auto sc) { constexpr int s = decltype(sc)::value; gload16a(xr[s][1], A + (28 + s) * 1024); };
  ldG(IC(0)); ldG(IC(1)); ldXa(IC(0)); ldXb(IC(0)); ldG(IC(2)); ldG(IC(3)); ldXa(IC(1)); ldXb(IC(1));
  const char* S = ws + OFF_STATS + (size_t)mt * 4096 + (hi * 4 * 32 + l31) * 16;
  static_for<2, 7>([&](auto sc) { ldG(IC(sc + 2)); ldXa(sc); ldXb(sc); });
  f32x16 acc[2][4];
#pragma unroll
  for (int i = 0; i < 8; ++i)
#pragma unroll
    for (int r = 0; r < 16; ++r) acc[i >> 2][i & 3][r] = 0.f;
  const unsigned wa0 = lds_addr(lds) + lane * 16, wa1 = wa0 + 65536;
  u32x4 wb[3][4];
  float rstd[2], var[2], ta[4], tb[4], d[8], mu;
  f32x4 p[4];
  auto stat = [&](auto tc, auto sc) {
    constexpr int tt = decltype(tc)::value, q = decltype(sc)::value;
    if constexpr (q == 0) {
#pragma unroll
      for (int i = 0; i < 4; ++i) p[i] = F4(st[tt][i]);
      if (hi) p[3] = f32x4{0.f, 0.f, 0.f, 0.f};
    } else if constexpr (q == 1 || q == 2) {
#pragma unroll
      for (int i = 2 * q - 2; i < 2 * q; ++i) { ta[i] = fadd1(p[i][0], p[i][2]); tb[i] = fadd1(p[i][1], p[i][3]); }
    } else if constexpr (q == 3) {
      ta[0] = fadd1(ta[0], ta[1]); ta[2] = fadd1(ta[2], ta[3]); tb[0] = fadd1(tb[0], tb[1]); tb[2] = fadd1(tb[2], tb[3]);
    } else if constexpr (q == 4) {
      ta[0] = fadd1(ta[0], ta[2]); tb[0] = fadd1(tb[0], tb[2]);
    } else if constexpr (q == 5) {
      ta[0] = xhalf_sum(ta[0]);
    } else if constexpr (q == 6) {
      mu = fmul1(ta[0], 1.f / 14.f);
      if (hi) { p[3][0] = mu; p[3][2] = mu; }
    } else if constexpr (q == 7 || q == 8) {
#pragma unroll
      for (int i = 2 * q - 14; i < 2 * q - 12; ++i) { d[2 * i] = fsub1(p[i][0], mu); d[2 * i + 1] = fsub1(p[i][2], mu); }
    } else if constexpr (q == 9) {
#pragma unroll
      for (int i = 0; i < 4; ++i) ta[i] = fmul1(d[i], d[i]);
    } else if constexpr (q == 10) {
#pragma unroll
      for (int i = 0; i < 4; ++i) fmac1(ta[i], d[4 + i], d[4 + i]);
    } else if constexpr (q == 11) {
      ta[0] = fadd1(ta[0], ta[1]); ta[2] = fadd1(ta[2], ta[3]);
      ta[1] = rbf(mu);
    } else if constexpr (q == 12) {
      fmac1(tb[0], fadd1(ta[0], ta[2]), 32.f);
    } else if constexpr (q == 13) {
      tb[0] = xhalf_sum(tb[0]);
    } else if constexpr (q == 14) {
      var[tt] = fmaf(tb[0], 1.f / 448.f, 1e-5f);
      rstd[tt] = __builtin_amdgcn_rsqf(var[tt]);
    } else if constexpr (q == 15) {
      ta[0] = fmul1(var[tt], rstd[tt]);
      ta[3] = fsub1(ta[1], mu);
    } else if constexpr (q == 16) {
      ta[2] = rbf(ta[0]);
    } else if constexpr (q == 17) {
      const unsigned pm = pack2(-ta[1], ta[3]), ps = pack2(ta[2], fsub1(ta[0], ta[2]));
      xr[28][tt] = u32x4{pm, pm, ps, ps};
    }
  };
  auto rdw = [&](auto kc) {
    constexpr int k = decltype(kc)::value;
    constexpr int o = (k & 15) * 4096;
    const unsigned a = (k >= 16) ? wa1 : wa0;
    lread16<o>(wb[k % 3][0], a); lread16<o + 1024>(wb[k % 3][1], a);
    lread16<o + 2048>(wb[k % 3][2], a); lread16<o + 3072>(wb[k % 3][3], a);
  };
  PDECL2;
  BST(0);
  vbar<21>();
  BST(8);
  STAMP(1);
  rdw(IC(0)); rdw(IC(1));
  vlwait<19, 4>();
  rdya(xr[0][0]); rdya(xr[0][1]);
  rdyv(wb[0][0]); rdyv(wb[0][1]); rdyv(wb[0][2]); rdyv(wb[0][3]);
  SB();
  static_for<0, 29>([&](auto kc) {
    constexpr int k = decltype(kc)::value;
    static_for<0, 8>([&](auto gc) {
      constexpr int g = decltype(gc)::value;
      acc[g & 1][g >> 1] = MFMA32(BF8(wb[k % 3][g >> 1]), BF8(xr[k][g & 1]), acc[g & 1][g >> 1]);
      if constexpr (g == 0) {
        if constexpr (k % 2 == 0 && k + 2 < 29) {
          if constexpr (k % 4 == 0) BST(1 + k / 4);
          vbar<k4_iss(k) - 1 - k4_posG(k + 3 < 29 ? k + 3 : 28)>();
          if constexpr (k % 4 == 0) BST(9 + k / 4);
        }
        if constexpr (k + 2 < 29) rdw(IC(k + 2));
      }
      if constexpr (g == 1 && k + 9 < 29) ldG(IC(k + 9));
      if constexpr (g == 3 && k + 7 < 28) ldXa(IC(k + 7));
      if constexpr (g == 5 && k + 7 < 28) ldXb(IC(k + 7));
      if constexpr (k < 2 && (g == 2 || g == 4 || g >= 6)) gload16(st[k & 1][g == 7 ? 3 : g / 2 - 1], S + k * 4096 + (g == 7 ? 3 : g / 2 - 1) * 512);
      if constexpr (k >= 9 && k < 14) stat(IC((((k - 9) * 8 + g) / 20) & 1), IC(((k - 9) * 8 + g) % 20));
      if constexpr (g == 7 && k + 1 < 29) {
        vlwait<(k + 1 < 28) ? k4_iss(k + 1) - 1 - k4_posX(k + 1) : 63, (k + 2 < 29) ? 4 : 0>();
        if constexpr (k + 1 < 28) { rdya(xr[k + 1][0]); rdya(xr[k + 1][1]); }
        rdyv(wb[(k + 1) % 3][0]); rdyv(wb[(k + 1) % 3][1]); rdyv(wb[(k + 1) % 3][2]); rdyv(wb[(k + 1) % 3][3]);
        if constexpr (k == 8) {
#pragma unroll
          for (int i = 0; i < 8; ++i) rdyv(st[i >> 2][i & 3]);
        }
      }
      SB();
    });
  });
  STAMP(2);
  const float c0 = -2.f * 0.7978845608028654f * 1.4426950408889634f, c1 = c0 * 0.044715f;
  char* hdst = ws + OFF_HFF + ((size_t)(mt * 112 + fb * 4) * 64 + lane) * 16;
#pragma unroll
  for (int tt = 0; tt < 2; ++tt)
#pragma unroll
    for (int n = 0; n < 4; ++n) {
      const float e0 = c0 * rstd[tt], e1 = c1 * rstd[tt] * rstd[tt] * rstd[tt];
      float hh[8];
#pragma unroll
      for (int j = 0; j < 8; ++j) {
        const float g = acc[tt][n][j], u = acc[tt][n][8 + j];
        const float e = __builtin_amdgcn_exp2f(g * fmaf(g * g, e1, e0));
        hh[j] = g * u * __builtin_amdgcn_rcpf(fmaf(e, var[tt], var[tt]));
      }
      u32x4 o = {pack2(hh[0], hh[1]), pack2(hh[2], hh[3]), pack2(hh[4], hh[5]), pack2(hh[6], hh[7])};
      st16w<4>(hdst + (tt * 112 + n) * 1024, o);
    }
  PFLUSH(4);
  PFLUSH2(8);
}

__global__ __launch_bounds__(256) __attribute__((amdgpu_waves_per_eu(1, 1))) void k5_down(char* __restrict__ ws,
                                                                                          uint16_t* __restrict__ out) {
  __shared__ __attribute__((aligned(16))) char lds[48 * 1024];
  const int t = threadIdx.x, lane = t & 63, w = __builtin_amdgcn_readfirstlane(t >> 6);
  PDECL; STAMP(0);
  const int xc = blockIdx.x & 7, bi = blockIdx.x >> 3;
  const int mb = xc * 4 + (bi & 3), nb = bi >> 2;
  const char* A = ws + OFF_HFF + ((size_t)mb * 2 * 112 + 28 * w) * 1024 + lane * 16;
  const char* B = ws + OFF_WD + ((size_t)nb * 2 * 112 + 28 * w) * 1024 + lane * 16;
  const int wm = w & 1, wn = w >> 1;
  const int hi = lane >> 5, l31 = lane & 31;
  const int mt = mb * 2 + wm, nt = nb * 2 + wn;
  constexpr int D = 12;
  bf16x8 a[D][2], bw[D][2];
  auto ld = [&](int k) {
    a[k % D][0] = *(const bf16x8*)(A + k * 1024);
    bw[k % D][0] = *(const bf16x8*)(B + k * 1024);
    a[k % D][1] = *(const bf16x8*)(A + (112 + k) * 1024);
    bw[k % D][1] = *(const bf16x8*)(B + (112 + k) * 1024);
  };
#pragma unroll
  for (int k = 0; k < D; ++k) ld(k);
  u32x4 xv[2];
  f32x4 gv[4];
  f32x16 acc[4];
#pragma unroll
  for (int i = 0; i < 4; ++i)
#pragma unroll
    for (int r = 0; r < 16; ++r) acc[i][r] = 0.f;
  __builtin_amdgcn_sched_barrier(0);
#pragma unroll
  for (int k = 0; k < 28; ++k) {
    acc[0] = MFMA32(bw[k % D][0], a[k % D][0], acc[0]);
    acc[1] = MFMA32(bw[k % D][0], a[k % D][1], acc[1]);
    acc[2] = MFMA32(bw[k % D][1], a[k % D][0], acc[2]);
    acc[3] = MFMA32(bw[k % D][1], a[k % D][1], acc[3]);
    if (k + D < 28) ld(k + D);
    if (k + D == 28) {
      const char* xs = ws + OFF_X1 + ((size_t)(mt * 28 + nt * 2) * 64 + lane) * 16;
      xv[0] = *(const u32x4*)xs;
      xv[1] = *(const u32x4*)(xs + 1024);
      const float* gp = (const float*)(ws + OFF_GAB) + 448 + nt * 32 + hi * 8;
      gv[0] = *(const f32x4*)gp; gv[1] = *(const f32x4*)(gp + 4);
      gv[2] = *(const f32x4*)(gp + 16); gv[3] = *(const f32x4*)(gp + 20);
    }
    __builtin_amdgcn_sched_barrier(0);
  }
  STAMP(1);
  const f32x16 r = ksplit_reduce(acc, lds, w, lane);
  STAMP(2);
#pragma unroll
  for (int s = 0; s < 2; ++s) {
    u32x4 o;
#pragma unroll
    for (int j = 0; j < 4; ++j) {
      const float ga0 = gv[2 * s + (j >> 1)][(2 * j) & 3], ga1 = gv[2 * s + (j >> 1)][(2 * j + 1) & 3];
      o[j] = pack2(bflo(xv[s][j]) + ga0 * r[8 * s + 2 * j], bfhi(xv[s][j]) + ga1 * r[8 * s + 2 * j + 1]);
    }
    st16w<5>(out + (size_t)(mt * 32 + l31) * 448 + nt * 32 + s * 16 + hi * 8, o);
  }
  PFLUSH(5);
}

constexpr int LDS_K1 = 94 * 1024;
constexpr int LDS_K2 = 56 * 1024;
constexpr int LDS_K4 = 116 * 1024;

extern "C" void kernel_launch(void* const* d_in, const int* in_sizes, int n_in, void* d_out, int out_size, void* d_ws,
                              size_t ws_size, hipStream_t stream) {
  const uint16_t* x = (const uint16_t*)d_in[0];
  const uint16_t* wqkv = (const uint16_t*)d_in[1];
  const uint16_t* wo = (const uint16_t*)d_in[2];
  const uint16_t* wg = (const uint16_t*)d_in[4];
  const uint16_t* wu = (const uint16_t*)d_in[5];
  const uint16_t* wd = (const uint16_t*)d_in[6];
  const uint16_t* cv = (const uint16_t*)d_in[7];
  const uint16_t* wmod = (const uint16_t*)d_in[8];
  char* ws = (char*)d_ws;
  (void)hipFuncSetAttribute((const void*)k1_qkv, hipFuncAttributeMaxDynamicSharedMemorySize, LDS_K1);
  (void)hipFuncSetAttribute((const void*)k2_attn, hipFuncAttributeMaxDynamicSharedMemorySize, LDS_K2);
  (void)hipFuncSetAttribute((const void*)k4_gu, hipFuncAttributeMaxDynamicSharedMemorySize, LDS_K4);
  k0_prep<<<178, 256, 0, stream>>>(x, wqkv, cv, wmod, ws);
  k1_qkv<<<256, 256, LDS_K1, stream>>>(ws);
  k2_attn<<<494, 512, LDS_K2, stream>>>(wo, wg, wu, ws);
  k3_wo<<<256, 256, 0, stream>>>(x, ws);
  k4_gu<<<256, 256, LDS_K4, stream>>>(wd, cv, wmod, ws);
  k5_down<<<224, 256, 0, stream>>>(ws, (uint16_t*)d_out);
}
```

```cpp
#include <hip/hip_runtime.h>
#include <stdint.h>

typedef __attribute__((ext_vector_type(8))) short bf16x8;
typedef __attribute__((ext_vector_type(16))) float f32x16;
typedef __attribute__((ext_vector_type(4))) float f32x4;
typedef __attribute__((ext_vector_type(2))) float f32x2;
typedef __attribute__((ext_vector_type(2))) __bf16 bf16x2_t;
typedef __attribute__((ext_vector_type(4))) unsigned u32x4;
typedef __attribute__((ext_vector_type(2))) unsigned u32x2;

#define STMASK 151
#define ST16(p, v) (*(u32x4*)(p) = (v))
#define ST8(p, v) (*(u32x2*)(p) = (v))
template <int BIT>
__device__ __forceinline__ void st16w(void* p, u32x4 v) {
  if constexpr ((STMASK >> BIT) & 1)
    asm volatile("global_store_dwordx4 %0, %1, off sc1\n s_nop 1" ::"v"(p), "v"(v) : "memory");
  else
    *(u32x4*)p = v;
}

#define DEVI __device__ __forceinline__

DEVI unsigned pack2(float a, float b) {
  f32x2 v = {a, b};
  bf16x2_t r = __builtin_convertvector(v, bf16x2_t);
  return __builtin_bit_cast(unsigned, r);
}
DEVI float bflo(unsigned w) { return __builtin_bit_cast(float, w << 16); }
DEVI float bfhi(unsigned w) { return __builtin_bit_cast(float, w & 0xffff0000u); }
DEVI float rbf(float a) { return bflo(pack2(a, a)); }
DEVI int swap23(int d) { return (d & ~12) | ((d & 4) << 1) | ((d & 8) >> 1); }

constexpr size_t OFF_XN = 0;
constexpr size_t OFF_WQKV = OFF_XN + 64 * 28 * 1024;
constexpr size_t OFF_WO = OFF_WQKV + 48 * 28 * 1024;
constexpr size_t OFF_WGU = OFF_WO + 14 * 28 * 1024;
constexpr size_t OFF_WD = OFF_WGU + 112 * 29 * 1024;
constexpr size_t OFF_Q = OFF_WD + 14 * 112 * 1024;
constexpr size_t OFF_K = OFF_Q + 16 * 128 * 1024;
constexpr size_t OFF_V = OFF_K + 16 * 128 * 1024;
constexpr size_t OFF_O = OFF_V + 16 * 128 * 1024;
constexpr size_t OFF_X1 = OFF_O + 64 * 28 * 1024;
constexpr size_t OFF_HFF = OFF_X1 + 64 * 28 * 1024;
constexpr size_t OFF_STATS = OFF_HFF + 64 * 112 * 1024;
constexpr size_t OFF_MODA = OFF_STATS + 64 * 4096;
constexpr size_t OFF_MODB = OFF_MODA + 4096;
constexpr size_t OFF_GAB = OFF_MODB + 4096;
constexpr size_t OFF_PART = OFF_GAB + 4096;
constexpr size_t WS_END = OFF_PART + 28 * 3584 * 8;

#define PDECL
#define STAMP(i)
#define PFLUSH(kid)
#define PDECL2
#define BST(i)
#define PFLUSH2(kid)
#define GLDS16(src, dst) __builtin_amdgcn_global_load_lds((const unsigned*)(src), (unsigned*)(dst), 16, 0, 0)
#define GLDS4(src, dst) __builtin_amdgcn_global_load_lds((const unsigned*)(src), (unsigned*)(dst), 4, 0, 0)

template <int N>
DEVI void wait_bar() {
  asm volatile("s_waitcnt vmcnt(%0) lgkmcnt(0)\n\ts_barrier" ::"n"(N) : "memory");
}
template <int N>
DEVI void wait_bar4(u32x4& a, u32x4& b, u32x4& c, u32x4& d) {
  asm volatile("s_waitcnt vmcnt(%4) lgkmcnt(0)\n\ts_barrier" : "+v"(a), "+v"(b), "+v"(c), "+v"(d) : "n"(N) : "memory");
}
DEVI void gload16(u32x4& r, const char* p) {
  asm volatile("global_load_dwordx4 %0, %1, off" : "=v"(r) : "v"(p) : "memory");
}

typedef __attribute__((address_space(3))) char lds_char;
DEVI unsigned lds_addr(const char* p) { return (unsigned)(size_t)(lds_char*)p; }
template <int OFF>
DEVI void lread16(u32x4& r, unsigned a) {
  asm volatile("ds_read_b128 %0, %1 offset:%2" : "=v"(r) : "v"(a), "n"(OFF) : "memory");
}
template <int OFF>
DEVI void lwrite16(const u32x4& r, unsigned a) {
  asm volatile("ds_write_b128 %1, %0 offset:%2" ::"v"(r), "v"(a), "n"(OFF) : "memory");
}
template <int N>
DEVI void lwait() { asm volatile("s_waitcnt lgkmcnt(%0)" ::"n"(N) : "memory"); }
template <int N>
DEVI void vwait() { asm volatile("s_waitcnt vmcnt(%0)" ::"n"(N) : "memory"); }
DEVI void rdyv(u32x4& a) { asm volatile("" : "+v"(a)); }
DEVI void rdya(u32x4& a) { asm volatile("" : "+a"(a)); }
DEVI void gload16a(u32x4& r, const char* p) {
  asm volatile("global_load_dwordx4 %0, %1, off" : "=a"(r) : "v"(p) : "memory");
}
template <int OFF>
DEVI void gload16o(u32x4& r, const char* p) {
  asm volatile("global_load_dwordx4 %0, %1, off offset:%2" : "=v"(r) : "v"(p), "n"(OFF) : "memory");
}
template <int OFF>
DEVI void gload16ao(u32x4& r, const char* p) {
  asm volatile("global_load_dwordx4 %0, %1, off offset:%2" : "=a"(r) : "v"(p), "n"(OFF) : "memory");
}
DEVI unsigned scale2(unsigned w, float s0, float s1) {
  unsigned r; float t0, t1;
  asm("v_lshlrev_b32 %1, 16, %3\n\tv_and_b32 %2, 0xffff0000, %3\n\tv_mul_f32 %1, %1, %4\n\tv_mul_f32 %2, %2, %5\n\t"
      "v_cvt_pk_bf16_f32 %0, %1, %2" : "=v"(r), "=&v"(t0), "=&v"(t1) : "v"(w), "v"(s0), "v"(s1));
  return r;
}
DEVI float dot2(unsigned a, unsigned b, float c) {
  return __builtin_amdgcn_fdot2_f32_bf16(__builtin_bit_cast(bf16x2_t, a), __builtin_bit_cast(bf16x2_t, b), c, false);
}
DEVI float fmul1(float a, float b) { float r; asm("v_mul_f32 %0, %1, %2" : "=v"(r) : "v"(a), "v"(b)); return r; }
DEVI float fadd1(float a, float b) { float r; asm("v_add_f32 %0, %1, %2" : "=v"(r) : "v"(a), "v"(b)); return r; }
DEVI float fsub1(float a, float b) { float r; asm("v_sub_f32 %0, %1, %2" : "=v"(r) : "v"(a), "v"(b)); return r; }
DEVI void fmac1(float& c, float a, float b) { asm("v_fmac_f32 %0, %1, %2" : "+v"(c) : "v"(a), "v"(b)); }
template <int NV, int NL>
DEVI void vlwait() { asm volatile("s_waitcnt vmcnt(%0) lgkmcnt(%1)" ::"n"(NV), "n"(NL) : "memory"); }
template <int N>
DEVI void vbar() { asm volatile("s_waitcnt vmcnt(%0)\n\ts_barrier" ::"n"(N) : "memory"); }
DEVI float xhalf_sum(float v) {
  const unsigned u = __builtin_bit_cast(unsigned, v);
  const f32x2 r = __builtin_bit_cast(f32x2, __builtin_amdgcn_permlane32_swap(u, u, false, false));
  return fadd1(r[0], r[1]);
}
#define F4(x) __builtin_bit_cast(f32x4, x)
#define SB() __builtin_amdgcn_sched_barrier(0)
#define BF8(x) __builtin_bit_cast(bf16x8, x)
#define IC(v) std::integral_constant<int, (v)>{}

template <int I, int N, class F>
DEVI void static_for(F&& f) {
  if constexpr (I < N) {
    f(std::integral_constant<int, I>{});
    static_for<I + 1, N>(f);
  }
}

#define MFMA32(a, b, c) __builtin_amdgcn_mfma_f32_32x32x16_bf16(a, b, c, 0, 0, 0)
#define MFMA16(a, b, c) __builtin_amdgcn_mfma_f32_16x16x32_bf16(a, b, c, 0, 0, 0)

DEVI void mod_unit(const uint16_t* __restrict__ cvec, const uint16_t* __restrict__ wmod, char* __restrict__ ws, int col0,
                   int t, float* sm) {
  const int lane = t & 63, w = t >> 6, q4 = t & 3, rs = t >> 2;
  const uint16_t* mp = wmod + (size_t)rs * 2688 + col0 + q4 * 8;
  u32x4 mv[7];
  float cs[7];
#pragma unroll
  for (int i = 0; i < 7; ++i) mv[i] = *(const u32x4*)(mp + (size_t)i * 64 * 2688);
#pragma unroll
  for (int i = 0; i < 7; ++i) {
    float cv = bflo((unsigned)cvec[rs + 64 * i]);
    cs[i] = rbf(cv / (1.f + __expf(-cv)));
  }
  float acc[8];
#pragma unroll
  for (int j = 0; j < 8; ++j) acc[j] = 0.f;
#pragma unroll
  for (int i = 0; i < 7; ++i)
#pragma unroll
    for (int j = 0; j < 4; ++j) {
      acc[2 * j] += cs[i] * bflo(mv[i][j]);
      acc[2 * j + 1] += cs[i] * bfhi(mv[i][j]);
    }
#pragma unroll
  for (int j = 0; j < 8; ++j) {
    acc[j] += __shfl_xor(acc[j], 4);
    acc[j] += __shfl_xor(acc[j], 8);
    acc[j] += __shfl_xor(acc[j], 16);
    acc[j] += __shfl_xor(acc[j], 32);
  }
  if (lane < 4) {
#pragma unroll
    for (int j = 0; j < 8; ++j) sm[w * 32 + lane * 8 + j] = acc[j];
  }
  __syncthreads();
  if (t < 32) {
    const float m = rbf(sm[t] + sm[32 + t] + sm[64 + t] + sm[96 + t]);
    const int part = col0 / 448, k = col0 % 448 + t;
    if (part == 2 || part == 5) {
      ((float*)(ws + OFF_GAB))[(part == 5 ? 448 : 0) + k] = m;
    } else {
      float* dst = (float*)(ws + (part >= 3 ? OFF_MODB : OFF_MODA)) + (k >> 3) * 16 + (k & 7);
      if (part == 1 || part == 4) dst[0] = rbf(1.f + m);
      else if (part == 3) dst[8] = m;
      else ((uint16_t*)(ws + OFF_MODA + (k >> 3) * 64 + 32))[k & 7] = (uint16_t)pack2(m, m);
    }
  }
}

DEVI void retile_unit(const uint16_t* __restrict__ wsrc, char* __restrict__ ws, int role, int kr, int sub, int t) {
  const int k0 = kr * 32;
  const int koct = t / 56, coct = t % 56;
  const int ldw = role == 0 ? 1344 : 448, c0 = role == 0 ? sub * 448 : 0;
  if (t < 224) {
    u32x4 wv[8];
    const uint16_t* p = wsrc + (size_t)(k0 + koct * 8) * ldw + c0 + coct * 8;
#pragma unroll
    for (int i = 0; i < 8; ++i) wv[i] = *(const u32x4*)(p + (size_t)i * ldw);
    const int ksl = koct >> 1, half = koct & 1;
#pragma unroll
    for (int c = 0; c < 8; ++c) {
      u32x4 o;
#pragma unroll
      for (int i = 0; i < 4; ++i)
        o[i] = __builtin_amdgcn_perm(wv[2 * i + 1][c >> 1], wv[2 * i][c >> 1], (c & 1) ? 0x07060302u : 0x05040100u);
      const int n = coct * 8 + c;
      char* dst;
      if (role == 0) {
        const int head = n / 28, d = n % 28;
        const int idx = (sub < 2) ? swap23(d) : d;
        dst = ws + OFF_WQKV + ((size_t)(head * 84 + ((2 * kr + ksl) & 3) * 21 + (kr >> 1) * 3 + sub) * 64 + half * 32 + idx) * 16;
      } else if (role == 2) {
        dst = ws + OFF_WO + ((size_t)((n >> 5) * 28 + 2 * kr + ksl) * 64 + half * 32 + swap23(n & 31)) * 16;
      } else {
        dst = ws + OFF_WD + ((size_t)((n >> 5) * 112 + 2 * kr + ksl) * 64 + half * 32 + swap23(n & 31)) * 16;
      }
      ST16(dst, o);
    }
  } else if (role == 0) {
    const int z = t - 224;
#pragma unroll
    for (int i = 0; i < 8; ++i) {
      const int pc = z * 8 + i, head = pc >> 4, d = 28 + ((pc >> 2) & 3), ko = pc & 3;
      const int idx = (sub < 2) ? swap23(d) : d;
      char* dst = ws + OFF_WQKV + ((size_t)(head * 84 + ((2 * kr + (ko >> 1)) & 3) * 21 + (kr >> 1) * 3 + sub) * 64 + (ko & 1) * 32 + idx) * 16;
      *(u32x4*)dst = u32x4{0, 0, 0, 0};
    }
  }
}

#define PARG
#define PPASS
DEVI void fold_unit(const uint16_t* __restrict__ wsrc, char* __restrict__ ws, int mat, int kr, int cq, int t, char* red PARG) {
  const int ko = t >= 224, c2 = t - 224 * ko, oct = kr * 2 + ko;
  if (t < 448) {
    unsigned wv[8];
    const uint16_t* p = wsrc + (size_t)(oct * 8) * 1792 + cq * 448 + c2 * 2;
#pragma unroll
    for (int i = 0; i < 8; ++i) wv[i] = *(const unsigned*)(p + (size_t)i * 1792);
    const f32x4* tp = (const f32x4*)(ws + OFF_MODB + oct * 64);
    const f32x4 t0 = tp[0], t1 = tp[1], t2 = tp[2], t3 = tp[3];
    const float sc[8] = {t0[0], t0[1], t0[2], t0[3], t1[0], t1[1], t1[2], t1[3]};
    const float sh[8] = {t2[0], t2[1], t2[2], t2[3], t3[0], t3[1], t3[2], t3[3]};
    f32x2 pr[2];
    asm volatile("s_waitcnt vmcnt(0)\n\ts_setprio 3" ::: "memory");
    STAMP(3);
    u32x4 oc[2];
#pragma unroll
    for (int c = 0; c < 2; ++c) {
      u32x4 o;
      float bs = 0.f, cs = 0.f;
#pragma unroll
      for (int i = 0; i < 4; ++i) {
        const float a = c ? bfhi(wv[2 * i]) : bflo(wv[2 * i]);
        const float b = c ? bfhi(wv[2 * i + 1]) : bflo(wv[2 * i + 1]);
        bs += a * sh[2 * i] + b * sh[2 * i + 1];
        o[i] = pack2(a * sc[2 * i], b * sc[2 * i + 1]);
        cs += bflo(o[i]) + bfhi(o[i]);
      }
      pr[c] = f32x2{cs, bs};
      oc[c] = o;
    }
    {
      const int b1 = (c2 >> 1) & 1;
      u32x4 r;
#pragma unroll
      for (int i = 0; i < 4; ++i)
        r[i] = (unsigned)__builtin_amdgcn_mov_dpp((int)(b1 ? oc[0][i] : oc[1][i]), 0x4e, 0xf, 0xf, true);
      const int fA = cq * 448 + (c2 & ~2) * 2 + b1, fB = fA + 4;
      char* base = ws + OFF_WGU + ((size_t)((fA >> 4) * 29 + kr) * 64 + ko * 32 + mat * 16) * 16;
      st16w<2>(base + swap23(fA & 15) * 16, b1 ? r : oc[0]);
      st16w<2>(base + swap23(fB & 15) * 16, b1 ? oc[1] : r);
    }
    *(f32x4*)(red + ko * 3584 + c2 * 16) = f32x4{pr[0][0], pr[0][1], pr[1][0], pr[1][1]};
  }
  STAMP(4);
  asm volatile("s_waitcnt lgkmcnt(0)\n\ts_barrier" ::: "memory");
  STAMP(6);
  if (t < 224)
    st16w<7>(ws + OFF_PART + ((size_t)kr * 3584 + mat * 1792 + cq * 448) * 8 + t * 16,
             __builtin_bit_cast(u32x4, *(const f32x4*)(red + t * 16) + *(const f32x4*)(red + 3584 + t * 16)));
}

__global__ __launch_bounds__(256) void k0_prep(const uint16_t* __restrict__ x, const uint16_t* __restrict__ wqkv,
                                               const uint16_t* __restrict__ cvec, const uint16_t* __restrict__ wmod,
                                               char* __restrict__ ws, const char* wo, const char* wg, const char* wu,
                                               const char* wd, const char* out) {
  __shared__ __attribute__((aligned(16))) float sm[512];
  const int b = blockIdx.x, t = threadIdx.x, lane = t & 63, w = t >> 6;
  PDECL; STAMP(0);
  if (b < 72) {
    const int u = (b & 7) * 9 + (b >> 3);
    if (u < 70) mod_unit(cvec, wmod, ws, u * 32, t, sm);
  } else if (b < 136) {
    const int mt = b - 72, tok = lane & 31, half = lane >> 5;
    const uint16_t* src = x + (size_t)(mt * 32 + tok) * 448 + (7 * w) * 16 + half * 8;
    u32x4 v[7];
#pragma unroll
    for (int i = 0; i < 7; ++i) v[i] = *(const u32x4*)(src + i * 16);
    float s = 0.f;
#pragma unroll
    for (int i = 0; i < 7; ++i)
#pragma unroll
      for (int j = 0; j < 4; ++j) s += bflo(v[i][j]) + bfhi(v[i][j]);
    const float lm = s * (1.f / 56.f);
    float m2 = 0.f;
#pragma unroll
    for (int i = 0; i < 7; ++i)
#pragma unroll
      for (int j = 0; j < 4; ++j) {
        float a = bflo(v[i][j]) - lm, c = bfhi(v[i][j]) - lm;
        m2 += a * a + c * c;
      }
    f32x2* part = (f32x2*)sm;
    part[(w * 2 + half) * 32 + tok] = f32x2{lm, m2};
    __syncthreads();
    STAMP(1);
    float mu = 0.f, M2 = 0.f;
    f32x2 p[8];
#pragma unroll
    for (int i = 0; i < 8; ++i) { p[i] = part[i * 32 + tok]; mu += p[i][0]; M2 += p[i][1]; }
    mu *= 0.125f;
#pragma unroll
    for (int i = 0; i < 8; ++i) { float d = p[i][0] - mu; M2 += 56.f * d * d; }
    const float rstd = rsqrtf(M2 * (1.f / 448.f) + 1e-5f);
    char* dst = ws + OFF_XN + ((size_t)(mt * 28 + 7 * w) * 64 + lane) * 16;
#pragma unroll
    for (int i = 0; i < 7; ++i) {
      u32x4 o;
#pragma unroll
      for (int j = 0; j < 4; ++j) o[j] = pack2((bflo(v[i][j]) - mu) * rstd, (bfhi(v[i][j]) - mu) * rstd);
      st16w<0>(dst + i * 1024, o);
    }
  } else if (b < 178) {
    const int rb = b - 136;
    retile_unit(wqkv, ws, 0, rb / 3, rb % 3, t);
  } else {
    const char* p = nullptr;
    if (b < 186) {
      if (t < 206) p = ws + (size_t)t * 131072;
    } else if (lane < 25 || (w == 3 && lane < 28)) {
      p = (w == 0 ? wg : w == 1 ? wu : w == 2 ? wd : out) + (size_t)lane * 65536;
    } else if (w == 0 && lane >= 32 && lane < 39) {
      p = wo + (size_t)(lane - 32) * 65536;
    }
    if (p) {
      unsigned d;
      asm volatile("global_load_dword %0, %1, off\n\ts_waitcnt vmcnt(0)" : "=v"(d) : "v"(p) : "memory");
    }
  }
  PFLUSH(0);
}

constexpr int k1_posW(int c) { return c == 0 ? 3 : c == 1 ? 10 : c == 2 ? 13 : 20 + 7 * (c - 3); }
constexpr int k1_posX(int k) { return (k < 4 ? 4 : k < 8 ? 10 : k < 24 ? 13 + 3 * (k / 4 - 2) : 22) + k; }
constexpr int k1_iss(int k) { return 18 + (2 * k < 32 ? 2 * k : 32); }
__global__ __launch_bounds__(256) __attribute__((amdgpu_waves_per_eu(1, 1))) void k1_qkv(char* __restrict__ ws) {
  extern __shared__ __attribute__((aligned(16))) char lds[];
  const int t = threadIdx.x, lane = t & 63, w = __builtin_amdgcn_readfirstlane(t >> 6);
  PDECL; STAMP(0);
  const int xc = blockIdx.x & 7, bi = blockIdx.x >> 3;
  const int mb = (xc & 3) * 4 + (bi & 3), head = (xc >> 2) * 8 + (bi >> 2);
  const int mt = mb * 4 + w;
  const int hi = lane >> 5, l31 = lane & 31;
  const char* A0 = ws + OFF_XN + (size_t)mt * 28 * 1024 + lane * 16 + 4096;
  const char* B0 = ws + OFF_WQKV + ((size_t)head * 84 + w * 21) * 1024 + lane * 16 + 4096;
  const char* A[4] = {A0, A0 + 8192, A0 + 16384, A0 + 24576};
  const char* B[3] = {B0, B0 + 8192, B0 + 16384};
  char* modv = lds + 87 * 1024 + w * 1024;
  float* P = (float*)(lds + 91 * 1024);
  GLDS16(ws + OFF_MODA + (4 * (lane >> 3) + w) * 128 + (lane & 7) * 16, modv);
  u32x4 xr[29], sw[7][3];
  auto ldW = [&](auto cc, auto ic) {
    constexpr int c = decltype(cc)::value, i = decltype(ic)::value;
    gload16o<((c * 3 + i) & 7) * 1024 - 4096>(sw[c][i], B[(c * 3 + i) >> 3]);
  };
  auto ldX = [&](auto kc) {
    constexpr int k = decltype(kc)::value;
    gload16ao<(k & 7) * 1024 - 4096>(xr[k], A[k >> 3]);
  };
  auto ldN = [&](auto nc) {
    constexpr int n = decltype(nc)::value, g = n / 7, r = n % 7;
    if constexpr (n >= 35) { if constexpr (n < 39) ldX(IC(24 + n - 35)); }
    else if constexpr (r < 3) ldW(IC(g + 2), IC(r));
    else ldX(IC(4 * (g + 1) + r - 3));
  };
  static_for<0, 3>([&](auto i) { ldW(IC(0), i); });
  static_for<0, 4>(ldX);
  static_for<0, 3>([&](auto i) { ldW(IC(1), i); });
  f32x16 acc[3], accb[3];
#pragma unroll
  for (int i = 0; i < 3; ++i)
#pragma unroll
    for (int r = 0; r < 16; ++r) { acc[i][r] = 0.f; accb[i][r] = 0.f; }
  const unsigned wl0 = lds_addr(lds) + lane * 16, wl1 = wl0 + 49152;
  const unsigned sa0 = wl0 + w * 3072, sa1 = sa0 + 49152;
  const unsigned ml = lds_addr(modv) + hi * 64;
  u32x4 wb[3][3], mv[7][3], so[3];
  auto rdw = [&](auto kc) {
    constexpr int k = decltype(kc)::value;
    constexpr int o = (k & 15) * 3072;
    const unsigned a = (k >= 16) ? wl1 : wl0;
    lread16<o>(wb[k % 3][0], a); lread16<o + 1024>(wb[k % 3][1], a); lread16<o + 2048>(wb[k % 3][2], a);
  };
  auto rdm = [&](auto cc) {
    constexpr int c = decltype(cc)::value;
    lread16<c * 128>(mv[c][0], ml); lread16<c * 128 + 16>(mv[c][1], ml);
    lread16<c * 128 + 32>(mv[c][2], ml);
  };
  auto rdym = [&](auto cc) {
    constexpr int c = decltype(cc)::value;
    rdyv(mv[c][0]); rdyv(mv[c][1]); rdyv(mv[c][2]);
  };
  auto word = [&](auto cc, auto qc) {
    constexpr int c = decltype(cc)::value, q = decltype(qc)::value, i = q >> 2, j = q & 3;
    if constexpr (j == 0) accb[i] = MFMA32(BF8(mv[c][2]), BF8(sw[c][i]), accb[i]);
    const f32x4 sc = F4(mv[c][j >> 1]);
    so[i][j] = scale2(sw[c][i][j], sc[(2 * j) & 3], sc[(2 * j + 1) & 3]);
    if constexpr (j == 3) lwrite16<(c & 3) * 12288 + i * 1024>(so[i], c < 4 ? sa0 : sa1);
  };
  PDECL2;
  BST(0);
  vwait<7>();
  BST(8);
  rdyv(sw[0][0]); rdyv(sw[0][1]); rdyv(sw[0][2]);
  rdm(IC(0)); rdm(IC(1)); rdm(IC(2));
  ldN(IC(0));
  lwait<0>();
  static_for<0, 3>(rdym);
  static_for<0, 12>([&](auto q) {
    word(IC(0), q);
    if constexpr (q % 2 == 0) ldN(IC(1 + q / 2));
    SB();
  });
  wait_bar<7>();
  STAMP(1);
  rdya(xr[0]);
  rdyv(sw[1][0]); rdyv(sw[1][1]); rdyv(sw[1][2]);
  rdw(IC(0)); rdw(IC(1));
  lwait<3>();
  rdyv(wb[0][0]); rdyv(wb[0][1]); rdyv(wb[0][2]);
  xr[28] = u32x4{0x3f803f80u, 0, 0, 0};
  SB();
  static_for<0, 29>([&](auto kc) {
    constexpr int k = decltype(kc)::value;
    constexpr int cn = (k < 2) ? 1 : (k + 6) / 4, r = (k + 6) % 4;
    constexpr bool PR = k < 22;
    auto gap = [&](auto gc) {
      constexpr int g = decltype(gc)::value;
      if constexpr (k < 2) { word(IC(1), IC(6 * k + 2 * g)); word(IC(1), IC(6 * k + 2 * g + 1)); }
      else if constexpr (PR) word(IC(cn), IC(3 * r + g));
    };
    acc[0] = MFMA32(BF8(wb[k % 3][0]), BF8(xr[k]), acc[0]);
    constexpr bool BS = (k + 2) % 4 == 0;
    if constexpr (BS && k < 22) {
      constexpr int j = (k + 2) / 4;
      vwait<k1_iss(k) - 1 - k1_posW(j + 1)>();
      rdyv(sw[j + 1][0]); rdyv(sw[j + 1][1]); rdyv(sw[j + 1][2]);
    }
    if constexpr (k >= 2 && r == 2 && cn + 1 < 7) rdm(IC(cn + 1));
    if constexpr (!BS && k + 2 < 29) rdw(IC(k + 2));
    gap(IC(0));
    SB();
    acc[1] = MFMA32(BF8(wb[k % 3][1]), BF8(xr[k]), acc[1]);
    if constexpr (BS) {
      BST((k + 2) / 4);
      wait_bar<63>();
      BST(8 + (k + 2) / 4);
      rdw(IC(k + 2));
    }
    ldN(IC(7 + 2 * k));
    gap(IC(1));
    if constexpr (k == 23) {
      if (w < 3) {
        float bs = P[(w * 4 + 0) * 64 + lane] + P[(w * 4 + 1) * 64 + lane] + P[(w * 4 + 2) * 64 + lane] + P[(w * 4 + 3) * 64 + lane];
        const float bh = rbf(bs);
        *(u32x4*)(lds + (84 + w) * 1024 + lane * 16) = u32x4{hi ? 0u : pack2(bh, bs - bh), 0, 0, 0};
      }
    }
    SB();
    acc[2] = MFMA32(BF8(xr[k]), BF8(wb[k % 3][2]), acc[2]);
    ldN(IC(8 + 2 * k));
    gap(IC(2));
    if constexpr (k == 21) {
#pragma unroll
      for (int i = 0; i < 3; ++i) P[(i * 4 + w) * 64 + lane] = accb[i][0];
    }
    if constexpr (k + 1 < 29) {
      constexpr int nw = k == 0 ? 1 : k == 1 ? 2 : k == 21 ? 4 : (PR && r != 0) ? 1 : 0;
      vlwait<(k + 1 < 28) ? k1_iss(k + 1) - 1 - k1_posX(k + 1) : 63, (k + 2 < 29) ? 3 + nw : 0>();
      if constexpr (k + 1 < 28) rdya(xr[k + 1]);
      rdyv(wb[(k + 1) % 3][0]); rdyv(wb[(k + 1) % 3][1]); rdyv(wb[(k + 1) % 3][2]);
      if constexpr (k >= 2 && r == 2 && cn + 1 < 7) rdym(IC(cn + 1));
    }
    SB();
  });
  STAMP(2);
  const float qs = 0.18898223650461362f * 1.4426950408889634f;
#pragma unroll
  for (int p = 0; p < 2; ++p) {
    char* base = ws + (p ? OFF_K : OFF_Q) + ((size_t)(head * 128 + mt * 2 + (l31 >> 4)) * 64 + (l31 & 15)) * 16;
    const float sc = p ? 1.f : qs;
#pragma unroll
    for (int s = 0; s < 2; ++s) {
      u32x4 o;
#pragma unroll
      for (int j = 0; j < 4; ++j) o[j] = pack2(acc[p][8 * s + 2 * j] * sc, acc[p][8 * s + 2 * j + 1] * sc);
      st16w<1>(base + (2 * s + hi) * 256, o);
    }
  }
  {
    char* base = ws + OFF_V + ((size_t)((head * 64 + mt) * 2 + (l31 >> 4)) * 64 + (l31 & 15)) * 16;
#pragma unroll
    for (int s = 0; s < 2; ++s) {
      u32x4 o = {pack2(acc[2][4 * s], acc[2][4 * s + 1]), pack2(acc[2][4 * s + 2], acc[2][4 * s + 3]),
                 pack2(acc[2][8 + 4 * s], acc[2][8 + 4 * s + 1]), pack2(acc[2][8 + 4 * s + 2], acc[2][8 + 4 * s + 3])};
      if (l31 == 28) o = u32x4{0x3f803f80u, 0x3f803f80u, 0x3f803f80u, 0x3f803f80u};
      st16w<1>(base + (2 * s + hi) * 256, o);
    }
  }
  PFLUSH(1);
  PFLUSH2(6);
}

template <bool ODD>
DEVI void attn_pv(const char* vp, const u32x2 (&p)[17], f32x4& o0, f32x4& o1) {
  bf16x8 vf[9][2];
#pragma unroll
  for (int v = 0; v < 3; ++v) { vf[v][0] = *(const bf16x8*)(vp + v * 2048); vf[v][1] = *(const bf16x8*)(vp + v * 2048 + 1024); }
#pragma unroll
  for (int v = 0; v < 9; ++v) {
    if (v + 3 < 9) {
      vf[v + 3][0] = *(const bf16x8*)(vp + (v + 3) * 2048);
      vf[v + 3][1] = *(const bf16x8*)(vp + (v + 3) * 2048 + 1024);
    }
    const int i0 = 2 * v - (ODD ? 1 : 0), i1 = i0 + 1;
    const u32x2 z = {0, 0};
    const u32x2 pa = i0 >= 0 ? p[i0 >= 0 ? i0 : 0] : z, pc = i1 < 17 ? p[i1 < 17 ? i1 : 0] : z;
    const u32x4 pw = {pa[0], pa[1], pc[0], pc[1]};
    const bf16x8 pf = __builtin_bit_cast(bf16x8, pw);
    o0 = MFMA16(vf[v][0], pf, o0);
    o1 = MFMA16(vf[v][1], pf, o1);
    SB();
  }
}

DEVI void attn_wave(const char* ldsK, const char* ldsV, const char* ldsQ, int w, int lane, int nneg, char* odst, int kbase) {
  const int g = lane >> 4, qq = lane & 15;
  const bf16x8 q = *(const bf16x8*)(ldsQ + w * 1024 + lane * 16);
  const float NEG = -1e30f;
  f32x4 s[17];
  const char* kp = ldsK + w * 1024 + lane * 16;
  bf16x8 kf[17];
#pragma unroll
  for (int tt = 0; tt < 4; ++tt) kf[tt] = *(const bf16x8*)(kp + tt * 1024);
#pragma unroll
  for (int tt = 0; tt < 17; ++tt) {
    if (tt + 4 < 17) kf[tt + 4] = *(const bf16x8*)(kp + (tt + 4) * 1024);
    s[tt] = MFMA16(kf[tt], q, (f32x4{0.f, 0.f, 0.f, 0.f}));
    SB();
  }
#pragma unroll
  for (int r = 0; r < 4; ++r) {
    s[0][r] = (4 * g + r > qq) ? s[0][r] : NEG;
    s[16][r] = (4 * g + r <= qq) ? s[16][r] : NEG;
  }
  if (nneg > 0) {
#pragma unroll
    for (int tt = 0; tt < 16; ++tt)
      if (w + tt < nneg) s[tt] = f32x4{NEG, NEG, NEG, NEG};
  }
  float m = NEG;
#pragma unroll
  for (int tt = 0; tt < 17; ++tt) m = fmaxf(fmaxf(m, fmaxf(s[tt][0], s[tt][1])), fmaxf(s[tt][2], s[tt][3]));
  m = fmaxf(m, __shfl_xor(m, 16));
  m = fmaxf(m, __shfl_xor(m, 32));
  u32x2 pb[17];
#pragma unroll
  for (int tt = 0; tt < 17; ++tt) {
    const float e0 = __builtin_amdgcn_exp2f(s[tt][0] - m), e1 = __builtin_amdgcn_exp2f(s[tt][1] - m);
    const float e2 = __builtin_amdgcn_exp2f(s[tt][2] - m), e3 = __builtin_amdgcn_exp2f(s[tt][3] - m);
    pb[tt] = u32x2{pack2(e0, e1), pack2(e2, e3)};
  }
  wait_bar<0>();
  f32x4 o0 = {0.f, 0.f, 0.f, 0.f}, o1 = {0.f, 0.f, 0.f, 0.f};
  const char* vp = ldsV + (w >> 1) * 2048 + lane * 16;
  if (w & 1) attn_pv<true>(vp, pb, o0, o1);
  else attn_pv<false>(vp, pb, o0, o1);
  const float sum = __shfl(o1[0], 48 + qq);
  const float inv = 1.f / sum;
  {
    const int k = kbase + 4 * g;
    u32x2 o = {pack2(o0[0] * inv, o0[1] * inv), pack2(o0[2] * inv, o0[3] * inv)};
    ST8(odst + (size_t)(k >> 4) * 1024 + ((k >> 3) & 1) * 512 + (k & 4) * 2, o);
  }
  if (g < 3) {
    const int k = kbase + 16 + 4 * g;
    u32x2 o = {pack2(o1[0] * inv, o1[1] * inv), pack2(o1[2] * inv, o1[3] * inv)};
    ST8(odst + (size_t)(k >> 4) * 1024 + ((k >> 3) & 1) * 512 + (k & 4) * 2, o);
  }
}

__global__ __launch_bounds__(512) __attribute__((amdgpu_waves_per_eu(4, 4))) void k2_attn(
    const uint16_t* __restrict__ wo, const uint16_t* __restrict__ wg, const uint16_t* __restrict__ wu,
    char* __restrict__ ws) {
  extern __shared__ __attribute__((aligned(16))) char lds[];
  const int t = threadIdx.x, lane = t & 63, w = __builtin_amdgcn_readfirstlane(t >> 6);
  PDECL; STAMP(0);
  if (__builtin_expect(blockIdx.x >= 256, 0)) {
    const int u = blockIdx.x - 256;
    if (u < 224) fold_unit((u & 4) ? wu : wg, ws, (u >> 2) & 1, u >> 3, u & 3, t, lds PPASS);
    else if (w < 4) retile_unit(wo, ws, 2, u - 224, 0, t);
    PFLUSH(2);
    return;
  }
  const int xc = blockIdx.x & 7, bi = blockIdx.x >> 3;
  const int head = xc * 2 + (bi & 1), qb = bi >> 1;
  char* ldsK = lds;
  char* ldsV = lds + 24 * 1024;
  char* ldsQ = lds + 48 * 1024;
  const int sub0 = qb * 8 - 16;
  GLDS16(ws + OFF_Q + (size_t)(head * 128 + qb * 8 + w) * 1024 + lane * 16, ldsQ + w * 1024);
#pragma unroll
  for (int i = 0; i < 3; ++i) {
    const int p = w * 3 + i;
    const int sidx = max(sub0 + p, 0);
    GLDS16(ws + OFF_K + (size_t)(head * 128 + sidx) * 1024 + lane * 16, ldsK + p * 1024);
  }
#pragma unroll
  for (int i = 0; i < 3; ++i) {
    const int p = w * 3 + i;
    const int sidx = max(sub0 + p, 0);
    GLDS16(ws + OFF_V + (size_t)(head * 128 + sidx) * 1024 + lane * 16, ldsV + p * 1024);
  }
  wait_bar<3>();
  STAMP(1);
  const int tok = qb * 128 + w * 16 + (lane & 15);
  char* odst = ws + OFF_O + (size_t)(tok >> 5) * 28 * 1024 + (tok & 31) * 16;
  const int kbase = head * 28;
  const int nneg = -sub0;
  attn_wave(ldsK, ldsV, ldsQ, w, lane, nneg, odst, kbase);
  PFLUSH(2);
}

DEVI f32x16 ksplit_reduce(f32x16 (&acc)[4], char* lds, int w, int lane) {
#pragma unroll
  for (int tl = 0; tl < 4; ++tl) {
    if (tl != w) {
      const int slot = (w > tl) ? w - 1 : w;
      char* p = lds + (tl * 3 + slot) * 4096 + lane * 16;
#pragma unroll
      for (int q = 0; q < 4; ++q)
        *(f32x4*)(p + q * 1024) = f32x4{acc[tl][4 * q], acc[tl][4 * q + 1], acc[tl][4 * q + 2], acc[tl][4 * q + 3]};
    }
  }
  f32x16 r = (w == 0) ? acc[0] : (w == 1) ? acc[1] : (w == 2) ? acc[2] : acc[3];
  __syncthreads();
#pragma unroll
  for (int s = 0; s < 3; ++s) {
    const char* p = lds + (w * 3 + s) * 4096 + lane * 16;
#pragma unroll
    for (int q = 0; q < 4; ++q) {
      const f32x4 v = *(const f32x4*)(p + q * 1024);
#pragma unroll
      for (int j = 0; j < 4; ++j) r[4 * q + j] += v[j];
    }
  }
  return r;
}

__global__ __launch_bounds__(256) __attribute__((amdgpu_waves_per_eu(1, 1))) void k3_wo(const uint16_t* __restrict__ x,
                                                                                        char* __restrict__ ws) {
  __shared__ __attribute__((aligned(16))) char lds[48 * 1024];
  const int t = threadIdx.x, lane = t & 63, w = __builtin_amdgcn_readfirstlane(t >> 6);
  PDECL; STAMP(0);
  if (__builtin_expect(blockIdx.x >= 224, 0)) {
    if (t < 224) {
      const int z = t >= 112, col = (blockIdx.x - 224) * 112 + t - 112 * z;
      f32x2 v = {0.f, 0.f};
#pragma unroll
      for (int i = 0; i < 28; ++i) v += *(const f32x2*)(ws + OFF_PART + ((size_t)i * 3584 + col) * 8);
      const float ch = rbf(v[0]), cl = v[0] - ch, bh = rbf(v[1]), bl = v[1] - bh;
      u32x4 o = {pack2(ch, ch), pack2(cl, cl), pack2(bh, bh), pack2(bl, bl)};
      if (z) o = u32x4{0, 0, 0, 0};
      const int mat = col >= 1792, f = col - 1792 * mat;
      ST16(ws + OFF_WGU + ((size_t)((f >> 4) * 29 + 28) * 64 + z * 32 + mat * 16 + swap23(f & 15)) * 16, o);
    }
    PFLUSH(3);
    return;
  }
  const int xc = blockIdx.x & 7, bi = blockIdx.x >> 3;
  const int mb = xc * 4 + (bi & 3), nb = bi >> 2;
  const char* A = ws + OFF_O + ((size_t)mb * 2 * 28 + 7 * w) * 1024 + lane * 16;
  const char* B = ws + OFF_WO + ((size_t)nb * 2 * 28 + 7 * w) * 1024 + lane * 16;
  const int wm = w & 1, wn = w >> 1;
  const int hi = lane >> 5, l31 = lane & 31;
  const int mt = mb * 2 + wm, nt = nb * 2 + wn;
  bf16x8 a[7][2], bw[7][2];
#pragma unroll
  for (int k = 0; k < 7; ++k) {
    a[k][0] = *(const bf16x8*)(A + k * 1024);
    bw[k][0] = *(const bf16x8*)(B + k * 1024);
    a[k][1] = *(const bf16x8*)(A + (28 + k) * 1024);
    bw[k][1] = *(const bf16x8*)(B + (28 + k) * 1024);
  }
  u32x4 xv[2];
  f32x4 gv[4];
  {
    const uint16_t* xs = x + (size_t)(mt * 32 + l31) * 448 + nt * 32 + hi * 8;
    xv[0] = *(const u32x4*)xs;
    xv[1] = *(const u32x4*)(xs + 16);
    const float* gp = (const float*)(ws + OFF_GAB) + nt * 32 + hi * 8;
    gv[0] = *(const f32x4*)gp; gv[1] = *(const f32x4*)(gp + 4);
    gv[2] = *(const f32x4*)(gp + 16); gv[3] = *(const f32x4*)(gp + 20);
  }
  __builtin_amdgcn_sched_barrier(0);
  f32x16 acc[4];
#pragma unroll
  for (int i = 0; i < 4; ++i)
#pragma unroll
    for (int r = 0; r < 16; ++r) acc[i][r] = 0.f;
#pragma unroll
  for (int k = 0; k < 7; ++k) {
    acc[0] = MFMA32(bw[k][0], a[k][0], acc[0]);
    acc[1] = MFMA32(bw[k][0], a[k][1], acc[1]);
    acc[2] = MFMA32(bw[k][1], a[k][0], acc[2]);
    acc[3] = MFMA32(bw[k][1], a[k][1], acc[3]);
  }
  STAMP(1);
  const f32x16 r = ksplit_reduce(acc, lds, w, lane);
  STAMP(2);
  float v[16];
#pragma unroll
  for (int s = 0; s < 2; ++s) {
    u32x4 o;
#pragma unroll
    for (int j = 0; j < 4; ++j) {
      const float ga0 = gv[2 * s + (j >> 1)][(2 * j) & 3], ga1 = gv[2 * s + (j >> 1)][(2 * j + 1) & 3];
      o[j] = pack2(bflo(xv[s][j]) + ga0 * r[8 * s + 2 * j], bfhi(xv[s][j]) + ga1 * r[8 * s + 2 * j + 1]);
      v[8 * s + 2 * j] = bflo(o[j]);
      v[8 * s + 2 * j + 1] = bfhi(o[j]);
    }
    st16w<3>(ws + OFF_X1 + ((size_t)(mt * 28 + nt * 2 + s) * 64 + lane) * 16, o);
  }
  float sm = 0.f;
#pragma unroll
  for (int j = 0; j < 16; ++j) sm += v[j];
  sm += __shfl_xor(sm, 32);
  const float mean = sm * (1.f / 32.f);
  float m2 = 0.f;
#pragma unroll
  for (int j = 0; j < 16; ++j) { const float d = v[j] - mean; m2 += d * d; }
  m2 += __shfl_xor(m2, 32);
  if (hi == 0) *(f32x2*)(ws + OFF_STATS + ((size_t)(mt * 8 + nb) * 32 + l31) * 16 + wn * 8) = f32x2{mean, m2};
  PFLUSH(3);
}

constexpr int k4_posG(int g) { return g < 2 ? g : g < 4 ? g + 2 : g <= 9 ? 8 + 3 * (g - 4) : g == 10 ? 30 : 4 + 3 * g; }
constexpr int k4_posX(int s) { return s == 0 ? 3 : s == 1 ? 7 : s <= 6 ? 10 + 3 * (s - 2) : s == 7 ? 27 : s == 8 ? 34 : s < 27 ? 12 + 3 * s : 92; }
constexpr int k4_iss(int k) { return k == 0 ? 23 : k == 1 ? 30 : k <= 20 ? 31 + 3 * k : 93; }
__global__ __launch_bounds__(256) __attribute__((amdgpu_waves_per_eu(1, 1))) void k4_gu(
    const uint16_t* __restrict__ wd, const uint16_t* __restrict__ cvec, const uint16_t* __restrict__ wmod,
    char* __restrict__ ws) {
  extern __shared__ __attribute__((aligned(16))) char lds[];
  const int t = threadIdx.x, lane = t & 63, w = __builtin_amdgcn_readfirstlane(t >> 6);
  PDECL; STAMP(0);
  if (__builtin_expect(blockIdx.x >= 224, 0)) {
    const int j = blockIdx.x - 224;
    u32x4 wv[2][8];
#pragma unroll
    for (int h = 0; h < 2; ++h) {
      const int id = min(t + 256 * h, 391);
      const uint16_t* p = wd + (size_t)((j * 7 + id / 56) * 8) * 448 + (id % 56) * 8;
#pragma unroll
      for (int i = 0; i < 8; ++i) wv[h][i] = *(const u32x4*)(p + (size_t)i * 448);
    }
    const int v = (j & 7) * 2 + (j >> 3);
    if (j < 16 && v < 14) mod_unit(cvec, wmod, ws, 5 * 448 + v * 32, t, (float*)lds);
#pragma unroll
    for (int h = 0; h < 2; ++h) {
      const int id = t + 256 * h, ko = j * 7 + id / 56, coct = id % 56;
      if (id < 392) {
#pragma unroll
        for (int c = 0; c < 8; ++c) {
          u32x4 o;
#pragma unroll
          for (int i = 0; i < 4; ++i)
            o[i] = __builtin_amdgcn_perm(wv[h][2 * i + 1][c >> 1], wv[h][2 * i][c >> 1], (c & 1) ? 0x07060302u : 0x05040100u);
          const int n = coct * 8 + c;
          ST16(ws + OFF_WD + ((size_t)((n >> 5) * 112 + (ko >> 1)) * 64 + (ko & 1) * 32 + swap23(n & 31)) * 16, o);
        }
      }
    }
    PFLUSH(4);
    return;
  }
  const int xc = blockIdx.x & 7, bi = blockIdx.x >> 3;
  const int mb = (xc & 1) * 4 + (bi & 3), fb = (xc >> 1) * 7 + (bi >> 2);
  const int mt = mb * 8 + 2 * w;
  const char* A = ws + OFF_X1 + (size_t)mt * 28 * 1024 + lane * 16;
  const char* B = ws + OFF_WGU + (size_t)(fb * 4 + w) * 29 * 1024 + lane * 16;
  const int hi = lane >> 5, l31 = lane & 31;
  u32x4 xr[29][2], st[2][4];
  auto ldG = [&](auto gc) {
    constexpr int g = decltype(gc)::value;
    GLDS16(B + g * 1024, lds + (g * 4 + w) * 1024);
  };
  auto ldXa = [&](auto sc) { constexpr int s = decltype(sc)::value; gload16a(xr[s][0], A + s * 1024); };
  auto ldXb = [&](auto sc) { constexpr int s = decltype(sc)::value; gload16a(xr[s][1], A + (28 + s) * 1024); };
  ldG(IC(0)); ldG(IC(1)); ldXa(IC(0)); ldXb(IC(0)); ldG(IC(2)); ldG(IC(3)); ldXa(IC(1)); ldXb(IC(1));
  const char* S = ws + OFF_STATS + (size_t)mt * 4096 + (hi * 4 * 32 + l31) * 16;
  static_for<2, 7>([&](auto sc) { ldG(IC(sc + 2)); ldXa(sc); ldXb(sc); });
  f32x16 acc[2][4];
#pragma unroll
  for (int i = 0; i < 8; ++i)
#pragma unroll
    for (int r = 0; r < 16; ++r) acc[i >> 2][i & 3][r] = 0.f;
  const unsigned wa0 = lds_addr(lds) + lane * 16, wa1 = wa0 + 65536;
  u32x4 wb[3][4];
  float rstd[2], var[2], ta[4], tb[4], d[8], mu;
  f32x4 p[4];
  auto stat = [&](auto tc, auto sc) {
    constexpr int tt = decltype(tc)::value, q = decltype(sc)::value;
    if constexpr (q == 0) {
#pragma unroll
      for (int i = 0; i < 4; ++i) p[i] = F4(st[tt][i]);
      if (hi) p[3] = f32x4{0.f, 0.f, 0.f, 0.f};
    } else if constexpr (q == 1 || q == 2) {
#pragma unroll
      for (int i = 2 * q - 2; i < 2 * q; ++i) { ta[i] = fadd1(p[i][0], p[i][2]); tb[i] = fadd1(p[i][1], p[i][3]); }
    } else if constexpr (q == 3) {
      ta[0] = fadd1(ta[0], ta[1]); ta[2] = fadd1(ta[2], ta[3]); tb[0] = fadd1(tb[0], tb[1]); tb[2] = fadd1(tb[2], tb[3]);
    } else if constexpr (q == 4) {
      ta[0] = fadd1(ta[0], ta[2]); tb[0] = fadd1(tb[0], tb[2]);
    } else if constexpr (q == 5) {
      ta[0] = xhalf_sum(ta[0]);
    } else if constexpr (q == 6) {
      mu = fmul1(ta[0], 1.f / 14.f);
      if (hi) { p[3][0] = mu; p[3][2] = mu; }
    } else if constexpr (q == 7 || q == 8) {
#pragma unroll
      for (int i = 2 * q - 14; i < 2 * q - 12; ++i) { d[2 * i] = fsub1(p[i][0], mu); d[2 * i + 1] = fsub1(p[i][2], mu); }
    } else if constexpr (q == 9) {
#pragma unroll
      for (int i = 0; i < 4; ++i) ta[i] = fmul1(d[i], d[i]);
    } else if constexpr (q == 10) {
#pragma unroll
      for (int i = 0; i < 4; ++i) fmac1(ta[i], d[4 + i], d[4 + i]);
    } else if constexpr (q == 11) {
      ta[0] = fadd1(ta[0], ta[1]); ta[2] = fadd1(ta[2], ta[3]);
      ta[1] = rbf(mu);
    } else if constexpr (q == 12) {
      fmac1(tb[0], fadd1(ta[0], ta[2]), 32.f);
    } else if constexpr (q == 13) {
      tb[0] = xhalf_sum(tb[0]);
    } else if constexpr (q == 14) {
      var[tt] = fmaf(tb[0], 1.f / 448.f, 1e-5f);
      rstd[tt] = __builtin_amdgcn_rsqf(var[tt]);
    } else if constexpr (q == 15) {
      ta[0] = fmul1(var[tt], rstd[tt]);
      ta[3] = fsub1(ta[1], mu);
    } else if constexpr (q == 16) {
      ta[2] = rbf(ta[0]);
    } else if constexpr (q == 17) {
      const unsigned pm = pack2(-ta[1], ta[3]), ps = pack2(ta[2], fsub1(ta[0], ta[2]));
      xr[28][tt] = u32x4{pm, pm, ps, ps};
    }
  };
  auto rdw = [&](auto kc) {
    constexpr int k = decltype(kc)::value;
    constexpr int o = (k & 15) * 4096;
    const unsigned a = (k >= 16) ? wa1 : wa0;
    lread16<o>(wb[k % 3][0], a); lread16<o + 1024>(wb[k % 3][1], a);
    lread16<o + 2048>(wb[k % 3][2], a); lread16<o + 3072>(wb[k % 3][3], a);
  };
  PDECL2;
  BST(0);
  vbar<21>();
  BST(8);
  STAMP(1);
  rdw(IC(0)); rdw(IC(1));
  vlwait<19, 4>();
  rdya(xr[0][0]); rdya(xr[0][1]);
  rdyv(wb[0][0]); rdyv(wb[0][1]); rdyv(wb[0][2]); rdyv(wb[0][3]);
  SB();
  static_for<0, 29>([&](auto kc) {
    constexpr int k = decltype(kc)::value;
    static_for<0, 8>([&](auto gc) {
      constexpr int g = decltype(gc)::value;
      acc[g & 1][g >> 1] = MFMA32(BF8(wb[k % 3][g >> 1]), BF8(xr[k][g & 1]), acc[g & 1][g >> 1]);
      if constexpr (g == 0) {
        if constexpr (k % 2 == 0 && k + 2 < 29) {
          if constexpr (k % 4 == 0) BST(1 + k / 4);
          vbar<k4_iss(k) - 1 - k4_posG(k + 3 < 29 ? k + 3 : 28)>();
          if constexpr (k % 4 == 0) BST(9 + k / 4);
        }
        if constexpr (k + 2 < 29) rdw(IC(k + 2));
      }
      if constexpr (g == 1 && k + 9 < 29) ldG(IC(k + 9));
      if constexpr (g == 3 && k + 7 < 28) ldXa(IC(k + 7));
      if constexpr (g == 5 && k + 7 < 28) ldXb(IC(k + 7));
      if constexpr (k < 2 && (g == 2 || g == 4 || g >= 6)) gload16(st[k & 1][g == 7 ? 3 : g / 2 - 1], S + k * 4096 + (g == 7 ? 3 : g / 2 - 1) * 512);
      if constexpr (k >= 9 && k < 14) stat(IC((((k - 9) * 8 + g) / 20) & 1), IC(((k - 9) * 8 + g) % 20));
      if constexpr (g == 7 && k + 1 < 29) {
        vlwait<(k + 1 < 28) ? k4_iss(k + 1) - 1 - k4_posX(k + 1) : 63, (k + 2 < 29) ? 4 : 0>();
        if constexpr (k + 1 < 28) { rdya(xr[k + 1][0]); rdya(xr[k + 1][1]); }
        rdyv(wb[(k + 1) % 3][0]); rdyv(wb[(k + 1) % 3][1]); rdyv(wb[(k + 1) % 3][2]); rdyv(wb[(k + 1) % 3][3]);
        if constexpr (k == 8) {
#pragma unroll
          for (int i = 0; i < 8; ++i) rdyv(st[i >> 2][i & 3]);
        }
      }
      SB();
    });
  });
  STAMP(2);
  const float c0 = -2.f * 0.7978845608028654f * 1.4426950408889634f, c1 = c0 * 0.044715f;
  char* hdst = ws + OFF_HFF + ((size_t)(mt * 112 + fb * 4) * 64 + lane) * 16;
#pragma unroll
  for (int tt = 0; tt < 2; ++tt)
#pragma unroll
    for (int n = 0; n < 4; ++n) {
      const float e0 = c0 * rstd[tt], e1 = c1 * rstd[tt] * rstd[tt] * rstd[tt];
      float hh[8];
#pragma unroll
      for (int j = 0; j < 8; ++j) {
        const float g = acc[tt][n][j], u = acc[tt][n][8 + j];
        const float e = __builtin_amdgcn_exp2f(g * fmaf(g * g, e1, e0));
        hh[j] = g * u * __builtin_amdgcn_rcpf(fmaf(e, var[tt], var[tt]));
      }
      u32x4 o = {pack2(hh[0], hh[1]), pack2(hh[2], hh[3]), pack2(hh[4], hh[5]), pack2(hh[6], hh[7])};
      st16w<4>(hdst + (tt * 112 + n) * 1024, o);
    }
  PFLUSH(4);
  PFLUSH2(8);
}

__global__ __launch_bounds__(256) __attribute__((amdgpu_waves_per_eu(1, 1))) void k5_down(char* __restrict__ ws,
                                                                                          uint16_t* __restrict__ out) {
  __shared__ __attribute__((aligned(16))) char lds[48 * 1024];
  const int t = threadIdx.x, lane = t & 63, w = __builtin_amdgcn_readfirstlane(t >> 6);
  PDECL; STAMP(0);
  const int xc = blockIdx.x & 7, bi = blockIdx.x >> 3;
  const int mb = xc * 4 + (bi & 3), nb = bi >> 2;
  const char* A = ws + OFF_HFF + ((size_t)mb * 2 * 112 + 28 * w) * 1024 + lane * 16;
  const char* B = ws + OFF_WD + ((size_t)nb * 2 * 112 + 28 * w) * 1024 + lane * 16;
  const int wm = w & 1, wn = w >> 1;
  const int hi = lane >> 5, l31 = lane & 31;
  const int mt = mb * 2 + wm, nt = nb * 2 + wn;
  constexpr int D = 12;
  bf16x8 a[D][2], bw[D][2];
  auto ld = [&](int k) {
    a[k % D][0] = *(const bf16x8*)(A + k * 1024);
    bw[k % D][0] = *(const bf16x8*)(B + k * 1024);
    a[k % D][1] = *(const bf16x8*)(A + (112 + k) * 1024);
    bw[k % D][1] = *(const bf16x8*)(B + (112 + k) * 1024);
  };
#pragma unroll
  for (int k = 0; k < D; ++k) ld(k);
  u32x4 xv[2];
  f32x4 gv[4];
  f32x16 acc[4];
#pragma unroll
  for (int i = 0; i < 4; ++i)
#pragma unroll
    for (int r = 0; r < 16; ++r) acc[i][r] = 0.f;
  __builtin_amdgcn_sched_barrier(0);
#pragma unroll
  for (int k = 0; k < 28; ++k) {
    acc[0] = MFMA32(bw[k % D][0], a[k % D][0], acc[0]);
    acc[1] = MFMA32(bw[k % D][0], a[k % D][1], acc[1]);
    acc[2] = MFMA32(bw[k % D][1], a[k % D][0], acc[2]);
    acc[3] = MFMA32(bw[k % D][1], a[k % D][1], acc[3]);
    if (k + D < 28) ld(k + D);
    if (k + D == 28) {
      const char* xs = ws + OFF_X1 + ((size_t)(mt * 28 + nt * 2) * 64 + lane) * 16;
      xv[0] = *(const u32x4*)xs;
      xv[1] = *(const u32x4*)(xs + 1024);
      const float* gp = (const float*)(ws + OFF_GAB) + 448 + nt * 32 + hi * 8;
      gv[0] = *(const f32x4*)gp; gv[1] = *(const f32x4*)(gp + 4);
      gv[2] = *(const f32x4*)(gp + 16); gv[3] = *(const f32x4*)(gp + 20);
    }
    __builtin_amdgcn_sched_barrier(0);
  }
  STAMP(1);
  const f32x16 r = ksplit_reduce(acc, lds, w, lane);
  STAMP(2);
#pragma unroll
  for (int s = 0; s < 2; ++s) {
    u32x4 o;
#pragma unroll
    for (int j = 0; j < 4; ++j) {
      const float ga0 = gv[2 * s + (j >> 1)][(2 * j) & 3], ga1 = gv[2 * s + (j >> 1)][(2 * j + 1) & 3];
      o[j] = pack2(bflo(xv[s][j]) + ga0 * r[8 * s + 2 * j], bfhi(xv[s][j]) + ga1 * r[8 * s + 2 * j + 1]);
    }
    st16w<5>(out + (size_t)(mt * 32 + l31) * 448 + nt * 32 + s * 16 + hi * 8, o);
  }
  PFLUSH(5);
}

constexpr int LDS_K1 = 94 * 1024;
constexpr int LDS_K2 = 56 * 1024;
constexpr int LDS_K4 = 116 * 1024;

extern "C" void kernel_launch(void* const* d_in, const int* in_sizes, int n_in, void* d_out, int out_size, void* d_ws,
                              size_t ws_size, hipStream_t stream) {
  const uint16_t* x = (const uint16_t*)d_in[0];
  const uint16_t* wqkv = (const uint16_t*)d_in[1];
  const uint16_t* wo = (const uint16_t*)d_in[2];
  const uint16_t* wg = (const uint16_t*)d_in[4];
  const uint16_t* wu = (const uint16_t*)d_in[5];
  const uint16_t* wd = (const uint16_t*)d_in[6];
  const uint16_t* cv = (const uint16_t*)d_in[7];
  const uint16_t* wmod = (const uint16_t*)d_in[8];
  char* ws = (char*)d_ws;
  (void)hipFuncSetAttribute((const void*)k1_qkv, hipFuncAttributeMaxDynamicSharedMemorySize, LDS_K1);
  (void)hipFuncSetAttribute((const void*)k2_attn, hipFuncAttributeMaxDynamicSharedMemorySize, LDS_K2);
  (void)hipFuncSetAttribute((const void*)k4_gu, hipFuncAttributeMaxDynamicSharedMemorySize, LDS_K4);
  k0_prep<<<194, 256, 0, stream>>>(x, wqkv, cv, wmod, ws, (const char*)wo, (const char*)wg, (const char*)wu, (const char*)wd,
                                   (const char*)d_out);
  k1_qkv<<<256, 256, LDS_K1, stream>>>(ws);
  k2_attn<<<494, 512, LDS_K2, stream>>>(wo, wg, wu, ws);
  k3_wo<<<256, 256, 0, stream>>>(x, ws);
  k4_gu<<<256, 256, LDS_K4, stream>>>(wd, cv, wmod, ws);
  k5_down<<<224, 256, 0, stream>>>(ws, (uint16_t*)d_out);
}
```

```cpp
#include <hip/hip_runtime.h>
#include <stdint.h>

typedef __attribute__((ext_vector_type(8))) short bf16x8;
typedef __attribute__((ext_vector_type(16))) float f32x16;
typedef __attribute__((ext_vector_type(4))) float f32x4;
typedef __attribute__((ext_vector_type(2))) float f32x2;
typedef __attribute__((ext_vector_type(2))) __bf16 bf16x2_t;
typedef __attribute__((ext_vector_type(4))) unsigned u32x4;
typedef __attribute__((ext_vector_type(2))) unsigned u32x2;

#define STMASK 151
#define ST16(p, v) (*(u32x4*)(p) = (v))
#define ST8(p, v) (*(u32x2*)(p) = (v))
template <int BIT>
__device__ __forceinline__ void st16w(void* p, u32x4 v) {
  if constexpr ((STMASK >> BIT) & 1)
    asm volatile("global_store_dwordx4 %0, %1, off sc1\n s_nop 1" ::"v"(p), "v"(v) : "memory");
  else
    *(u32x4*)p = v;
}

#define DEVI __device__ __forceinline__

DEVI unsigned pack2(float a, float b) {
  f32x2 v = {a, b};
  bf16x2_t r = __builtin_convertvector(v, bf16x2_t);
  return __builtin_bit_cast(unsigned, r);
}
DEVI float bflo(unsigned w) { return __builtin_bit_cast(float, w << 16); }
DEVI float bfhi(unsigned w) { return __builtin_bit_cast(float, w & 0xffff0000u); }
DEVI float rbf(float a) { return bflo(pack2(a, a)); }
#define CODE_PF(LIM)                                                                                   \
  unsigned pf_d;                                                                                       \
  {                                                                                                    \
    const char* pf_pc;                                                                                 \
    asm volatile("s_getpc_b64 %0" : "=s"(pf_pc));                                                      \
    const int pf_o = (int)(threadIdx.x & 255) * 64;                                                    \
    asm volatile("global_load_dword %0, %1, off ; PFC" : "=v"(pf_d) : "v"(pf_pc + (pf_o < (LIM) ? pf_o : (LIM))) : "memory"); \
  }
#define CODE_PF_END asm volatile("" ::"v"(pf_d))
DEVI int swap23(int d) { return (d & ~12) | ((d & 4) << 1) | ((d & 8) >> 1); }

constexpr size_t OFF_XN = 0;
constexpr size_t OFF_WQKV = OFF_XN + 64 * 28 * 1024;
constexpr size_t OFF_WO = OFF_WQKV + 48 * 28 * 1024;
constexpr size_t OFF_WGU = OFF_WO + 14 * 28 * 1024;
constexpr size_t OFF_WD = OFF_WGU + 112 * 29 * 1024;
constexpr size_t OFF_Q = OFF_WD + 14 * 112 * 1024;
constexpr size_t OFF_K = OFF_Q + 16 * 128 * 1024;
constexpr size_t OFF_V = OFF_K + 16 * 128 * 1024;
constexpr size_t OFF_O = OFF_V + 16 * 128 * 1024;
constexpr size_t OFF_X1 = OFF_O + 64 * 28 * 1024;
constexpr size_t OFF_HFF = OFF_X1 + 64 * 28 * 1024;
constexpr size_t OFF_STATS = OFF_HFF + 64 * 112 * 1024;
constexpr size_t OFF_MODA = OFF_STATS + 64 * 4096;
constexpr size_t OFF_MODB = OFF_MODA + 4096;
constexpr size_t OFF_GAB = OFF_MODB + 4096;
constexpr size_t OFF_PART = OFF_GAB + 4096;
constexpr size_t WS_END = OFF_PART + 28 * 3584 * 8;

#define PDECL
#define STAMP(i)
#define PFLUSH(kid)
#define PDECL2
#define BST(i)
#define PFLUSH2(kid)
#define GLDS16(src, dst) __builtin_amdgcn_global_load_lds((const unsigned*)(src), (unsigned*)(dst), 16, 0, 0)
#define GLDS4(src, dst) __builtin_amdgcn_global_load_lds((const unsigned*)(src), (unsigned*)(dst), 4, 0, 0)

template <int N>
DEVI void wait_bar() {
  asm volatile("s_waitcnt vmcnt(%0) lgkmcnt(0)\n\ts_barrier" ::"n"(N) : "memory");
}
template <int N>
DEVI void wait_bar4(u32x4& a, u32x4& b, u32x4& c, u32x4& d) {
  asm volatile("s_waitcnt vmcnt(%4) lgkmcnt(0)\n\ts_barrier" : "+v"(a), "+v"(b), "+v"(c), "+v"(d) : "n"(N) : "memory");
}
DEVI void gload16(u32x4& r, const char* p) {
  asm volatile("global_load_dwordx4 %0, %1, off" : "=v"(r) : "v"(p) : "memory");
}

typedef __attribute__((address_space(3))) char lds_char;
DEVI unsigned lds_addr(const char* p) { return (unsigned)(size_t)(lds_char*)p; }
template <int OFF>
DEVI void lread16(u32x4& r, unsigned a) {
  asm volatile("ds_read_b128 %0, %1 offset:%2" : "=v"(r) : "v"(a), "n"(OFF) : "memory");
}
template <int OFF>
DEVI void lwrite16(const u32x4& r, unsigned a) {
  asm volatile("ds_write_b128 %1, %0 offset:%2" ::"v"(r), "v"(a), "n"(OFF) : "memory");
}
template <int N>
DEVI void lwait() { asm volatile("s_waitcnt lgkmcnt(%0)" ::"n"(N) : "memory"); }
template <int N>
DEVI void vwait() { asm volatile("s_waitcnt vmcnt(%0)" ::"n"(N) : "memory"); }
DEVI void rdyv(u32x4& a) { asm volatile("" : "+v"(a)); }
DEVI void rdya(u32x4& a) { asm volatile("" : "+a"(a)); }
DEVI void gload16a(u32x4& r, const char* p) {
  asm volatile("global_load_dwordx4 %0, %1, off" : "=a"(r) : "v"(p) : "memory");
}
template <int OFF>
DEVI void gload16o(u32x4& r, const char* p) {
  asm volatile("global_load_dwordx4 %0, %1, off offset:%2" : "=v"(r) : "v"(p), "n"(OFF) : "memory");
}
template <int OFF>
DEVI void gload16ao(u32x4& r, const char* p) {
  asm volatile("global_load_dwordx4 %0, %1, off offset:%2" : "=a"(r) : "v"(p), "n"(OFF) : "memory");
}
DEVI unsigned scale2(unsigned w, float s0, float s1) {
  unsigned r; float t0, t1;
  asm("v_lshlrev_b32 %1, 16, %3\n\tv_and_b32 %2, 0xffff0000, %3\n\tv_mul_f32 %1, %1, %4\n\tv_mul_f32 %2, %2, %5\n\t"
      "v_cvt_pk_bf16_f32 %0, %1, %2" : "=v"(r), "=&v"(t0), "=&v"(t1) : "v"(w), "v"(s0), "v"(s1));
  return r;
}
DEVI float dot2(unsigned a, unsigned b, float c) {
  return __builtin_amdgcn_fdot2_f32_bf16(__builtin_bit_cast(bf16x2_t, a), __builtin_bit_cast(bf16x2_t, b), c, false);
}
DEVI float fmul1(float a, float b) { float r; asm("v_mul_f32 %0, %1, %2" : "=v"(r) : "v"(a), "v"(b)); return r; }
DEVI float fadd1(float a, float b) { float r; asm("v_add_f32 %0, %1, %2" : "=v"(r) : "v"(a), "v"(b)); return r; }
DEVI float fsub1(float a, float b) { float r; asm("v_sub_f32 %0, %1, %2" : "=v"(r) : "v"(a), "v"(b)); return r; }
DEVI void fmac1(float& c, float a, float b) { asm("v_fmac_f32 %0, %1, %2" : "+v"(c) : "v"(a), "v"(b)); }
template <int NV, int NL>
DEVI void vlwait() { asm volatile("s_waitcnt vmcnt(%0) lgkmcnt(%1)" ::"n"(NV), "n"(NL) : "memory"); }
template <int N>
DEVI void vbar() { asm volatile("s_waitcnt vmcnt(%0)\n\ts_barrier" ::"n"(N) : "memory"); }
DEVI float xhalf_sum(float v) {
  const unsigned u = __builtin_bit_cast(unsigned, v);
  const f32x2 r = __builtin_bit_cast(f32x2, __builtin_amdgcn_permlane32_swap(u, u, false, false));
  return fadd1(r[0], r[1]);
}
#define F4(x) __builtin_bit_cast(f32x4, x)
#define SB() __builtin_amdgcn_sched_barrier(0)
#define BF8(x) __builtin_bit_cast(bf16x8, x)
#define IC(v) std::integral_constant<int, (v)>{}

template <int I, int N, class F>
DEVI void static_for(F&& f) {
  if constexpr (I < N) {
    f(std::integral_constant<int, I>{});
    static_for<I + 1, N>(f);
  }
}

#define MFMA32(a, b, c) __builtin_amdgcn_mfma_f32_32x32x16_bf16(a, b, c, 0, 0, 0)
#define MFMA16(a, b, c) __builtin_amdgcn_mfma_f32_16x16x32_bf16(a, b, c, 0, 0, 0)

DEVI void mod_unit(const uint16_t* __restrict__ cvec, const uint16_t* __restrict__ wmod, char* __restrict__ ws, int col0,
                   int t, float* sm) {
  const int lane = t & 63, w = t >> 6, q4 = t & 3, rs = t >> 2;
  const uint16_t* mp = wmod + (size_t)rs * 2688 + col0 + q4 * 8;
  u32x4 mv[7];
  float cs[7];
#pragma unroll
  for (int i = 0; i < 7; ++i) mv[i] = *(const u32x4*)(mp + (size_t)i * 64 * 2688);
#pragma unroll
  for (int i = 0; i < 7; ++i) {
    float cv = bflo((unsigned)cvec[rs + 64 * i]);
    cs[i] = rbf(cv / (1.f + __expf(-cv)));
  }
  float acc[8];
#pragma unroll
  for (int j = 0; j < 8; ++j) acc[j] = 0.f;
#pragma unroll
  for (int i = 0; i < 7; ++i)
#pragma unroll
    for (int j = 0; j < 4; ++j) {
      acc[2 * j] += cs[i] * bflo(mv[i][j]);
      acc[2 * j + 1] += cs[i] * bfhi(mv[i][j]);
    }
#pragma unroll
  for (int j = 0; j < 8; ++j) {
    acc[j] += __shfl_xor(acc[j], 4);
    acc[j] += __shfl_xor(acc[j], 8);
    acc[j] += __shfl_xor(acc[j], 16);
    acc[j] += __shfl_xor(acc[j], 32);
  }
  if (lane < 4) {
#pragma unroll
    for (int j = 0; j < 8; ++j) sm[w * 32 + lane * 8 + j] = acc[j];
  }
  __syncthreads();
  if (t < 32) {
    const float m = rbf(sm[t] + sm[32 + t] + sm[64 + t] + sm[96 + t]);
    const int part = col0 / 448, k = col0 % 448 + t;
    if (part == 2 || part == 5) {
      ((float*)(ws + OFF_GAB))[(part == 5 ? 448 : 0) + k] = m;
    } else {
      float* dst = (float*)(ws + (part >= 3 ? OFF_MODB : OFF_MODA)) + (k >> 3) * 16 + (k & 7);
      if (part == 1 || part == 4) dst[0] = rbf(1.f + m);
      else if (part == 3) dst[8] = m;
      else ((uint16_t*)(ws + OFF_MODA + (k >> 3) * 64 + 32))[k & 7] = (uint16_t)pack2(m, m);
    }
  }
}

DEVI void retile_unit(const uint16_t* __restrict__ wsrc, char* __restrict__ ws, int role, int kr, int sub, int t) {
  const int k0 = kr * 32;
  const int koct = t / 56, coct = t % 56;
  const int ldw = role == 0 ? 1344 : 448, c0 = role == 0 ? sub * 448 : 0;
  if (t < 224) {
    u32x4 wv[8];
    const uint16_t* p = wsrc + (size_t)(k0 + koct * 8) * ldw + c0 + coct * 8;
#pragma unroll
    for (int i = 0; i < 8; ++i) wv[i] = *(const u32x4*)(p + (size_t)i * ldw);
    const int ksl = koct >> 1, half = koct & 1;
#pragma unroll
    for (int c = 0; c < 8; ++c) {
      u32x4 o;
#pragma unroll
      for (int i = 0; i < 4; ++i)
        o[i] = __builtin_amdgcn_perm(wv[2 * i + 1][c >> 1], wv[2 * i][c >> 1], (c & 1) ? 0x07060302u : 0x05040100u);
      const int n = coct * 8 + c;
      char* dst;
      if (role == 0) {
        const int head = n / 28, d = n % 28;
        const int idx = (sub < 2) ? swap23(d) : d;
        dst = ws + OFF_WQKV + ((size_t)(head * 84 + ((2 * kr + ksl) & 3) * 21 + (kr >> 1) * 3 + sub) * 64 + half * 32 + idx) * 16;
      } else if (role == 2) {
        dst = ws + OFF_WO + ((size_t)((n >> 5) * 28 + 2 * kr + ksl) * 64 + half * 32 + swap23(n & 31)) * 16;
      } else {
        dst = ws + OFF_WD + ((size_t)((n >> 5) * 112 + 2 * kr + ksl) * 64 + half * 32 + swap23(n & 31)) * 16;
      }
      ST16(dst, o);
    }
  } else if (role == 0) {
    const int z = t - 224;
#pragma unroll
    for (int i = 0; i < 8; ++i) {
      const int pc = z * 8 + i, head = pc >> 4, d = 28 + ((pc >> 2) & 3), ko = pc & 3;
      const int idx = (sub < 2) ? swap23(d) : d;
      char* dst = ws + OFF_WQKV + ((size_t)(head * 84 + ((2 * kr + (ko >> 1)) & 3) * 21 + (kr >> 1) * 3 + sub) * 64 + (ko & 1) * 32 + idx) * 16;
      *(u32x4*)dst = u32x4{0, 0, 0, 0};
    }
  }
}

#define PARG
#define PPASS
DEVI void fold_unit(const uint16_t* __restrict__ wsrc, char* __restrict__ ws, int mat, int kr, int cq, int t, char* red PARG) {
  const int ko = t >= 224, c2 = t - 224 * ko, oct = kr * 2 + ko;
  if (t < 448) {
    unsigned wv[8];
    const uint16_t* p = wsrc + (size_t)(oct * 8) * 1792 + cq * 448 + c2 * 2;
#pragma unroll
    for (int i = 0; i < 8; ++i) wv[i] = *(const unsigned*)(p + (size_t)i * 1792);
    const f32x4* tp = (const f32x4*)(ws + OFF_MODB + oct * 64);
    const f32x4 t0 = tp[0], t1 = tp[1], t2 = tp[2], t3 = tp[3];
    const float sc[8] = {t0[0], t0[1], t0[2], t0[3], t1[0], t1[1], t1[2], t1[3]};
    const float sh[8] = {t2[0], t2[1], t2[2], t2[3], t3[0], t3[1], t3[2], t3[3]};
    f32x2 pr[2];
    asm volatile("s_waitcnt vmcnt(0)\n\ts_setprio 3" ::: "memory");
    STAMP(3);
    u32x4 oc[2];
#pragma unroll
    for (int c = 0; c < 2; ++c) {
      u32x4 o;
      float bs = 0.f, cs = 0.f;
#pragma unroll
      for (int i = 0; i < 4; ++i) {
        const float a = c ? bfhi(wv[2 * i]) : bflo(wv[2 * i]);
        const float b = c ? bfhi(wv[2 * i + 1]) : bflo(wv[2 * i + 1]);
        bs += a * sh[2 * i] + b * sh[2 * i + 1];
        o[i] = pack2(a * sc[2 * i], b * sc[2 * i + 1]);
        cs += bflo(o[i]) + bfhi(o[i]);
      }
      pr[c] = f32x2{cs, bs};
      oc[c] = o;
    }
    {
      const int b1 = (c2 >> 1) & 1;
      u32x4 r;
#pragma unroll
      for (int i = 0; i < 4; ++i)
        r[i] = (unsigned)__builtin_amdgcn_mov_dpp((int)(b1 ? oc[0][i] : oc[1][i]), 0x4e, 0xf, 0xf, true);
      const int fA = cq * 448 + (c2 & ~2) * 2 + b1, fB = fA + 4;
      char* base = ws + OFF_WGU + ((size_t)((fA >> 4) * 29 + kr) * 64 + ko * 32 + mat * 16) * 16;
      st16w<2>(base + swap23(fA & 15) * 16, b1 ? r : oc[0]);
      st16w<2>(base + swap23(fB & 15) * 16, b1 ? oc[1] : r);
    }
    *(f32x4*)(red + ko * 3584 + c2 * 16) = f32x4{pr[0][0], pr[0][1], pr[1][0], pr[1][1]};
  }
  STAMP(4);
  asm volatile("s_waitcnt lgkmcnt(0)\n\ts_barrier" ::: "memory");
  STAMP(6);
  if (t < 224)
    st16w<7>(ws + OFF_PART + ((size_t)kr * 3584 + mat * 1792 + cq * 448) * 8 + t * 16,
             __builtin_bit_cast(u32x4, *(const f32x4*)(red + t * 16) + *(const f32x4*)(red + 3584 + t * 16)));
}

__global__ __launch_bounds__(256) void k0_prep(const uint16_t* __restrict__ x, const uint16_t* __restrict__ wqkv,
                                               const uint16_t* __restrict__ cvec, const uint16_t* __restrict__ wmod,
                                               char* __restrict__ ws) {
  __shared__ __attribute__((aligned(16))) float sm[512];
  const int b = blockIdx.x, t = threadIdx.x, lane = t & 63, w = t >> 6;
  PDECL; STAMP(0);
  CODE_PF(6336);
  if (b < 72) {
    const int u = (b & 7) * 9 + (b >> 3);
    if (u < 70) mod_unit(cvec, wmod, ws, u * 32, t, sm);
  } else if (b < 136) {
    const int mt = b - 72, tok = lane & 31, half = lane >> 5;
    const uint16_t* src = x + (size_t)(mt * 32 + tok) * 448 + (7 * w) * 16 + half * 8;
    u32x4 v[7];
#pragma unroll
    for (int i = 0; i < 7; ++i) v[i] = *(const u32x4*)(src + i * 16);
    float s = 0.f;
#pragma unroll
    for (int i = 0; i < 7; ++i)
#pragma unroll
      for (int j = 0; j < 4; ++j) s += bflo(v[i][j]) + bfhi(v[i][j]);
    const float lm = s * (1.f / 56.f);
    float m2 = 0.f;
#pragma unroll
    for (int i = 0; i < 7; ++i)
#pragma unroll
      for (int j = 0; j < 4; ++j) {
        float a = bflo(v[i][j]) - lm, c = bfhi(v[i][j]) - lm;
        m2 += a * a + c * c;
      }
    f32x2* part = (f32x2*)sm;
    part[(w * 2 + half) * 32 + tok] = f32x2{lm, m2};
    __syncthreads();
    STAMP(1);
    float mu = 0.f, M2 = 0.f;
    f32x2 p[8];
#pragma unroll
    for (int i = 0; i < 8; ++i) { p[i] = part[i * 32 + tok]; mu += p[i][0]; M2 += p[i][1]; }
    mu *= 0.125f;
#pragma unroll
    for (int i = 0; i < 8; ++i) { float d = p[i][0] - mu; M2 += 56.f * d * d; }
    const float rstd = rsqrtf(M2 * (1.f / 448.f) + 1e-5f);
    char* dst = ws + OFF_XN + ((size_t)(mt * 28 + 7 * w) * 64 + lane) * 16;
#pragma unroll
    for (int i = 0; i < 7; ++i) {
      u32x4 o;
#pragma unroll
      for (int j = 0; j < 4; ++j) o[j] = pack2((bflo(v[i][j]) - mu) * rstd, (bfhi(v[i][j]) - mu) * rstd);
      st16w<0>(dst + i * 1024, o);
    }
  } else {
    const int rb = b - 136;
    retile_unit(wqkv, ws, 0, rb / 3, rb % 3, t);
  }
  asm volatile("s_waitcnt vmcnt(0)" ::"v"(pf_d) : "memory");
  PFLUSH(0);
}

constexpr int k1_posW(int c) { return c == 0 ? 3 : c == 1 ? 10 : c == 2 ? 13 : 20 + 7 * (c - 3); }
constexpr int k1_posX(int k) { return (k < 4 ? 4 : k < 8 ? 10 : k < 24 ? 13 + 3 * (k / 4 - 2) : 22) + k; }
constexpr int k1_iss(int k) { return 18 + (2 * k < 32 ? 2 * k : 32); }
__global__ __launch_bounds__(256) __attribute__((amdgpu_waves_per_eu(1, 1))) void k1_qkv(char* __restrict__ ws) {
  extern __shared__ __attribute__((aligned(16))) char lds[];
  const int t = threadIdx.x, lane = t & 63, w = __builtin_amdgcn_readfirstlane(t >> 6);
  PDECL; STAMP(0);
  CODE_PF(5632);
  const int xc = blockIdx.x & 7, bi = blockIdx.x >> 3;
  const int mb = (xc & 3) * 4 + (bi & 3), head = (xc >> 2) * 8 + (bi >> 2);
  const int mt = mb * 4 + w;
  const int hi = lane >> 5, l31 = lane & 31;
  const char* A0 = ws + OFF_XN + (size_t)mt * 28 * 1024 + lane * 16 + 4096;
  const char* B0 = ws + OFF_WQKV + ((size_t)head * 84 + w * 21) * 1024 + lane * 16 + 4096;
  const char* A[4] = {A0, A0 + 8192, A0 + 16384, A0 + 24576};
  const char* B[3] = {B0, B0 + 8192, B0 + 16384};
  char* modv = lds + 87 * 1024 + w * 1024;
  float* P = (float*)(lds + 91 * 1024);
  GLDS16(ws + OFF_MODA + (4 * (lane >> 3) + w) * 128 + (lane & 7) * 16, modv);
  u32x4 xr[29], sw[7][3];
  auto ldW = [&](auto cc, auto ic) {
    constexpr int c = decltype(cc)::value, i = decltype(ic)::value;
    gload16o<((c * 3 + i) & 7) * 1024 - 4096>(sw[c][i], B[(c * 3 + i) >> 3]);
  };
  auto ldX = [&](auto kc) {
    constexpr int k = decltype(kc)::value;
    gload16ao<(k & 7) * 1024 - 4096>(xr[k], A[k >> 3]);
  };
  auto ldN = [&](auto nc) {
    constexpr int n = decltype(nc)::value, g = n / 7, r = n % 7;
    if constexpr (n >= 35) { if constexpr (n < 39) ldX(IC(24 + n - 35)); }
    else if constexpr (r < 3) ldW(IC(g + 2), IC(r));
    else ldX(IC(4 * (g + 1) + r - 3));
  };
  static_for<0, 3>([&](auto i) { ldW(IC(0), i); });
  static_for<0, 4>(ldX);
  static_for<0, 3>([&](auto i) { ldW(IC(1), i); });
  f32x16 acc[3], accb[3];
#pragma unroll
  for (int i = 0; i < 3; ++i)
#pragma unroll
    for (int r = 0; r < 16; ++r) { acc[i][r] = 0.f; accb[i][r] = 0.f; }
  const unsigned wl0 = lds_addr(lds) + lane * 16, wl1 = wl0 + 49152;
  const unsigned sa0 = wl0 + w * 3072, sa1 = sa0 + 49152;
  const unsigned ml = lds_addr(modv) + hi * 64;
  u32x4 wb[3][3], mv[7][3], so[3];
  auto rdw = [&](auto kc) {
    constexpr int k = decltype(kc)::value;
    constexpr int o = (k & 15) * 3072;
    const unsigned a = (k >= 16) ? wl1 : wl0;
    lread16<o>(wb[k % 3][0], a); lread16<o + 1024>(wb[k % 3][1], a); lread16<o + 2048>(wb[k % 3][2], a);
  };
  auto rdm = [&](auto cc) {
    constexpr int c = decltype(cc)::value;
    lread16<c * 128>(mv[c][0], ml); lread16<c * 128 + 16>(mv[c][1], ml);
    lread16<c * 128 + 32>(mv[c][2], ml);
  };
  auto rdym = [&](auto cc) {
    constexpr int c = decltype(cc)::value;
    rdyv(mv[c][0]); rdyv(mv[c][1]); rdyv(mv[c][2]);
  };
  auto word = [&](auto cc, auto qc) {
    constexpr int c = decltype(cc)::value, q = decltype(qc)::value, i = q >> 2, j = q & 3;
    if constexpr (j == 0) accb[i] = MFMA32(BF8(mv[c][2]), BF8(sw[c][i]), accb[i]);
    const f32x4 sc = F4(mv[c][j >> 1]);
    so[i][j] = scale2(sw[c][i][j], sc[(2 * j) & 3], sc[(2 * j + 1) & 3]);
    if constexpr (j == 3) lwrite16<(c & 3) * 12288 + i * 1024>(so[i], c < 4 ? sa0 : sa1);
  };
  PDECL2;
  BST(0);
  vwait<7>();
  BST(8);
  rdyv(sw[0][0]); rdyv(sw[0][1]); rdyv(sw[0][2]);
  rdm(IC(0)); rdm(IC(1)); rdm(IC(2));
  ldN(IC(0));
  lwait<0>();
  static_for<0, 3>(rdym);
  static_for<0, 12>([&](auto q) {
    word(IC(0), q);
    if constexpr (q % 2 == 0) ldN(IC(1 + q / 2));
    SB();
  });
  wait_bar<7>();
  STAMP(1);
  rdya(xr[0]);
  rdyv(sw[1][0]); rdyv(sw[1][1]); rdyv(sw[1][2]);
  rdw(IC(0)); rdw(IC(1));
  lwait<3>();
  rdyv(wb[0][0]); rdyv(wb[0][1]); rdyv(wb[0][2]);
  xr[28] = u32x4{0x3f803f80u, 0, 0, 0};
  SB();
  static_for<0, 29>([&](auto kc) {
    constexpr int k = decltype(kc)::value;
    constexpr int cn = (k < 2) ? 1 : (k + 6) / 4, r = (k + 6) % 4;
    constexpr bool PR = k < 22;
    auto gap = [&](auto gc) {
      constexpr int g = decltype(gc)::value;
      if constexpr (k < 2) { word(IC(1), IC(6 * k + 2 * g)); word(IC(1), IC(6 * k + 2 * g + 1)); }
      else if constexpr (PR) word(IC(cn), IC(3 * r + g));
    };
    acc[0] = MFMA32(BF8(wb[k % 3][0]), BF8(xr[k]), acc[0]);
    constexpr bool BS = (k + 2) % 4 == 0;
    if constexpr (BS && k < 22) {
      constexpr int j = (k + 2) / 4;
      vwait<k1_iss(k) - 1 - k1_posW(j + 1)>();
      rdyv(sw[j + 1][0]); rdyv(sw[j + 1][1]); rdyv(sw[j + 1][2]);
    }
    if constexpr (k >= 2 && r == 2 && cn + 1 < 7) rdm(IC(cn + 1));
    if constexpr (!BS && k + 2 < 29) rdw(IC(k + 2));
    gap(IC(0));
    SB();
    acc[1] = MFMA32(BF8(wb[k % 3][1]), BF8(xr[k]), acc[1]);
    if constexpr (BS) {
      BST((k + 2) / 4);
      wait_bar<63>();
      BST(8 + (k + 2) / 4);
      rdw(IC(k + 2));
    }
    ldN(IC(7 + 2 * k));
    gap(IC(1));
    if constexpr (k == 23) {
      if (w < 3) {
        float bs = P[(w * 4 + 0) * 64 + lane] + P[(w * 4 + 1) * 64 + lane] + P[(w * 4 + 2) * 64 + lane] + P[(w * 4 + 3) * 64 + lane];
        const float bh = rbf(bs);
        *(u32x4*)(lds + (84 + w) * 1024 + lane * 16) = u32x4{hi ? 0u : pack2(bh, bs - bh), 0, 0, 0};
      }
    }
    SB();
    acc[2] = MFMA32(BF8(xr[k]), BF8(wb[k % 3][2]), acc[2]);
    ldN(IC(8 + 2 * k));
    gap(IC(2));
    if constexpr (k == 21) {
#pragma unroll
      for (int i = 0; i < 3; ++i) P[(i * 4 + w) * 64 + lane] = accb[i][0];
    }
    if constexpr (k + 1 < 29) {
      constexpr int nw = k == 0 ? 1 : k == 1 ? 2 : k == 21 ? 4 : (PR && r != 0) ? 1 : 0;
      vlwait<(k + 1 < 28) ? k1_iss(k + 1) - 1 - k1_posX(k + 1) : 63, (k + 2 < 29) ? 3 + nw : 0>();
      if constexpr (k + 1 < 28) rdya(xr[k + 1]);
      rdyv(wb[(k + 1) % 3][0]); rdyv(wb[(k + 1) % 3][1]); rdyv(wb[(k + 1) % 3][2]);
      if constexpr (k >= 2 && r == 2 && cn + 1 < 7) rdym(IC(cn + 1));
    }
    SB();
  });
  STAMP(2);
  const float qs = 0.18898223650461362f * 1.4426950408889634f;
#pragma unroll
  for (int p = 0; p < 2; ++p) {
    char* base = ws + (p ? OFF_K : OFF_Q) + ((size_t)(head * 128 + mt * 2 + (l31 >> 4)) * 64 + (l31 & 15)) * 16;
    const float sc = p ? 1.f : qs;
#pragma unroll
    for (int s = 0; s < 2; ++s) {
      u32x4 o;
#pragma unroll
      for (int j = 0; j < 4; ++j) o[j] = pack2(acc[p][8 * s + 2 * j] * sc, acc[p][8 * s + 2 * j + 1] * sc);
      st16w<1>(base + (2 * s + hi) * 256, o);
    }
  }
  {
    char* base = ws + OFF_V + ((size_t)((head * 64 + mt) * 2 + (l31 >> 4)) * 64 + (l31 & 15)) * 16;
#pragma unroll
    for (int s = 0; s < 2; ++s) {
      u32x4 o = {pack2(acc[2][4 * s], acc[2][4 * s + 1]), pack2(acc[2][4 * s + 2], acc[2][4 * s + 3]),
                 pack2(acc[2][8 + 4 * s], acc[2][8 + 4 * s + 1]), pack2(acc[2][8 + 4 * s + 2], acc[2][8 + 4 * s + 3])};
      if (l31 == 28) o = u32x4{0x3f803f80u, 0x3f803f80u, 0x3f803f80u, 0x3f803f80u};
      st16w<1>(base + (2 * s + hi) * 256, o);
    }
  }
  CODE_PF_END;
  PFLUSH(1);
  PFLUSH2(6);
}

template <bool ODD>
DEVI void attn_pv(const char* vp, const u32x2 (&p)[17], f32x4& o0, f32x4& o1) {
  bf16x8 vf[9][2];
#pragma unroll
  for (int v = 0; v < 3; ++v) { vf[v][0] = *(const bf16x8*)(vp + v * 2048); vf[v][1] = *(const bf16x8*)(vp + v * 2048 + 1024); }
#pragma unroll
  for (int v = 0; v < 9; ++v) {
    if (v + 3 < 9) {
      vf[v + 3][0] = *(const bf16x8*)(vp + (v + 3) * 2048);
      vf[v + 3][1] = *(const bf16x8*)(vp + (v + 3) * 2048 + 1024);
    }
    const int i0 = 2 * v - (ODD ? 1 : 0), i1 = i0 + 1;
    const u32x2 z = {0, 0};
    const u32x2 pa = i0 >= 0 ? p[i0 >= 0 ? i0 : 0] : z, pc = i1 < 17 ? p[i1 < 17 ? i1 : 0] : z;
    const u32x4 pw = {pa[0], pa[1], pc[0], pc[1]};
    const bf16x8 pf = __builtin_bit_cast(bf16x8, pw);
    o0 = MFMA16(vf[v][0], pf, o0);
    o1 = MFMA16(vf[v][1], pf, o1);
    SB();
  }
}

DEVI void attn_wave(const char* ldsK, const char* ldsV, const char* ldsQ, int w, int lane, int nneg, char* odst, int kbase) {
  const int g = lane >> 4, qq = lane & 15;
  const bf16x8 q = *(const bf16x8*)(ldsQ + w * 1024 + lane * 16);
  const float NEG = -1e30f;
  f32x4 s[17];
  const char* kp = ldsK + w * 1024 + lane * 16;
  bf16x8 kf[17];
#pragma unroll
  for (int tt = 0; tt < 4; ++tt) kf[tt] = *(const bf16x8*)(kp + tt * 1024);
#pragma unroll
  for (int tt = 0; tt < 17; ++tt) {
    if (tt + 4 < 17) kf[tt + 4] = *(const bf16x8*)(kp + (tt + 4) * 1024);
    s[tt] = MFMA16(kf[tt], q, (f32x4{0.f, 0.f, 0.f, 0.f}));
    SB();
  }
#pragma unroll
  for (int r = 0; r < 4; ++r) {
    s[0][r] = (4 * g + r > qq) ? s[0][r] : NEG;
    s[16][r] = (4 * g + r <= qq) ? s[16][r] : NEG;
  }
  if (nneg > 0) {
#pragma unroll
    for (int tt = 0; tt < 16; ++tt)
      if (w + tt < nneg) s[tt] = f32x4{NEG, NEG, NEG, NEG};
  }
  float m = NEG;
#pragma unroll
  for (int tt = 0; tt < 17; ++tt) m = fmaxf(fmaxf(m, fmaxf(s[tt][0], s[tt][1])), fmaxf(s[tt][2], s[tt][3]));
  m = fmaxf(m, __shfl_xor(m, 16));
  m = fmaxf(m, __shfl_xor(m, 32));
  u32x2 pb[17];
#pragma unroll
  for (int tt = 0; tt < 17; ++tt) {
    const float e0 = __builtin_amdgcn_exp2f(s[tt][0] - m), e1 = __builtin_amdgcn_exp2f(s[tt][1] - m);
    const float e2 = __builtin_amdgcn_exp2f(s[tt][2] - m), e3 = __builtin_amdgcn_exp2f(s[tt][3] - m);
    pb[tt] = u32x2{pack2(e0, e1), pack2(e2, e3)};
  }
  wait_bar<0>();
  f32x4 o0 = {0.f, 0.f, 0.f, 0.f}, o1 = {0.f, 0.f, 0.f, 0.f};
  const char* vp = ldsV + (w >> 1) * 2048 + lane * 16;
  if (w & 1) attn_pv<true>(vp, pb, o0, o1);
  else attn_pv<false>(vp, pb, o0, o1);
  const float sum = __shfl(o1[0], 48 + qq);
  const float inv = 1.f / sum;
  {
    const int k = kbase + 4 * g;
    u32x2 o = {pack2(o0[0] * inv, o0[1] * inv), pack2(o0[2] * inv, o0[3] * inv)};
    ST8(odst + (size_t)(k >> 4) * 1024 + ((k >> 3) & 1) * 512 + (k & 4) * 2, o);
  }
  if (g < 3) {
    const int k = kbase + 16 + 4 * g;
    u32x2 o = {pack2(o1[0] * inv, o1[1] * inv), pack2(o1[2] * inv, o1[3] * inv)};
    ST8(odst + (size_t)(k >> 4) * 1024 + ((k >> 3) & 1) * 512 + (k & 4) * 2, o);
  }
}

__global__ __launch_bounds__(512) __attribute__((amdgpu_waves_per_eu(4, 4))) void k2_attn(
    const uint16_t* __restrict__ wo, const uint16_t* __restrict__ wg, const uint16_t* __restrict__ wu,
    char* __restrict__ ws) {
  extern __shared__ __attribute__((aligned(16))) char lds[];
  const int t = threadIdx.x, lane = t & 63, w = __builtin_amdgcn_readfirstlane(t >> 6);
  PDECL; STAMP(0);
  if (__builtin_expect(blockIdx.x >= 256, 0)) {
    const int u = blockIdx.x - 256;
    if (u < 224) fold_unit((u & 4) ? wu : wg, ws, (u >> 2) & 1, u >> 3, u & 3, t, lds PPASS);
    else if (w < 4) retile_unit(wo, ws, 2, u - 224, 0, t);
    PFLUSH(2);
    return;
  }
  CODE_PF(5504);
  const int xc = blockIdx.x & 7, bi = blockIdx.x >> 3;
  const int head = xc * 2 + (bi & 1), qb = bi >> 1;
  char* ldsK = lds;
  char* ldsV = lds + 24 * 1024;
  char* ldsQ = lds + 48 * 1024;
  const int sub0 = qb * 8 - 16;
  GLDS16(ws + OFF_Q + (size_t)(head * 128 + qb * 8 + w) * 1024 + lane * 16, ldsQ + w * 1024);
#pragma unroll
  for (int i = 0; i < 3; ++i) {
    const int p = w * 3 + i;
    const int sidx = max(sub0 + p, 0);
    GLDS16(ws + OFF_K + (size_t)(head * 128 + sidx) * 1024 + lane * 16, ldsK + p * 1024);
  }
#pragma unroll
  for (int i = 0; i < 3; ++i) {
    const int p = w * 3 + i;
    const int sidx = max(sub0 + p, 0);
    GLDS16(ws + OFF_V + (size_t)(head * 128 + sidx) * 1024 + lane * 16, ldsV + p * 1024);
  }
  wait_bar<3>();
  STAMP(1);
  const int tok = qb * 128 + w * 16 + (lane & 15);
  char* odst = ws + OFF_O + (size_t)(tok >> 5) * 28 * 1024 + (tok & 31) * 16;
  const int kbase = head * 28;
  const int nneg = -sub0;
  attn_wave(ldsK, ldsV, ldsQ, w, lane, nneg, odst, kbase);
  CODE_PF_END;
  PFLUSH(2);
}

DEVI f32x16 ksplit_reduce(f32x16 (&acc)[4], char* lds, int w, int lane) {
#pragma unroll
  for (int tl = 0; tl < 4; ++tl) {
    if (tl != w) {
      const int slot = (w > tl) ? w - 1 : w;
      char* p = lds + (tl * 3 + slot) * 4096 + lane * 16;
#pragma unroll
      for (int q = 0; q < 4; ++q)
        *(f32x4*)(p + q * 1024) = f32x4{acc[tl][4 * q], acc[tl][4 * q + 1], acc[tl][4 * q + 2], acc[tl][4 * q + 3]};
    }
  }
  f32x16 r = (w == 0) ? acc[0] : (w == 1) ? acc[1] : (w == 2) ? acc[2] : acc[3];
  __syncthreads();
#pragma unroll
  for (int s = 0; s < 3; ++s) {
    const char* p = lds + (w * 3 + s) * 4096 + lane * 16;
#pragma unroll
    for (int q = 0; q < 4; ++q) {
      const f32x4 v = *(const f32x4*)(p + q * 1024);
#pragma unroll
      for (int j = 0; j < 4; ++j) r[4 * q + j] += v[j];
    }
  }
  return r;
}

__global__ __launch_bounds__(256) __attribute__((amdgpu_waves_per_eu(1, 1))) void k3_wo(const uint16_t* __restrict__ x,
                                                                                        char* __restrict__ ws) {
  __shared__ __attribute__((aligned(16))) char lds[48 * 1024];
  const int t = threadIdx.x, lane = t & 63, w = __builtin_amdgcn_readfirstlane(t >> 6);
  PDECL; STAMP(0);
  if (__builtin_expect(blockIdx.x >= 224, 0)) {
    if (t < 224) {
      const int z = t >= 112, col = (blockIdx.x - 224) * 112 + t - 112 * z;
      f32x2 v = {0.f, 0.f};
#pragma unroll
      for (int i = 0; i < 28; ++i) v += *(const f32x2*)(ws + OFF_PART + ((size_t)i * 3584 + col) * 8);
      const float ch = rbf(v[0]), cl = v[0] - ch, bh = rbf(v[1]), bl = v[1] - bh;
      u32x4 o = {pack2(ch, ch), pack2(cl, cl), pack2(bh, bh), pack2(bl, bl)};
      if (z) o = u32x4{0, 0, 0, 0};
      const int mat = col >= 1792, f = col - 1792 * mat;
      ST16(ws + OFF_WGU + ((size_t)((f >> 4) * 29 + 28) * 64 + z * 32 + mat * 16 + swap23(f & 15)) * 16, o);
    }
    PFLUSH(3);
    return;
  }
  CODE_PF(3264);
  const int xc = blockIdx.x & 7, bi = blockIdx.x >> 3;
  const int mb = xc * 4 + (bi & 3), nb = bi >> 2;
  const char* A = ws + OFF_O + ((size_t)mb * 2 * 28 + 7 * w) * 1024 + lane * 16;
  const char* B = ws + OFF_WO + ((size_t)nb * 2 * 28 + 7 * w) * 1024 + lane * 16;
  const int wm = w & 1, wn = w >> 1;
  const int hi = lane >> 5, l31 = lane & 31;
  const int mt = mb * 2 + wm, nt = nb * 2 + wn;
  bf16x8 a[7][2], bw[7][2];
#pragma unroll
  for (int k = 0; k < 7; ++k) {
    a[k][0] = *(const bf16x8*)(A + k * 1024);
    bw[k][0] = *(const bf16x8*)(B + k * 1024);
    a[k][1] = *(const bf16x8*)(A + (28 + k) * 1024);
    bw[k][1] = *(const bf16x8*)(B + (28 + k) * 1024);
  }
  u32x4 xv[2];
  f32x4 gv[4];
  {
    const uint16_t* xs = x + (size_t)(mt * 32 + l31) * 448 + nt * 32 + hi * 8;
    xv[0] = *(const u32x4*)xs;
    xv[1] = *(const u32x4*)(xs + 16);
    const float* gp = (const float*)(ws + OFF_GAB) + nt * 32 + hi * 8;
    gv[0] = *(const f32x4*)gp; gv[1] = *(const f32x4*)(gp + 4);
    gv[2] = *(const f32x4*)(gp + 16); gv[3] = *(const f32x4*)(gp + 20);
  }
  __builtin_amdgcn_sched_barrier(0);
  f32x16 acc[4];
#pragma unroll
  for (int i = 0; i < 4; ++i)
#pragma unroll
    for (int r = 0; r < 16; ++r) acc[i][r] = 0.f;
#pragma unroll
  for (int k = 0; k < 7; ++k) {
    acc[0] = MFMA32(bw[k][0], a[k][0], acc[0]);
    acc[1] = MFMA32(bw[k][0], a[k][1], acc[1]);
    acc[2] = MFMA32(bw[k][1], a[k][0], acc[2]);
    acc[3] = MFMA32(bw[k][1], a[k][1], acc[3]);
  }
  STAMP(1);
  const f32x16 r = ksplit_reduce(acc, lds, w, lane);
  STAMP(2);
  float v[16];
#pragma unroll
  for (int s = 0; s < 2; ++s) {
    u32x4 o;
#pragma unroll
    for (int j = 0; j < 4; ++j) {
      const float ga0 = gv[2 * s + (j >> 1)][(2 * j) & 3], ga1 = gv[2 * s + (j >> 1)][(2 * j + 1) & 3];
      o[j] = pack2(bflo(xv[s][j]) + ga0 * r[8 * s + 2 * j], bfhi(xv[s][j]) + ga1 * r[8 * s + 2 * j + 1]);
      v[8 * s + 2 * j] = bflo(o[j]);
      v[8 * s + 2 * j + 1] = bfhi(o[j]);
    }
    st16w<3>(ws + OFF_X1 + ((size_t)(mt * 28 + nt * 2 + s) * 64 + lane) * 16, o);
  }
  float sm = 0.f;
#pragma unroll
  for (int j = 0; j < 16; ++j) sm += v[j];
  sm += __shfl_xor(sm, 32);
  const float mean = sm * (1.f / 32.f);
  float m2 = 0.f;
#pragma unroll
  for (int j = 0; j < 16; ++j) { const float d = v[j] - mean; m2 += d * d; }
  m2 += __shfl_xor(m2, 32);
  if (hi == 0) *(f32x2*)(ws + OFF_STATS + ((size_t)(mt * 8 + nb) * 32 + l31) * 16 + wn * 8) = f32x2{mean, m2};
  CODE_PF_END;
  PFLUSH(3);
}

constexpr int k4_posG(int g) { return g < 2 ? g : g < 4 ? g + 2 : g <= 9 ? 8 + 3 * (g - 4) : g == 10 ? 30 : 4 + 3 * g; }
constexpr int k4_posX(int s) { return s == 0 ? 3 : s == 1 ? 7 : s <= 6 ? 10 + 3 * (s - 2) : s == 7 ? 27 : s == 8 ? 34 : s < 27 ? 12 + 3 * s : 92; }
constexpr int k4_iss(int k) { return k == 0 ? 23 : k == 1 ? 30 : k <= 20 ? 31 + 3 * k : 93; }
__global__ __launch_bounds__(256) __attribute__((amdgpu_waves_per_eu(1, 1))) void k4_gu(
    const uint16_t* __restrict__ wd, const uint16_t* __restrict__ cvec, const uint16_t* __restrict__ wmod,
    char* __restrict__ ws) {
  extern __shared__ __attribute__((aligned(16))) char lds[];
  const int t = threadIdx.x, lane = t & 63, w = __builtin_amdgcn_readfirstlane(t >> 6);
  PDECL; STAMP(0);
  if (__builtin_expect(blockIdx.x >= 224, 0)) {
    const int j = blockIdx.x - 224;
    u32x4 wv[2][8];
#pragma unroll
    for (int h = 0; h < 2; ++h) {
      const int id = min(t + 256 * h, 391);
      const uint16_t* p = wd + (size_t)((j * 7 + id / 56) * 8) * 448 + (id % 56) * 8;
#pragma unroll
      for (int i = 0; i < 8; ++i) wv[h][i] = *(const u32x4*)(p + (size_t)i * 448);
    }
    const int v = (j & 7) * 2 + (j >> 3);
    if (j < 16 && v < 14) mod_unit(cvec, wmod, ws, 5 * 448 + v * 32, t, (float*)lds);
#pragma unroll
    for (int h = 0; h < 2; ++h) {
      const int id = t + 256 * h, ko = j * 7 + id / 56, coct = id % 56;
      if (id < 392) {
#pragma unroll
        for (int c = 0; c < 8; ++c) {
          u32x4 o;
#pragma unroll
          for (int i = 0; i < 4; ++i)
            o[i] = __builtin_amdgcn_perm(wv[h][2 * i + 1][c >> 1], wv[h][2 * i][c >> 1], (c & 1) ? 0x07060302u : 0x05040100u);
          const int n = coct * 8 + c;
          ST16(ws + OFF_WD + ((size_t)((n >> 5) * 112 + (ko >> 1)) * 64 + (ko & 1) * 32 + swap23(n & 31)) * 16, o);
        }
      }
    }
    PFLUSH(4);
    return;
  }
  CODE_PF(13760);
  const int xc = blockIdx.x & 7, bi = blockIdx.x >> 3;
  const int mb = (xc & 1) * 4 + (bi & 3), fb = (xc >> 1) * 7 + (bi >> 2);
  const int mt = mb * 8 + 2 * w;
  const char* A = ws + OFF_X1 + (size_t)mt * 28 * 1024 + lane * 16;
  const char* B = ws + OFF_WGU + (size_t)(fb * 4 + w) * 29 * 1024 + lane * 16;
  const int hi = lane >> 5, l31 = lane & 31;
  u32x4 xr[29][2], st[2][4];
  auto ldG = [&](auto gc) {
    constexpr int g = decltype(gc)::value;
    GLDS16(B + g * 1024, lds + (g * 4 + w) * 1024);
  };
  auto ldXa = [&](auto sc) { constexpr int s = decltype(sc)::value; gload16a(xr[s][0], A + s * 1024); };
  auto ldXb = [&](auto sc) { constexpr int s = decltype(sc)::value; gload16a(xr[s][1], A + (28 + s) * 1024); };
  ldG(IC(0)); ldG(IC(1)); ldXa(IC(0)); ldXb(IC(0)); ldG(IC(2)); ldG(IC(3)); ldXa(IC(1)); ldXb(IC(1));
  const char* S = ws + OFF_STATS + (size_t)mt * 4096 + (hi * 4 * 32 + l31) * 16;
  static_for<2, 7>([&](auto sc) { ldG(IC(sc + 2)); ldXa(sc); ldXb(sc); });
  f32x16 acc[2][4];
#pragma unroll
  for (int i = 0; i < 8; ++i)
#pragma unroll
    for (int r = 0; r < 16; ++r) acc[i >> 2][i & 3][r] = 0.f;
  const unsigned wa0 = lds_addr(lds) + lane * 16, wa1 = wa0 + 65536;
  u32x4 wb[3][4];
  float rstd[2], var[2], ta[4], tb[4], d[8], mu;
  f32x4 p[4];
  auto stat = [&](auto tc, auto sc) {
    constexpr int tt = decltype(tc)::value, q = decltype(sc)::value;
    if constexpr (q == 0) {
#pragma unroll
      for (int i = 0; i < 4; ++i) p[i] = F4(st[tt][i]);
      if (hi) p[3] = f32x4{0.f, 0.f, 0.f, 0.f};
    } else if constexpr (q == 1 || q == 2) {
#pragma unroll
      for (int i = 2 * q - 2; i < 2 * q; ++i) { ta[i] = fadd1(p[i][0], p[i][2]); tb[i] = fadd1(p[i][1], p[i][3]); }
    } else if constexpr (q == 3) {
      ta[0] = fadd1(ta[0], ta[1]); ta[2] = fadd1(ta[2], ta[3]); tb[0] = fadd1(tb[0], tb[1]); tb[2] = fadd1(tb[2], tb[3]);
    } else if constexpr (q == 4) {
      ta[0] = fadd1(ta[0], ta[2]); tb[0] = fadd1(tb[0], tb[2]);
    } else if constexpr (q == 5) {
      ta[0] = xhalf_sum(ta[0]);
    } else if constexpr (q == 6) {
      mu = fmul1(ta[0], 1.f / 14.f);
      if (hi) { p[3][0] = mu; p[3][2] = mu; }
    } else if constexpr (q == 7 || q == 8) {
#pragma unroll
      for (int i = 2 * q - 14; i < 2 * q - 12; ++i) { d[2 * i] = fsub1(p[i][0], mu); d[2 * i + 1] = fsub1(p[i][2], mu); }
    } else if constexpr (q == 9) {
#pragma unroll
      for (int i = 0; i < 4; ++i) ta[i] = fmul1(d[i], d[i]);
    } else if constexpr (q == 10) {
#pragma unroll
      for (int i = 0; i < 4; ++i) fmac1(ta[i], d[4 + i], d[4 + i]);
    } else if constexpr (q == 11) {
      ta[0] = fadd1(ta[0], ta[1]); ta[2] = fadd1(ta[2], ta[3]);
      ta[1] = rbf(mu);
    } else if constexpr (q == 12) {
      fmac1(tb[0], fadd1(ta[0], ta[2]), 32.f);
    } else if constexpr (q == 13) {
      tb[0] = xhalf_sum(tb[0]);
    } else if constexpr (q == 14) {
      var[tt] = fmaf(tb[0], 1.f / 448.f, 1e-5f);
      rstd[tt] = __builtin_amdgcn_rsqf(var[tt]);
    } else if constexpr (q == 15) {
      ta[0] = fmul1(var[tt], rstd[tt]);
      ta[3] = fsub1(ta[1], mu);
    } else if constexpr (q == 16) {
      ta[2] = rbf(ta[0]);
    } else if constexpr (q == 17) {
      const unsigned pm = pack2(-ta[1], ta[3]), ps = pack2(ta[2], fsub1(ta[0], ta[2]));
      xr[28][tt] = u32x4{pm, pm, ps, ps};
    }
  };
  auto rdw = [&](auto kc) {
    constexpr int k = decltype(kc)::value;
    constexpr int o = (k & 15) * 4096;
    const unsigned a = (k >= 16) ? wa1 : wa0;
    lread16<o>(wb[k % 3][0], a); lread16<o + 1024>(wb[k % 3][1], a);
    lread16<o + 2048>(wb[k % 3][2], a); lread16<o + 3072>(wb[k % 3][3], a);
  };
  PDECL2;
  BST(0);
  vbar<21>();
  BST(8);
  STAMP(1);
  rdw(IC(0)); rdw(IC(1));
  vlwait<19, 4>();
  rdya(xr[0][0]); rdya(xr[0][1]);
  rdyv(wb[0][0]); rdyv(wb[0][1]); rdyv(wb[0][2]); rdyv(wb[0][3]);
  SB();
  static_for<0, 29>([&](auto kc) {
    constexpr int k = decltype(kc)::value;
    static_for<0, 8>([&](auto gc) {
      constexpr int g = decltype(gc)::value;
      acc[g & 1][g >> 1] = MFMA32(BF8(wb[k % 3][g >> 1]), BF8(xr[k][g & 1]), acc[g & 1][g >> 1]);
      if constexpr (g == 0) {
        if constexpr (k % 2 == 0 && k + 2 < 29) {
          if constexpr (k % 4 == 0) BST(1 + k / 4);
          vbar<k4_iss(k) - 1 - k4_posG(k + 3 < 29 ? k + 3 : 28)>();
          if constexpr (k % 4 == 0) BST(9 + k / 4);
        }
        if constexpr (k + 2 < 29) rdw(IC(k + 2));
      }
      if constexpr (g == 1 && k + 9 < 29) ldG(IC(k + 9));
      if constexpr (g == 3 && k + 7 < 28) ldXa(IC(k + 7));
      if constexpr (g == 5 && k + 7 < 28) ldXb(IC(k + 7));
      if constexpr (k < 2 && (g == 2 || g == 4 || g >= 6)) gload16(st[k & 1][g == 7 ? 3 : g / 2 - 1], S + k * 4096 + (g == 7 ? 3 : g / 2 - 1) * 512);
      if constexpr (k >= 9 && k < 14) stat(IC((((k - 9) * 8 + g) / 20) & 1), IC(((k - 9) * 8 + g) % 20));
      if constexpr (g == 7 && k + 1 < 29) {
        vlwait<(k + 1 < 28) ? k4_iss(k + 1) - 1 - k4_posX(k + 1) : 63, (k + 2 < 29) ? 4 : 0>();
        if constexpr (k + 1 < 28) { rdya(xr[k + 1][0]); rdya(xr[k + 1][1]); }
        rdyv(wb[(k + 1) % 3][0]); rdyv(wb[(k + 1) % 3][1]); rdyv(wb[(k + 1) % 3][2]); rdyv(wb[(k + 1) % 3][3]);
        if constexpr (k == 8) {
#pragma unroll
          for (int i = 0; i < 8; ++i) rdyv(st[i >> 2][i & 3]);
        }
      }
      SB();
    });
  });
  STAMP(2);
  const float c0 = -2.f * 0.7978845608028654f * 1.4426950408889634f, c1 = c0 * 0.044715f;
  char* hdst = ws + OFF_HFF + ((size_t)(mt * 112 + fb * 4) * 64 + lane) * 16;
#pragma unroll
  for (int tt = 0; tt < 2; ++tt)
#pragma unroll
    for (int n = 0; n < 4; ++n) {
      const float e0 = c0 * rstd[tt], e1 = c1 * rstd[tt] * rstd[tt] * rstd[tt];
      float hh[8];
#pragma unroll
      for (int j = 0; j < 8; ++j) {
        const float g = acc[tt][n][j], u = acc[tt][n][8 + j];
        const float e = __builtin_amdgcn_exp2f(g * fmaf(g * g, e1, e0));
        hh[j] = g * u * __builtin_amdgcn_rcpf(fmaf(e, var[tt], var[tt]));
      }
      u32x4 o = {pack2(hh[0], hh[1]), pack2(hh[2], hh[3]), pack2(hh[4], hh[5]), pack2(hh[6], hh[7])};
      st16w<4>(hdst + (tt * 112 + n) * 1024, o);
    }
  CODE_PF_END;
  PFLUSH(4);
  PFLUSH2(8);
}

__global__ __launch_bounds__(256) __attribute__((amdgpu_waves_per_eu(1, 1))) void k5_down(char* __restrict__ ws,
                                                                                          uint16_t* __restrict__ out) {
  __shared__ __attribute__((aligned(16))) char lds[48 * 1024];
  const int t = threadIdx.x, lane = t & 63, w = __builtin_amdgcn_readfirstlane(t >> 6);
  PDECL; STAMP(0);
  CODE_PF(3264);
  const int xc = blockIdx.x & 7, bi = blockIdx.x >> 3;
  const int mb = xc * 4 + (bi & 3), nb = bi >> 2;
  const char* A = ws + OFF_HFF + ((size_t)mb * 2 * 112 + 28 * w) * 1024 + lane * 16;
  const char* B = ws + OFF_WD + ((size_t)nb * 2 * 112 + 28 * w) * 1024 + lane * 16;
  const int wm = w & 1, wn = w >> 1;
  const int hi = lane >> 5, l31 = lane & 31;
  const int mt = mb * 2 + wm, nt = nb * 2 + wn;
  constexpr int D = 12;
  bf16x8 a[D][2], bw[D][2];
  auto ld = [&](int k) {
    a[k % D][0] = *(const bf16x8*)(A + k * 1024);
    bw[k % D][0] = *(const bf16x8*)(B + k * 1024);
    a[k % D][1] = *(const bf16x8*)(A + (112 + k) * 1024);
    bw[k % D][1] = *(const bf16x8*)(B + (112 + k) * 1024);
  };
#pragma unroll
  for (int k = 0; k < D; ++k) ld(k);
  u32x4 xv[2];
  f32x4 gv[4];
  f32x16 acc[4];
#pragma unroll
  for (int i = 0; i < 4; ++i)
#pragma unroll
    for (int r = 0; r < 16; ++r) acc[i][r] = 0.f;
  __builtin_amdgcn_sched_barrier(0);
#pragma unroll
  for (int k = 0; k < 28; ++k) {
    acc[0] = MFMA32(bw[k % D][0], a[k % D][0], acc[0]);
    acc[1] = MFMA32(bw[k % D][0], a[k % D][1], acc[1]);
    acc[2] = MFMA32(bw[k % D][1], a[k % D][0], acc[2]);
    acc[3] = MFMA32(bw[k % D][1], a[k % D][1], acc[3]);
    if (k + D < 28) ld(k + D);
    if (k + D == 28) {
      const char* xs = ws + OFF_X1 + ((size_t)(mt * 28 + nt * 2) * 64 + lane) * 16;
      xv[0] = *(const u32x4*)xs;
      xv[1] = *(const u32x4*)(xs + 1024);
      const float* gp = (const float*)(ws + OFF_GAB) + 448 + nt * 32 + hi * 8;
      gv[0] = *(const f32x4*)gp; gv[1] = *(const f32x4*)(gp + 4);
      gv[2] = *(const f32x4*)(gp + 16); gv[3] = *(const f32x4*)(gp + 20);
    }
    __builtin_amdgcn_sched_barrier(0);
  }
  STAMP(1);
  const f32x16 r = ksplit_reduce(acc, lds, w, lane);
  STAMP(2);
#pragma unroll
  for (int s = 0; s < 2; ++s) {
    u32x4 o;
#pragma unroll
    for (int j = 0; j < 4; ++j) {
      const float ga0 = gv[2 * s + (j >> 1)][(2 * j) & 3], ga1 = gv[2 * s + (j >> 1)][(2 * j + 1) & 3];
      o[j] = pack2(bflo(xv[s][j]) + ga0 * r[8 * s + 2 * j], bfhi(xv[s][j]) + ga1 * r[8 * s + 2 * j + 1]);
    }
    st16w<5>(out + (size_t)(mt * 32 + l31) * 448 + nt * 32 + s * 16 + hi * 8, o);
  }
  CODE_PF_END;
  PFLUSH(5);
}

constexpr int LDS_K1 = 94 * 1024;
constexpr int LDS_K2 = 56 * 1024;
constexpr int LDS_K4 = 116 * 1024;

extern "C" void kernel_launch(void* const* d_in, const int* in_sizes, int n_in, void* d_out, int out_size, void* d_ws,
                              size_t ws_size, hipStream_t stream) {
  const uint16_t* x = (const uint16_t*)d_in[0];
  const uint16_t* wqkv = (const uint16_t*)d_in[1];
  const uint16_t* wo = (const uint16_t*)d_in[2];
  const uint16_t* wg = (const uint16_t*)d_in[4];
  const uint16_t* wu = (const uint16_t*)d_in[5];
  const uint16_t* wd = (const uint16_t*)d_in[6];
  const uint16_t* cv = (const uint16_t*)d_in[7];
  const uint16_t* wmod = (const uint16_t*)d_in[8];
  char* ws = (char*)d_ws;
  (void)hipFuncSetAttribute((const void*)k1_qkv, hipFuncAttributeMaxDynamicSharedMemorySize, LDS_K1);
  (void)hipFuncSetAttribute((const void*)k2_attn, hipFuncAttributeMaxDynamicSharedMemorySize, LDS_K2);
  (void)hipFuncSetAttribute((const void*)k4_gu, hipFuncAttributeMaxDynamicSharedMemorySize, LDS_K4);
  k0_prep<<<178, 256, 0, stream>>>(x, wqkv, cv, wmod, ws);
  k1_qkv<<<256, 256, LDS_K1, stream>>>(ws);
  k2_attn<<<494, 512, LDS_K2, stream>>>(wo, wg, wu, ws);
  k3_wo<<<256, 256, 0, stream>>>(x, ws);
  k4_gu<<<256, 256, LDS_K4, stream>>>(wd, cv, wmod, ws);
  k5_down<<<224, 256, 0, stream>>>(ws, (uint16_t*)d_out);
}
```
